```python
import torch

def _softmax(x, axis=-1):
    return torch.softmax(x, dim=axis)
def _gelu(x):
    return torch.nn.functional.gelu(x, approximate="tanh")
def _silu(x):
    return torch.nn.functional.silu(x)
def _sigmoid(x):
    return torch.sigmoid(x)
def _layernorm(x, axis=-1, eps=1e-5):
    m = x.mean(dim=axis, keepdim=True)
    v = x.var(dim=axis, keepdim=True, unbiased=False)
    return (x - m) / torch.sqrt(v + eps)
def _scatter_add(tgt, idx, src, axis=0):
    return tgt.clone().index_add_(axis, idx, src)
def _segment_sum(src, seg, n):
    out = torch.zeros((n,) + src.shape[1:], dtype=src.dtype)
    return out.index_add_(0, seg, src)
def _pad(x, pw):
    flat = tuple(v for pair in reversed(pw) for v in pair)
    return torch.nn.functional.pad(x, flat)

kernel_name = 'recurrent_scan__ssm_conv_s4d__mixer_channel_mlp__prompt'

def setup_inputs():

    _w = (lambda a: (lambda t: t.to(torch.bfloat16) if t.is_floating_point() else t)(torch.as_tensor(a)))
    def _A_setup():
        Bsz, L, D, N = 1, 2048, 640, 128
        return {
            "u": _w((lambda *s: torch.randn(*s, dtype=torch.bfloat16) * 0.05)(Bsz, L, D)),
            "log_A_real": _w((lambda *s: torch.randn(*s, dtype=torch.bfloat16) * 3.2)(D, N) * 0.5),
            "A_imag": _w((lambda *s: torch.randn(*s, dtype=torch.bfloat16) * 0.2)(D, N) * 3.14),
            "C_real": _w((lambda *s: torch.randn(*s, dtype=torch.bfloat16) * 0.8)(D, N) / N ** 0.5),
            "C_imag": _w((lambda *s: torch.randn(*s, dtype=torch.bfloat16) * 0.8)(D, N) / N ** 0.5),
            "log_dt": _w((lambda *s: torch.randn(*s, dtype=torch.bfloat16) * 0.2)(D) * 0.5 - 4.0),
            "D_skip": _w((lambda *s: torch.randn(*s, dtype=torch.bfloat16) * 0.2)(D)),
        }
    _A_in = _A_setup()

    def _B_setup():
        B, S, D, F = 1, 2048, 640, 1728
        return {
            "x": _w((lambda *s: torch.randn(*s, dtype=torch.bfloat16) * 0.05)(B, S, D)),
            "w1": _w((lambda *s: torch.randn(*s, dtype=torch.bfloat16) * 1.6)(D, F) * (D ** -0.5)),
            "b1": _w((lambda *s: torch.randn(*s, dtype=torch.bfloat16) * 0.8)(F)),
            "w2": _w((lambda *s: torch.randn(*s, dtype=torch.bfloat16) * 0.1)(F, D) * (F ** -0.5)),
            "b2": _w((lambda *s: torch.randn(*s, dtype=torch.bfloat16) * 0.1)(D)),
        }
    _B_in = _B_setup()

    return {
        "A_u": _A_in["u"],
        "A_log_A_real": _A_in["log_A_real"],
        "A_A_imag": _A_in["A_imag"],
        "A_C_real": _A_in["C_real"],
        "A_C_imag": _A_in["C_imag"],
        "A_log_dt": _A_in["log_dt"],
        "A_D_skip": _A_in["D_skip"],
        "B_x": _B_in["x"],
        "B_w1": _B_in["w1"],
        "B_b1": _B_in["b1"],
        "B_w2": _B_in["w2"],
        "B_b2": _B_in["b2"],
    }

def reference(A_u, A_log_A_real, A_A_imag, A_C_real, A_C_imag, A_log_dt, A_D_skip, B_x, B_w1, B_b1, B_w2, B_b2):
    def _A_ref(u, log_A_real, A_imag, C_real, C_imag, log_dt, D_skip):
        L = u.shape[1]
        dt = torch.exp(log_dt)
        A = -torch.exp(log_A_real) + 1j * A_imag
        C = (C_real + 1j * C_imag) * dt[:, None]
        pos = (lambda x, dt: x.to(dt))(torch.arange(L), log_dt.dtype)
        P = torch.exp((A * dt[:, None])[:, :, None] * pos[None, None, :])
        K = 2.0 * torch.einsum("dn,dnl->dl", C, P).real
        L2 = 2 * L
        u_t = (lambda x, *d: x.permute(*d))(u, 0, 2, 1)
        Kf = (lambda x, *a, **k: torch.fft.rfft(x.float(), *a, **k))(K, L2)
        Uf = (lambda x, *a, **k: torch.fft.rfft(x.float(), *a, **k))(u_t, L2)
        y = (lambda x, *a, **k: torch.fft.irfft(x, *a, **k).to(torch.bfloat16))(Uf * Kf[None, :, :], L2)[:, :, :L]
        y = (lambda x, *d: x.permute(*d))(y, 0, 2, 1)
        return y + u * D_skip[None, None, :]

    def _B_ref(x, w1, b1, w2, b2):
        y = _layernorm(x, axis=-1)
        h = _gelu(torch.einsum("bsd,df->bsf", y, w1) + b1)
        return torch.einsum("bsf,fd->bsd", h, w2) + b2

    x = A_u
    xn = _layernorm(x, axis=-1)
    s = _A_ref(xn, A_log_A_real, A_A_imag, A_C_real, A_C_imag, A_log_dt, A_D_skip)
    g = _silu(_B_ref(xn, B_w1, B_b1, B_w2, B_b2))
    return x + s * g
```

```cpp
#include <hip/hip_runtime.h>
#include <hip/hip_bf16.h>
#include <stdint.h>

constexpr int L = 2048, D = 640, NM = 128, F = 1728;
typedef unsigned short u16;

__device__ __forceinline__ float bf2f(u16 b) { return __uint_as_float((unsigned)b << 16); }
__device__ __forceinline__ u16 f2bf(float f) { __hip_bfloat16 h = __float2bfloat16(f); return *(u16*)&h; }
__device__ __forceinline__ float rbf(float f) { return bf2f(f2bf(f)); }

__device__ float block_sum(float v, float* red) {
    for (int o = 32; o; o >>= 1) v += __shfl_xor(v, o);
    int w = threadIdx.x >> 6;
    __syncthreads();
    if ((threadIdx.x & 63) == 0) red[w] = v;
    __syncthreads();
    return red[0] + red[1];
}

__global__ void k_ln(const u16* x, u16* xn, u16* y2) {
    __shared__ float red[2];
    int r = blockIdx.x, t = threadIdx.x;
    float v[5];
    for (int i = 0; i < 5; i++) v[i] = bf2f(x[r * D + t + 128 * i]);
    for (int pass = 0; pass < 2; pass++) {
        float s = 0; for (int i = 0; i < 5; i++) s += v[i];
        float m = block_sum(s, red) / D;
        float q = 0; for (int i = 0; i < 5; i++) q += (v[i] - m) * (v[i] - m);
        float var = block_sum(q, red) / D;
        float mb = rbf(m), sd = rbf(sqrtf(rbf(rbf(var) + 1e-5f)));
        for (int i = 0; i < 5; i++) v[i] = rbf(rbf(v[i] - mb) / sd);
        u16* o = pass == 0 ? xn : y2;
        for (int i = 0; i < 5; i++) o[r * D + t + 128 * i] = f2bf(v[i]);
    }
}

__global__ void k_kgen(const u16* lar, const u16* aim, const u16* cr, const u16* ci, const u16* ldt, float* K) {
    int d = blockIdx.x;
    __shared__ float sa[NM], sb[NM], sc[NM], sd_[NM];
    float dt = rbf(expf(bf2f(ldt[d])));
    if (threadIdx.x < NM) {
        int n = threadIdx.x;
        sa[n] = -rbf(expf(bf2f(lar[d * NM + n]))) * dt;
        sb[n] = bf2f(aim[d * NM + n]) * dt;
        sc[n] = bf2f(cr[d * NM + n]) * dt;
        sd_[n] = bf2f(ci[d * NM + n]) * dt;
    }
    __syncthreads();
    for (int l = threadIdx.x; l < L; l += blockDim.x) {
        float p = rbf((float)l);
        float acc = 0;
        for (int n = 0; n < NM; n++) {
            float e = expf(sa[n] * p), s, c;
            sincosf(sb[n] * p, &s, &c);
            acc += e * (sc[n] * c - sd_[n] * s);
        }
        K[d * L + l] = 2.f * acc;
    }
}

__global__ void k_conv(const float* K, const u16* xn, const u16* dsk, u16* s) {
    int d = blockIdx.x;
    __shared__ float sk[L], su[L];
    for (int l = threadIdx.x; l < L; l += blockDim.x) { sk[l] = K[d * L + l]; su[l] = bf2f(xn[l * D + d]); }
    __syncthreads();
    float ds = bf2f(dsk[d]);
    for (int t = threadIdx.x; t < L; t += blockDim.x) {
        float acc = 0;
        for (int j = 0; j <= t; j++) acc += sk[j] * su[t - j];
        s[t * D + d] = f2bf(rbf(acc) + rbf(su[t] * ds));
    }
}

__global__ void k_gemm1(const u16* y2, const u16* w1, const u16* b1, u16* h) {
    int r = blockIdx.y, f = blockIdx.x * 64 + threadIdx.x;
    float acc = 0;
    for (int k = 0; k < D; k++) acc += bf2f(y2[r * D + k]) * bf2f(w1[k * F + f]);
    float z = rbf(rbf(acc) + bf2f(b1[f]));
    float g = 0.5f * z * (1.f + tanhf(0.7978845608028654f * (z + 0.044715f * z * z * z)));
    h[r * F + f] = f2bf(g);
}

__global__ void k_gemm2(const u16* h, const u16* w2, const u16* b2, const u16* x, const u16* s, u16* out) {
    int r = blockIdx.y, c = blockIdx.x * 64 + threadIdx.x;
    float acc = 0;
    for (int k = 0; k < F; k++) acc += bf2f(h[r * F + k]) * bf2f(w2[k * D + c]);
    float z = rbf(rbf(acc) + bf2f(b2[c]));
    float g = rbf(z / (1.f + expf(-z)));
    float sg = rbf(bf2f(s[r * D + c]) * g);
    out[r * D + c] = f2bf(bf2f(x[r * D + c]) + sg);
}

extern "C" void kernel_launch(void* const* d_in, const int* in_sizes, int n_in,
                              void* d_out, int out_size, void* d_ws, size_t ws_size,
                              hipStream_t stream) {
    const u16* x = (const u16*)d_in[0];
    char* ws = (char*)d_ws;
    u16* xn = (u16*)ws;
    u16* y2 = (u16*)(ws + (4 << 20));
    u16* s = (u16*)(ws + (8 << 20));
    float* K = (float*)(ws + (12 << 20));
    u16* h = (u16*)(ws + (20 << 20));
    k_ln<<<L, 128, 0, stream>>>(x, xn, y2);
    k_kgen<<<D, 256, 0, stream>>>((const u16*)d_in[1], (const u16*)d_in[2], (const u16*)d_in[3], (const u16*)d_in[4], (const u16*)d_in[5], K);
    k_conv<<<D, 256, 0, stream>>>(K, xn, (const u16*)d_in[6], s);
    k_gemm1<<<dim3(F / 64, L), 64, 0, stream>>>(y2, (const u16*)d_in[8], (const u16*)d_in[9], h);
    k_gemm2<<<dim3(D / 64, L), 64, 0, stream>>>(h, (const u16*)d_in[10], (const u16*)d_in[11], x, s, (u16*)d_out);
}
```

```cpp
#include <hip/hip_runtime.h>
#include <stdint.h>
#include <utility>

typedef unsigned short u16;
typedef unsigned long long u64;
typedef unsigned v4u __attribute__((ext_vector_type(4)));
typedef unsigned v2u __attribute__((ext_vector_type(2)));
typedef float f2 __attribute__((ext_vector_type(2)));
typedef float f4 __attribute__((ext_vector_type(4)));
typedef float f16v __attribute__((ext_vector_type(16)));
typedef short s4v __attribute__((ext_vector_type(4)));
typedef __bf16 bf2v __attribute__((ext_vector_type(2)));
typedef __amdgpu_buffer_rsrc_t rsrc_t;
#define LAS __attribute__((address_space(3)))
#define CAS __attribute__((address_space(4)))
#define DI __device__ __forceinline__
#define PIN() __builtin_amdgcn_sched_barrier(0)
#define BARRIER() do { PIN(); asm volatile("s_waitcnt lgkmcnt(0)\n\ts_barrier" ::: "memory"); PIN(); } while (0)
#define VMWAIT(n) do { PIN(); asm volatile("s_waitcnt vmcnt(%0)" :: "i"(n) : "memory"); PIN(); } while (0)

#define MAP42 0
#define STAMPS 0

constexpr int SL = 2048, SD = 640, SF = 1728, NKS = SF / 32;
constexpr unsigned OOB = 0x40000000u;
constexpr unsigned OFF_W = 0, OFF_TMO = 128, OFF_F1 = 4096, OFF_F2 = 8192, OFF_F3 = 12288, OFF_DBG = 16384;
constexpr unsigned OFF_STATS = 65536, OFF_XNB = 131072, OFF_W2T = OFF_XNB + SL * SD * 2, OFF_ST = OFF_W2T + SF * SD * 2;
constexpr unsigned OFF_H = OFF_ST + SL * SD * 2, WS_USED = OFF_H + SL * SF * 2;
constexpr unsigned CODE_LINES = 256;
constexpr unsigned SPIN_CAP = 1u << 16;

constexpr unsigned L_AUX = 131072, L_LNT = L_AUX, L_W2S = L_AUX + 10240, L_CS = L_AUX + 22528, L_B1 = L_CS + 512;
constexpr unsigned L_XY = 0, XY_ROW = 528, XY_X = 32 * XY_ROW, XY_CH = 28160;
constexpr unsigned L_KP = 57344, KP_CH = 4608, L_RC = L_KP + 2 * KP_CH, RC_COPY = 4160, RC_CH = 4 * RC_COPY;
constexpr unsigned L_U = L_RC + 2 * RC_CH, U_CH = 4608, L_RED2 = L_U + 2 * U_CH;
constexpr unsigned L_SIMG = L_AUX, SIMG_ROW = 144, L_RED = 0;
constexpr unsigned LDS_BYTES = 163840;
static_assert(L_RED2 + 16384 <= L_AUX, "LDS map");

struct Params {
    const u16 *x, *lar, *aim, *cr, *ci, *ldt, *dsk, *w1, *b1, *w2, *b2;
    u16* out;
    char* ws;
};

template <int... Is, class Fn> DI void sfor_impl(std::integer_sequence<int, Is...>, Fn&& fn) { (fn(std::integral_constant<int, Is>{}), ...); }
template <int N, class Fn> DI void sfor(Fn&& fn) { sfor_impl(std::make_integer_sequence<int, N>{}, fn); }

DI unsigned rfl(unsigned v) { return __builtin_amdgcn_readfirstlane(v); }
DI unsigned opaque(unsigned v) { asm volatile("" : "+v"(v)); return v; }
DI float blo(unsigned d) { return __uint_as_float(d << 16); }
DI float bhi(unsigned d) { return __uint_as_float(d & 0xffff0000u); }
DI unsigned pk(float lo, float hi) { f2 v = {lo, hi}; return __builtin_bit_cast(unsigned, __builtin_convertvector(v, bf2v)); }
DI float rbf(float v) { return blo(pk(v, v)); }
DI float dot2(unsigned a, unsigned b, float c) { return __builtin_amdgcn_fdot2_f32_bf16(__builtin_bit_cast(bf2v, a), __builtin_bit_cast(bf2v, b), c, false); }
template <class T> DI T lds_ld(unsigned a) { return *(const LAS T*)(size_t)a; }
template <class T> DI void lds_st(unsigned a, T v) { *(LAS T*)(size_t)a = v; }
DI v2u lds_tr(unsigned a) { return __builtin_bit_cast(v2u, __builtin_amdgcn_ds_read_tr16_b64_v4i16((LAS s4v*)(size_t)a)); }

template <int CTRL, int RM> DI float dpp_add(float v) {
    return v + __builtin_bit_cast(float, __builtin_amdgcn_update_dpp(0, __builtin_bit_cast(int, v), CTRL, RM, 0xf, false));
}
template <int N> DI void wave_sum(float (&v)[N]) {
    sfor<N>([&](auto i) { v[i] = dpp_add<0xB1, 0xf>(v[i]); });
    sfor<N>([&](auto i) { v[i] = dpp_add<0x4E, 0xf>(v[i]); });
    sfor<N>([&](auto i) { v[i] = dpp_add<0x141, 0xf>(v[i]); });
    sfor<N>([&](auto i) { v[i] = dpp_add<0x140, 0xf>(v[i]); });
    sfor<N>([&](auto i) { v[i] = dpp_add<0x142, 0xa>(v[i]); });
    sfor<N>([&](auto i) { v[i] = dpp_add<0x143, 0xc>(v[i]); });
    sfor<N>([&](auto i) { v[i] = __builtin_bit_cast(float, __builtin_amdgcn_readlane(__builtin_bit_cast(int, v[i]), 63)); });
}

template <int IMM> DI void dma(unsigned vo, rsrc_t rs, unsigned so) {
    asm volatile("buffer_load_dwordx4 %0, %1, %2 offen offset:%3 lds" :: "v"(vo), "s"(rs), "s"(so), "i"(IMM) : "memory");
}
DI void set_m0(unsigned v) { asm volatile("s_mov_b32 m0, %0\n\ts_nop 0" :: "s"(v) : "memory"); }
template <int IMM> DI unsigned ld32(unsigned vo, rsrc_t rs, unsigned so) { unsigned r; asm volatile("buffer_load_dword %0, %1, %2, %3 offen offset:%4" : "=v"(r) : "v"(vo), "s"(rs), "s"(so), "i"(IMM) : "memory"); return r; }
DI unsigned ld16(unsigned vo, rsrc_t rs, unsigned so) { unsigned r; asm volatile("buffer_load_ushort %0, %1, %2, %3 offen" : "=v"(r) : "v"(vo), "s"(rs), "s"(so) : "memory"); return r; }
template <int IMM> DI v2u ld64(unsigned vo, rsrc_t rs, unsigned so) { v2u r; asm volatile("buffer_load_dwordx2 %0, %1, %2, %3 offen offset:%4" : "=v"(r) : "v"(vo), "s"(rs), "s"(so), "i"(IMM) : "memory"); return r; }
template <int IMM> DI v2u ld64c(unsigned vo, rsrc_t rs, unsigned so) { v2u r; asm volatile("buffer_load_dwordx2 %0, %1, %2, %3 offen offset:%4 sc1" : "=v"(r) : "v"(vo), "s"(rs), "s"(so), "i"(IMM) : "memory"); return r; }
template <int IMM> DI v4u ld128c(unsigned vo, rsrc_t rs, unsigned so) { v4u r; asm volatile("buffer_load_dwordx4 %0, %1, %2, %3 offen offset:%4 sc1" : "=v"(r) : "v"(vo), "s"(rs), "s"(so), "i"(IMM) : "memory"); return r; }
template <int A, int IMM> DI void ld128a(unsigned vo, rsrc_t rs, unsigned so) {
    asm volatile("buffer_load_dwordx4 a[%c3:%c4], %0, %1, %2 offen offset:%5 sc1" :: "v"(vo), "s"(rs), "s"(so), "i"(A), "i"(A + 3), "i"(IMM) : "memory");
}
template <int IMM> DI void st128c(v4u v, unsigned vo, rsrc_t rs, unsigned so) { asm volatile("buffer_store_dwordx4 %0, %1, %2, %3 offen offset:%4 sc1\n\ts_nop 1" :: "v"(v), "v"(vo), "s"(rs), "s"(so), "i"(IMM) : "memory"); }
DI void st64c(v2u v, unsigned vo, rsrc_t rs, unsigned so) { asm volatile("buffer_store_dwordx2 %0, %1, %2, %3 offen sc1" :: "v"(v), "v"(vo), "s"(rs), "s"(so) : "memory"); }
template <int IMM> DI void st64(v2u v, unsigned vo, rsrc_t rs, unsigned so) { asm volatile("buffer_store_dwordx2 %0, %1, %2, %3 offen offset:%4" :: "v"(v), "v"(vo), "s"(rs), "s"(so), "i"(IMM) : "memory"); }
template <class T> DI void tie(T& v) { asm volatile("" : "+v"(v)); }

DI void mfma32(f16v& c, v4u a, v4u b) { asm volatile("v_mfma_f32_32x32x16_bf16 %0, %1, %2, %0" : "+v"(c) : "v"(a), "v"(b)); }
DI void mfma16(f4& c, v4u a, v4u b) { asm volatile("v_mfma_f32_16x16x32_bf16 %0, %1, %2, %0" : "+v"(c) : "v"(a), "v"(b)); }
DI void mfma16z(f4& c, v4u a, v4u b) { asm volatile("v_mfma_f32_16x16x32_bf16 %0, %1, %2, 0" : "=&v"(c) : "v"(a), "v"(b)); }
DI void mfma32z(f16v& c, v4u a, v4u b) { asm volatile("v_mfma_f32_32x32x16_bf16 %0, %1, %2, 0" : "=&v"(c) : "v"(a), "v"(b)); }
template <int A> DI void mfma16a(f4& c, v4u b) { asm volatile("v_mfma_f32_16x16x32_bf16 %0, a[%c2:%c3], %1, %0" : "+v"(c) : "v"(b), "i"(A), "i"(A + 3)); }
template <int A> DI void mfma16az(f4& c, v4u b) { asm volatile("v_mfma_f32_16x16x32_bf16 %0, a[%c2:%c3], %1, 0" : "=&v"(c) : "v"(b), "i"(A), "i"(A + 3)); }
#define MFMA_DRAIN() do { PIN(); asm volatile("s_nop 15" ::: "memory"); PIN(); } while (0)

DI f2 cmul(f2 p, f2 w) {
    f2 t, r;
    asm("v_pk_mul_f32 %0, %1, %2 op_sel:[0,0] op_sel_hi:[0,1]" : "=v"(t) : "v"(p), "v"(w));
    asm("v_pk_fma_f32 %0, %1, %2, %3 op_sel:[1,1,0] op_sel_hi:[1,0,1] neg_lo:[0,1,0]" : "=v"(r) : "v"(p), "v"(w), "v"(t));
    return r;
}
DI f2 cexp_at(float er, float rev, float p) {
    const float m = __builtin_amdgcn_exp2f(er * p), fr = __builtin_amdgcn_fractf(rev * p);
    return f2{m * __builtin_amdgcn_cosf(fr), m * __builtin_amdgcn_sinf(fr)};
}

#define ACLOB_16(n) "a" #n "0", "a" #n "1", "a" #n "2", "a" #n "3", "a" #n "4", "a" #n "5", "a" #n "6", "a" #n "7", "a" #n "8", "a" #n "9"
#define ACLOB_ALL "a0", "a1", "a2", "a3", "a4", "a5", "a6", "a7", "a8", "a9", ACLOB_16(1), ACLOB_16(2), ACLOB_16(3), ACLOB_16(4), ACLOB_16(5), ACLOB_16(6), \
    ACLOB_16(7), ACLOB_16(8), ACLOB_16(9), ACLOB_16(10), ACLOB_16(11), ACLOB_16(12), ACLOB_16(13), ACLOB_16(14), ACLOB_16(15), ACLOB_16(16), ACLOB_16(17), \
    ACLOB_16(18), ACLOB_16(19), ACLOB_16(20), ACLOB_16(21), ACLOB_16(22), ACLOB_16(23), ACLOB_16(24), "a250", "a251", "a252", "a253", "a254", "a255"

struct Ctx {
    rsrc_t rws;
    unsigned e_lo, e_hi;
    unsigned lane, w16a, w16b;
    unsigned thr, w2a, w2b, w2l, w2so;
};
#define STAMP(i) do {} while (0)

DI bool flags_match(v2u v, const Ctx& cx) { return __all(v.x == cx.e_lo && v.y == cx.e_hi); }
DI void poll(unsigned vo, const Ctx& cx) {
    for (unsigned spins = 0;; ++spins) {
        v2u v = ld64c<0>(vo, cx.rws, 0);
        VMWAIT(0); tie(v);
        if (flags_match(v, cx)) break;
        if (spins > SPIN_CAP) { st64c(v2u{1u, 1u}, cx.lane == 0 ? OFF_TMO : OOB, cx.rws, 0); break; }
        __builtin_amdgcn_s_sleep(2);
    }
}
template <int S, int STRIDE> DI unsigned slot_off(unsigned base, unsigned wrapped, unsigned last, unsigned thr) {
    if constexpr (S == 13) return last;
    unsigned r;
    asm volatile("s_cmp_le_u32 %3, %4\n\ts_cselect_b32 %0, %2, %1\n\ts_add_u32 %0, %0, %5" : "=&s"(r) : "s"(base), "s"(wrapped), "s"(thr), "i"(S), "i"(S * STRIDE) : "scc");
    return r;
}
template <int S> DI unsigned slot_w2(const Ctx& cx) { return slot_off<S, 5120>(cx.w2a, cx.w2b, cx.w2l, cx.thr); }
template <int LO, int HI> DI void w2_prefetch(Ctx& cx) {
    sfor<HI - LO>([&](auto ii) {
        constexpr int f = LO + ii, s = f / 5, c = f % 5;
        if constexpr (c == 0) cx.w2so = slot_w2<s>(cx);
        if constexpr (c < 4) ld128a<4 * f, c * 1024>(cx.w16a, cx.rws, cx.w2so); else ld128a<4 * f, 0>(cx.w16b, cx.rws, cx.w2so);
    });
}

__global__ void __launch_bounds__(256, 1) s4d_mixer_kernel(Params p) {
    extern __shared__ __attribute__((aligned(16))) char smem[];
    const unsigned tid = threadIdx.x, lane = tid & 63, w = rfl(tid >> 6);
    const unsigned b = blockIdx.x, g = b & 7, q = b >> 3;
    unsigned pf;
    {
        u64 pc;
        asm volatile("s_getpc_b64 %0" : "=s"(pc));
        const unsigned line = (q & 7) * (CODE_LINES / 8) + w * (CODE_LINES / 32) + (lane & (CODE_LINES / 32 - 1));
        asm volatile("global_load_dword %0, %1, %2" : "=v"(pf) : "v"(line * 128u), "s"(pc & ~127ull) : "memory");
    }
    asm volatile("" ::: ACLOB_ALL);
    asm volatile("" :: "v"((unsigned)(size_t)(LAS char*)smem));
    const bool spare = q >= 28;
    const unsigned ti = 2 * g + (q >= 14), tj = q - (q >= 14 ? 14 : 0);
    const unsigned tb = 32 * g + q, rt = 4 * g + (q >> 3), ct = q & 7;
    const unsigned wf = w >> 1, wt = w & 1;
    unsigned ch0 = 80 * g + (spare ? 56 + 6 * (q - 28) : 2 * q);
    const rsrc_t rx = __builtin_amdgcn_make_buffer_rsrc((void*)p.x, 0, SL * SD * 2, 0x00020000);
    const rsrc_t rw1 = __builtin_amdgcn_make_buffer_rsrc((void*)p.w1, 0, SD * SF * 2, 0x00020000);
    const rsrc_t rw2 = __builtin_amdgcn_make_buffer_rsrc((void*)p.w2, 0, SD * SF * 2, 0x00020000);
    Ctx cx;
    cx.rws = __builtin_amdgcn_make_buffer_rsrc((void*)p.ws, 0, WS_USED, 0x00020000);
    cx.lane = lane;
    cx.w16a = lane * 16; cx.w16b = lane * 16 + 4096;
    const unsigned nk = 14 - (w >> 1), rot = (ct * 13) >> 3, k0s = 14 * w - (w == 3 ? 1 : 0) + rot;
    const unsigned klast = k0s + 13 - (rot ? 14 : 0);
    cx.thr = nk - rot; cx.w2a = OFF_W2T + (ct * NKS + k0s) * 5120; cx.w2b = cx.w2a - nk * 5120;
    cx.w2l = w < 2 ? OFF_W2T + (ct * NKS + klast) * 5120 : WS_USED; cx.w2so = 0;
#define STAMP2(i) do {} while (0)
    STAMP2(0);

    unsigned xa[5], xb[5];
    {
        const unsigned so = (8 * tb + 2 * w) * (SD * 2);
        asm volatile("s_nop 4" ::: "memory");
        sfor<5>([&](auto i) { xa[i] = ld32<i * 256>(lane * 4, rx, so); });
        sfor<5>([&](auto i) { xb[i] = ld32<SD * 2 + i * 256>(lane * 4, rx, so); });
    }
    v2u wv = ld64c<0>(opaque(OFF_W), cx.rws, 0);
    const rsrc_t rla = __builtin_amdgcn_make_buffer_rsrc((void*)p.lar, 0, SD * 256, 0x00020000);
    const rsrc_t rai = __builtin_amdgcn_make_buffer_rsrc((void*)p.aim, 0, SD * 256, 0x00020000);
    const rsrc_t rcr = __builtin_amdgcn_make_buffer_rsrc((void*)p.cr, 0, SD * 256, 0x00020000);
    const rsrc_t rci = __builtin_amdgcn_make_buffer_rsrc((void*)p.ci, 0, SD * 256, 0x00020000);
    const unsigned pvo = w * 128 + lane * 2;
    unsigned pla = ld16(pvo, rla, ch0 * 256), pai = ld16(pvo, rai, ch0 * 256), pcr = ld16(pvo, rcr, ch0 * 256), pci = ld16(pvo, rci, ch0 * 256);
    const rsrc_t rb1 = __builtin_amdgcn_make_buffer_rsrc((void*)p.b1, 0, SF * 2, 0x00020000);
    unsigned b1v = ld16((tid & 127) * 2, rb1, spare ? 0u : tj * 256);

    const unsigned sid = 27 * g + (q < 26 ? q : 26), sks = sid >> 2, sqq = sid & 3;
    unsigned gx[4], gw[4];
    if (!spare) {
        set_m0(L_W2S + w * 3072);
        sfor<3>([&](auto ii) {
            const unsigned P = 64 * (3 * w + ii) + lane, row = P / 20, chk = P - row * 20;
            dma<ii * 1024>(P < 640 ? row * 1280 + chk * 16 - ii * 1024 : OOB, rw2, sks * (32 * 1280) + sqq * 320);
        });
        sfor<4>([&](auto ii) {
            gx[ii] = (32 * w + 8 * ii + (lane >> 3)) * 1280 + 16 * ((lane & 7) ^ (4 * (ii & 1) + (lane >> 4))) - ii * 1024;
            gw[ii] = (16 * w + 4 * ii + (lane >> 4)) * (SF * 2) + 16 * ((lane & 15) ^ (((lane >> 4) << 2) | ii)) - ii * 1024;
        });
    }
    const unsigned sx0 = ti * (128 * 1280), sw0 = tj * 256;
    auto stage_x = [&](auto s_) { constexpr int s = s_; set_m0((s & 3) * 32768 + w * 4096); };
    auto stage_w = [&](auto s_) { constexpr int s = s_; set_m0((s & 3) * 32768 + 16384 + w * 4096); };
    auto piece_x = [&](auto s_, auto ii) { constexpr int s = s_; dma<ii * 1024>(gx[ii], rx, sx0 + s * 128); };
    auto piece_w = [&](auto s_, auto ii) { constexpr int s = s_; dma<ii * 1024>(gw[ii], rw1, sw0 + s * (64 * SF * 2)); };
    if (!spare) {
        sfor<4>([&](auto s) {
            stage_x(s); sfor<4>([&](auto ii) { piece_x(s, ii); });
            stage_w(s); sfor<4>([&](auto ii) { piece_w(s, ii); });
        });
        VMWAIT(41);
    } else {
        VMWAIT(6);
    }
    sfor<5>([&](auto i) { tie(xa[i]); tie(xb[i]); });
    tie(pf);
    STAMP2(1);

    float sta, stb;
    {
        constexpr unsigned ONES = 0x3f803f80u;
        float s1[2] = {0.f, 0.f};
        sfor<5>([&](auto i) { s1[0] = dot2(xa[i], ONES, s1[0]); s1[1] = dot2(xb[i], ONES, s1[1]); });
        wave_sum(s1);
        const float ma = s1[0] * (1.f / SD), mb = s1[1] * (1.f / SD), mra = rbf(ma), mrb = rbf(mb);
        f2 da[5], db[5], qa = {0.f, 0.f}, qb = {0.f, 0.f};
        sfor<5>([&](auto i) {
            da[i] = f2{blo(xa[i]), bhi(xa[i])} - mra; db[i] = f2{blo(xb[i]), bhi(xb[i])} - mrb;
            qa = da[i] * da[i] + qa; qb = db[i] * db[i] + qb;
        });
        float s2[2] = {qa.x + qa.y, qb.x + qb.y};
        wave_sum(s2);
        const float va = fmaxf(s2[0] * (1.f / SD) - (ma - mra) * (ma - mra), 0.f), vb = fmaxf(s2[1] * (1.f / SD) - (mb - mrb) * (mb - mrb), 0.f);
        const float ra = __builtin_amdgcn_rcpf(rbf(__builtin_amdgcn_sqrtf(rbf(rbf(va) + 1e-5f)))), rb = __builtin_amdgcn_rcpf(rbf(__builtin_amdgcn_sqrtf(rbf(rbf(vb) + 1e-5f))));
        float s3[4] = {0.f, 0.f, 0.f, 0.f};
        sfor<5>([&](auto i) {
            const unsigned ta = pk(da[i].x, da[i].y), tb2 = pk(db[i].x, db[i].y);
            const f2 na = f2{blo(ta), bhi(ta)} * ra, nb = f2{blo(tb2), bhi(tb2)} * rb;
            const unsigned ua = pk(na.x, na.y), ub = pk(nb.x, nb.y);
            s3[0] = dot2(ua, ONES, s3[0]); s3[1] = dot2(ua, ua, s3[1]); s3[2] = dot2(ub, ONES, s3[2]); s3[3] = dot2(ub, ub, s3[3]);
            lds_st<v2u>(L_LNT + w * 2560 + lane * 8 + i * 512, v2u{__builtin_amdgcn_perm(ub, ua, 0x05040100u), __builtin_amdgcn_perm(ub, ua, 0x07060302u)});
        });
        wave_sum(s3);
        const bool second = lane == 1;
        const float m2 = (second ? s3[2] : s3[0]) * (1.f / SD), v2 = fmaxf((second ? s3[3] : s3[1]) * (1.f / SD) - m2 * m2, 0.f);
        const float r2 = __builtin_amdgcn_rcpf(rbf(__builtin_amdgcn_sqrtf(rbf(rbf(v2) + 1e-5f))));
        const float r1 = second ? rb : ra, m1 = second ? mrb : mra;
        sta = r1 * r2; stb = -(m1 * r1 + rbf(m2)) * r2;
    }
    if (!spare) VMWAIT(32); else VMWAIT(0);
    tie(wv); tie(pla); tie(pai); tie(pcr); tie(pci); tie(b1v);
    {
        const u64 e = ((u64)rfl(wv.y) << 32 | rfl(wv.x)) + 0x9E3779B97F4A7C15ull;
        cx.e_lo = (unsigned)e; cx.e_hi = (unsigned)(e >> 32);
    }
    BARRIER();
    sfor<3>([&](auto ii) {
        const unsigned c = tid + 256 * ii, a = L_LNT + c * 4;
        const v4u v = {lds_ld<unsigned>(a), lds_ld<unsigned>(a + 2560), lds_ld<unsigned>(a + 5120), lds_ld<unsigned>(a + 7680)};
        st128c<0>(v, c < 640 ? c * 16 : OOB, cx.rws, OFF_XNB + tb * 10240);
    });
    st64c(v2u{__float_as_uint(sta), __float_as_uint(stb)}, lane < 2 ? lane * 8 : OOB, cx.rws, OFF_STATS + (8 * tb + 2 * w) * 8);
    const v2u epoch = {cx.e_lo, cx.e_hi};
    const unsigned flag_vo = tid == 0 ? 0u : OOB;
    v4u ub0, ub1;

    if (!spare) {
        {
            const unsigned g4 = lane >> 4, a0 = L_W2S + (16 * (g4 >> 1) + 4 * (g4 & 1) + ((lane & 15) >> 2)) * 320 + 8 * (lane & 3) + w * 96;
            sfor<3>([&](auto ii) {
                const unsigned fi = 3 * w + ii, ctl = fi >= 5 ? 1 : 0;
                const v2u lo = lds_tr(a0 + ii * 32), hi = lds_tr(a0 + ii * 32 + 8 * 320);
                st128c<0>(v4u{lo.x, lo.y, hi.x, hi.y}, fi < 10 ? lane * 16 : OOB, cx.rws, OFF_W2T + ((2 * sqq + ctl) * NKS + sks) * 5120 + (fi - 5 * ctl) * 1024);
            });
        }
        STAMP2(2);
        unsigned ax[2][4], aw[2][4];
        {
            const unsigned row = 64 * wt + (lane & 31), hh = lane >> 5, blk = (lane >> 4) & 1, qr = (lane & 15) >> 2, pp = lane & 3;
            sfor<4>([&](auto ks) { ax[0][ks] = opaque(row * 128 + 16 * ((2 * ks + hh) ^ ((row >> 1) & 7))); ax[1][ks] = opaque(ax[0][ks] + 65536); });
            sfor<4>([&](auto i) {
                constexpr int mf = i >> 1, t = i & 1;
                const unsigned r = 8 * hh + 4 * t + qr, chn = 4 * (2 * wf + (mf ^ wt)) + 2 * blk + (pp >> 1);
                aw[0][i] = opaque(16384 + 256 * r + 16 * (chn ^ ((qr << 2) | ((2 * hh + t) & 3))) + 8 * (pp & 1)); aw[1][i] = opaque(aw[0][i] + 65536);
            });
        }
        const unsigned onesel = ((lane ^ (lane >> 4)) & 1) ? 0u : 0x3f803f80u;
        const v4u ones = {onesel, onesel, onesel, onesel};
        f16v acc[2][2];
        f4 csum;
        v4u fa[2][2], fb[2][2];
        v2u plv, st0, st1;
        auto rd = [&](auto s_, auto ks_) {
            constexpr int s = s_, ks = ks_, bf = ks & 1, hi = (s & 3) >> 1;
            constexpr unsigned so = (s & 1) * 32768;
            fb[bf][0] = lds_ld<v4u>(ax[hi][ks] + so);
            const v2u a0 = lds_tr(aw[hi][0] + so + ks * 4096), a1 = lds_tr(aw[hi][1] + so + ks * 4096);
            fa[bf][0] = v4u{a0.x, a0.y, a1.x, a1.y};
            fb[bf][1] = lds_ld<v4u>(ax[hi][ks] + so + 4096);
            const v2u a2 = lds_tr(aw[hi][2] + so + ks * 4096), a3 = lds_tr(aw[hi][3] + so + ks * 4096);
            fa[bf][1] = v4u{a2.x, a2.y, a3.x, a3.y};
        };
        auto mm = [&](auto first_, auto bf_, auto&& mid) {
            constexpr int bf = bf_; constexpr bool first = first_;
            if constexpr (first) mfma32z(acc[0][0], fa[bf][0], fb[bf][0]); else mfma32(acc[0][0], fa[bf][0], fb[bf][0]);
            mid(std::integral_constant<int, 0>{});
            if constexpr (first) mfma32z(acc[0][1], fa[bf][0], fb[bf][1]); else mfma32(acc[0][1], fa[bf][0], fb[bf][1]);
            if constexpr (first) mfma32z(acc[1][0], fa[bf][1], fb[bf][0]); else mfma32(acc[1][0], fa[bf][1], fb[bf][0]);
            mid(std::integral_constant<int, 1>{});
            if constexpr (first) mfma32z(acc[1][1], fa[bf][1], fb[bf][1]); else mfma32(acc[1][1], fa[bf][1], fb[bf][1]);
            if constexpr (first) mfma16z(csum, fa[bf][0], ones); else mfma16(csum, fa[bf][0], ones);
        };
        sfor<10>([&](auto s_) {
            constexpr int s = s_;
            constexpr int WAITS[10] = {31, 23, 23, 16, 17, 17, 16, 17, 8, 12};
            VMWAIT(WAITS[s]);
            if constexpr (s == 8) { tie(plv); if (!flags_match(plv, cx)) poll(OFF_F1 + tid * 8, cx); }
            BARRIER();
            if constexpr (s == 3) st64c(epoch, flag_vo, cx.rws, OFF_F1 + tb * 8);
            if constexpr (s == 6) plv = ld64c<0>(tid * 8, cx.rws, OFF_F1);
            if constexpr (s == 8) {
                const unsigned vo = (128 * ti + 64 * wt + (lane & 31)) * 8;
                st0 = ld64c<0>(vo, cx.rws, OFF_STATS); st1 = ld64c<256>(vo, cx.rws, OFF_STATS);
                ub0 = ld128c<0>(tid * 10240, cx.rws, OFF_XNB + ch0 * 16); ub1 = ld128c<16>(tid * 10240, cx.rws, OFF_XNB + ch0 * 16);
            }
            sfor<4>([&](auto ks_) {
                constexpr int ks = ks_;
                rd(s_, ks_);
                PIN();
                auto mid = [&](auto m_) {
                    constexpr int pi = 2 * ks + m_;
                    PIN();
                    if constexpr (s >= 1 && s <= 6) {
                        std::integral_constant<int, s + 3> sn;
                        if constexpr (pi == 0) stage_x(sn);
                        if constexpr (pi == 4) stage_w(sn);
                        if constexpr (pi < 4) piece_x(sn, std::integral_constant<int, pi & 3>{}); else piece_w(sn, std::integral_constant<int, pi & 3>{});
                    }
                    if constexpr (s >= 8) w2_prefetch<(s - 8) * 8 + pi, (s - 8) * 8 + pi + 1>(cx);
                    PIN();
                };
                if constexpr (s == 0 && ks == 0) { }
                else mm(std::bool_constant<(s == 0 && ks == 1)>{}, std::integral_constant<int, (ks + 1) & 1>{}, mid);
                PIN();
            });
        });
        mm(std::false_type{}, std::integral_constant<int, 1>{}, [&](auto) {});
        STAMP2(3);
        MFMA_DRAIN();
        tie(csum);
        if ((lane & 15) < 2) lds_st<f4>(L_CS + (64 * wf + 32 * wt + 16 * (lane & 15) + 4 * (lane >> 4)) * 4, csum);
        if (tid < 128) lds_st<float>(L_B1 + tid * 4, blo(b1v));
        VMWAIT(16);
        tie(st0); tie(st1); tie(ub0); tie(ub1);
        BARRIER();
        {
            const unsigned hh = lane >> 5;
            const unsigned hvo = (tj == 13 && wf == 1) ? OOB : ((lane >> 4) & 1) * (NKS * 1024) + (hh * 16 + (lane & 15)) * 16;
            const unsigned hso = OFF_H + ((8 * ti + 4 * wt) * NKS + 4 * tj + 2 * wf) * 1024;
            const unsigned cam[2] = {L_CS + (64 * wf + 32 * wt + 4 * hh) * 4, L_CS + (64 * wf + 32 * (wt ^ 1) + 4 * hh) * 4};
            constexpr float C1 = -2.302208198f, C2 = -0.1029432397f;
            sfor<4>([&](auto it) {
                constexpr int mf = it >> 1, nt = it & 1;
                tie(acc[mf][nt]);
                const v2u sv = nt ? st1 : st0;
                const float sa = __uint_as_float(sv.x), sb = __uint_as_float(sv.y);
                sfor<2>([&](auto chk) {
                    unsigned o[4];
                    sfor<2>([&](auto gg) {
                        constexpr int ga = 2 * chk + gg;
                        const f4 cs = lds_ld<f4>(cam[mf] + 32 * ga), bb = lds_ld<f4>(cam[mf] + 512 + 32 * ga);
                        sfor<2>([&](auto e2) {
                            const f2 a2 = {acc[mf][nt][4 * ga + 2 * e2], acc[mf][nt][4 * ga + 2 * e2 + 1]};
                            const f2 z = a2 * sa + (f2{cs[2 * e2], cs[2 * e2 + 1]} * sb + f2{bb[2 * e2], bb[2 * e2 + 1]});
                            const f2 u = z * (z * z * C2 + C1);
                            const f2 d = f2{__builtin_amdgcn_exp2f(u.x), __builtin_amdgcn_exp2f(u.y)} + 1.f;
                            const f2 r = z * f2{__builtin_amdgcn_rcpf(d.x), __builtin_amdgcn_rcpf(d.y)};
                            o[2 * gg + e2] = pk(r.x, r.y);
                        });
                    });
                    constexpr int f0 = 16 + 13 * (2 * it + chk) / 4, f1 = 16 + 13 * (2 * it + chk + 1) / 4;
                    w2_prefetch<f0, f1>(cx);
                    st128c<chk * 512>(v4u{o[0], o[1], o[2], o[3]}, hvo, cx.rws, hso + (2 * nt * NKS + (mf ^ wt)) * 1024);
                });
            });
        }
        w2_prefetch<42, 52>(cx);
        STAMP2(4);
    } else {
        VMWAIT(0);
        BARRIER();
        st64c(epoch, flag_vo, cx.rws, OFF_F1 + tb * 8);
        poll(OFF_F1 + tid * 8, cx);
        BARRIER();
        ub0 = ld128c<0>(tid * 10240, cx.rws, OFF_XNB + ch0 * 16); ub1 = ld128c<16>(tid * 10240, cx.rws, OFF_XNB + ch0 * 16);
        VMWAIT(0);
        tie(ub0); tie(ub1);
        STAMP2(2); STAMP2(3); STAMP2(4);
    }

    const unsigned cidx = w >> 1, half = w & 1;
    const unsigned nround = spare ? 3 : 1, f2so = rfl(OFF_F2 + (16 * ti + tj) * 8);
    for (unsigned round = 0; round < nround; ++round) {
        const unsigned mode = spare ? round + 1 : 0;
        const unsigned ldt2 = *(const CAS unsigned*)(p.ldt + ch0), dsk2 = *(const CAS unsigned*)(p.dsk + ch0);
        const float dskip = cidx ? bhi(dsk2) : blo(dsk2);
        {
            constexpr float LOG2E = 1.44269504088896f, INV2PI = 0.159154943091895f;
            const float dt = rbf(__builtin_amdgcn_exp2f((cidx ? bhi(ldt2) : blo(ldt2)) * LOG2E));
            const float ar = -rbf(__builtin_amdgcn_exp2f(blo(pla) * LOG2E)) * dt, ai = blo(pai) * dt;
            const float er = ar * LOG2E, rev = ai * INV2PI;
            const f2 c0 = {2.f * (blo(pcr) * dt), -2.f * (blo(pci) * dt)};
            const f2 w8 = cexp_at(er, rev, 8.f), w32 = cexp_at(er, rev, 32.f);
            const f2 v1 = cexp_at(er, -rev, 1.f), v256 = cexp_at(er, -rev, 256.f);
            const unsigned ya = L_XY + cidx * XY_CH + (64 * half + lane) * 4, xa_ = ya + XY_X;
            f2 y[4];
            y[0] = f2{1.f, 0.f}; y[1] = w8; y[2] = cmul(w8, w8); y[3] = cmul(y[2], w8);
            sfor<8>([&](auto jj) {
                sfor<4>([&](auto c) {
                    lds_st<unsigned>(ya + (4 * jj + c) * XY_ROW, pk(y[c].x, y[c].y));
                    if constexpr (jj == 0 && c == 0) y[c] = w32; else if constexpr (jj < 7) y[c] = cmul(y[c], w32);
                });
            });
            f2 xs[8];
            const f2 v2 = cmul(v1, v1);
            xs[0] = c0; xs[1] = cmul(c0, v1);
            sfor<6>([&](auto i) { xs[i + 2] = cmul(xs[i], v2); });
            sfor<8>([&](auto i) { lds_st<unsigned>(xa_ + i * XY_ROW, pk(xs[i].x, xs[i].y)); });
            f2 t[4];
            sfor<4>([&](auto i) { t[i] = cmul(xs[2 * i], v256); lds_st<unsigned>(xa_ + (8 + i) * XY_ROW, pk(t[i].x, t[i].y)); });
            sfor<2>([&](auto k) {
                t[0] = cmul(t[0], v256); t[2] = cmul(t[2], v256);
                lds_st<unsigned>(xa_ + (12 + 2 * k) * XY_ROW, pk(t[0].x, t[0].y)); lds_st<unsigned>(xa_ + (13 + 2 * k) * XY_ROW, pk(t[2].x, t[2].y));
            });
            sfor<5>([&](auto k) { t[0] = cmul(t[0], v256); lds_st<unsigned>(xa_ + (16 + k) * XY_ROW, pk(t[0].x, t[0].y)); });
        }
        lds_st<v4u>(L_U + 480 + tid * 16, ub0);
        lds_st<v4u>(L_U + U_CH + 480 + tid * 16, ub1);
        if (tid < 60) lds_st<v4u>(L_U + (tid >= 30 ? U_CH - 480 : 0) + tid * 16, v4u{0, 0, 0, 0});
        if (tid < 16) lds_st<v4u>(L_RC + (tid >> 1) * RC_COPY + 4096 + (tid & 1) * 16, v4u{0, 0, 0, 0});
        if (mode == 0) VMWAIT(10); else VMWAIT(0);
        BARRIER();
        if (mode == 0) st64c(epoch, flag_vo, cx.rws, f2so);
        if (round + 1 < nround) {
            const unsigned cn = ch0 + 2;
            pla = ld16(pvo, rla, cn * 256); pai = ld16(pvo, rai, cn * 256); pcr = ld16(pvo, rcr, cn * 256); pci = ld16(pvo, rci, cn * 256);
            ub0 = ld128c<0>(tid * 10240, cx.rws, OFF_XNB + cn * 16); ub1 = ld128c<16>(tid * 10240, cx.rws, OFF_XNB + cn * 16);
        }
        STAMP2(5);
        {
            const unsigned r = lane & 15, g4 = lane >> 4;
            const unsigned ya = L_XY + cidx * XY_CH + (16 * half + r) * XY_ROW + g4 * 16, xa_ = L_XY + cidx * XY_CH + XY_X + r * XY_ROW + g4 * 16;
            f4 k0, k1;
            sfor<8>([&](auto ks) {
                const v4u a = lds_ld<v4u>(ya + ks * 64), b0 = lds_ld<v4u>(xa_ + ks * 64), b1 = lds_ld<v4u>(xa_ + 16 * XY_ROW + ks * 64);
                if constexpr (ks == 0) { mfma16z(k0, a, b0); mfma16z(k1, a, b1); } else { mfma16(k0, a, b0); mfma16(k1, a, b1); }
            });
            const unsigned i0 = r < 8 ? r : r < 12 ? 256 + 2 * (r - 8) : 512 + 256 * ((r - 12) >> 1) + 4 * (r & 1);
            const unsigned i1 = r <= 4 ? 1024 + 256 * r : 2049;
            const unsigned kp = L_KP + cidx * KP_CH + 16 * (16 * half + 4 * g4);
            MFMA_DRAIN();
            tie(k0); tie(k1);
            sfor<2>([&](auto e2) {
                const unsigned v0 = pk(k0[2 * e2], k0[2 * e2 + 1]), v1 = pk(k1[2 * e2], k1[2 * e2 + 1]);
                lds_st<u16>(kp + 2 * i0 + 32 * e2, (u16)v0); lds_st<u16>(kp + 2 * i0 + 32 * e2 + 16, (u16)(v0 >> 16));
                lds_st<u16>(kp + 2 * i1 + 32 * e2, (u16)v1); lds_st<u16>(kp + 2 * i1 + 32 * e2 + 16, (u16)(v1 >> 16));
            });
        }
        if (mode == 0) w2_prefetch<52, 56>(cx); else if (mode == 1) w2_prefetch<0, 6>(cx); else if (mode == 2) w2_prefetch<22, 28>(cx); else w2_prefetch<44, 50>(cx);
        BARRIER();
        const unsigned rcb = L_RC + cidx * RC_CH;
        v4u o0, o1;
        {
            const unsigned m = 64 * half + lane, kp = L_KP + cidx * KP_CH + 16 * m;
            const v4u d = lds_ld<v4u>(kp);
            const unsigned d4 = lds_ld<unsigned>(kp + 16), e0 = lds_ld<unsigned>(kp + 2048), e4 = lds_ld<unsigned>(kp + 2048 + 16);
            const bool s1 = m < 32, s3 = m >= 64;
            const unsigned t1 = s3 ? d.x : d.y;
            o0.w = __builtin_amdgcn_perm(d.x, d.x, s1 ? 0x01000302u : 0x01000100u);
            o0.z = __builtin_amdgcn_perm(d.z, t1, s1 ? 0x01000302u : 0x01000504u);
            o0.y = __builtin_amdgcn_perm(d.z, d.z, s1 ? 0x01000302u : 0x01000100u);
            o0.x = __builtin_amdgcn_perm(d4, d.w, s1 ? 0x01000302u : s3 ? 0x05040504u : 0x01000504u);
            o1.w = __builtin_amdgcn_perm(e0, e0, 0x01000100u);
            o1.z = o1.w;
            o1.y = __builtin_amdgcn_perm(e4, e0, (m & 1) ? 0x05040504u : 0x01000504u);
            o1.x = __builtin_amdgcn_perm(e4, e4, 0x01000100u);
            lds_st<v4u>(rcb + 16 * (255 - m), o0);
            lds_st<v4u>(rcb + 16 * (127 - m), o1);
        }
        BARRIER();
        {
            const unsigned m = 64 * half + lane;
            sfor<2>([&](auto it) {
                const v4u d = it ? o1 : o0;
                const unsigned a = rcb + 16 * ((it ? 127 : 255) - m);
                const v2u n = lds_ld<v2u>(a + 16);
                const unsigned q0 = __builtin_amdgcn_alignbyte(d.y, d.x, 2), q1 = __builtin_amdgcn_alignbyte(d.z, d.y, 2), q2 = __builtin_amdgcn_alignbyte(d.w, d.z, 2);
                const unsigned q3 = __builtin_amdgcn_alignbyte(n.x, d.w, 2), q4 = __builtin_amdgcn_alignbyte(n.y, n.x, 2);
                lds_st<v4u>(a + RC_COPY, v4u{q0, q1, q2, q3});
                lds_st<v4u>(a + 2 * RC_COPY, v4u{d.y, d.z, d.w, n.x});
                lds_st<v4u>(a + 3 * RC_COPY, v4u{q1, q2, q3, q4});
            });
        }
        BARRIER();
        STAMP2(6);
        f4 ca[8];
        const unsigned ubase = L_U + cidx * U_CH;
        {
            const unsigned r = lane & 15, g4 = lane >> 4;
            const unsigned aa0 = rcb + ((~r) & 3) * RC_COPY + 2 * (2028 - 4 * (r >> 2) + 8 * g4) - 3584 - 256 * half - 192;
            const unsigned aa = opaque(aa0), ab = opaque(aa0 + 8), bb = opaque(ubase + 448 + 32 * r + 16 * g4 - 256 * half - 192);
            v4u fa[2][8], fb[2][8];
            auto rdA = [&](auto d_, auto e_) { constexpr int d = d_, e = e_; const v2u lo = lds_ld<v2u>(aa + (7 - e) * 512 + (3 - d) * 64), hi = lds_ld<v2u>(ab + (7 - e) * 512 + (3 - d) * 64); fa[d & 1][e] = v4u{lo.x, lo.y, hi.x, hi.y}; };
            auto rdB = [&](auto d_, auto e_) { constexpr int d = d_, e = e_; fb[d & 1][e] = lds_ld<v4u>(bb + e * 512 + (3 - d) * 64); };
            sfor<8>([&](auto e) { rdA(std::integral_constant<int, 0>{}, e); rdB(std::integral_constant<int, 0>{}, e); });
            PIN();
            sfor<4>([&](auto d_) {
                constexpr int d = d_;
                sfor<8>([&](auto e_) {
                    constexpr int e = e_;
                    if constexpr (d < 3) { rdA(std::integral_constant<int, d + 1>{}, e_); rdB(std::integral_constant<int, d + 1>{}, e_); }
                    PIN();
                    sfor<8 - e>([&](auto k) { constexpr int n = e + k; if constexpr (d == 0 && e == 0) mfma16z(ca[n], fa[d & 1][e], fb[d & 1][k]); else mfma16(ca[n], fa[d & 1][e], fb[d & 1][k]); });
                    PIN();
                });
                if (mode == 0) w2_prefetch<56 + 2 * d, 58 + 2 * d>(cx);
                else if (mode == 1) w2_prefetch<6 + 4 * d, 10 + 4 * d>(cx);
                else if (mode == 2) w2_prefetch<28 + 4 * d, 32 + 4 * d>(cx);
                else w2_prefetch<50 + 14 * d / 4, 50 + 14 * (d + 1) / 4>(cx);
            });
        }
        MFMA_DRAIN();
        sfor<8>([&](auto n) { tie(ca[n]); });
        STAMP2(7);
        {
            const unsigned ra = L_RED2 + lane * 16;
            if (half == 0) { sfor<4>([&](auto i) { lds_st<f4>(ra + w * 4096 + i * 1024, ca[4 + i]); }); }
            else { sfor<4>([&](auto i) { lds_st<f4>(ra + w * 4096 + i * 1024, ca[i]); ca[i] = ca[4 + i]; }); }
            BARRIER();
            const unsigned c = lane & 15, g4 = lane >> 4;
            v2u pkd[4];
            sfor<4>([&](auto i) {
                const f4 y = ca[i] + lds_ld<f4>(ra + (w ^ 1) * 4096 + i * 1024);
                const v2u u = lds_ld<v2u>(ubase + 480 + 2 * (1024 * half + 256 * i + 16 * c + 4 * g4));
                pkd[i] = v2u{pk(y[0] + blo(u.x) * dskip, y[1] + bhi(u.x) * dskip), pk(y[2] + blo(u.y) * dskip, y[3] + bhi(u.y) * dskip)};
            });
            const unsigned vo = 2 * (256 * (g4 & 1) + 16 * c + 8 * (g4 >> 1)), so = OFF_ST + (ch0 + cidx) * 4096 + 2048 * half;
            sfor<2>([&](auto i2) {
                const auto sx = __builtin_amdgcn_permlane16_swap(pkd[2 * i2].x, pkd[2 * i2 + 1].x, false, false);
                const auto sy = __builtin_amdgcn_permlane16_swap(pkd[2 * i2].y, pkd[2 * i2 + 1].y, false, false);
                st128c<i2 * 1024>(v4u{sx[0], sy[0], sx[1], sy[1]}, vo, cx.rws, so);
            });
        }
        ch0 += 2;
        if (round + 1 < nround) { VMWAIT(0); tie(pla); tie(pai); tie(pcr); tie(pci); tie(ub0); tie(ub1); BARRIER(); }
    }
    STAMP2(8);

    v4u wx[6];
    wx[0] = ld128c<0>(cx.w16b, cx.rws, cx.w2so);
    {
        const unsigned so = slot_w2<13>(cx);
        sfor<4>([&](auto c) { wx[1 + c] = ld128c<c * 1024>(cx.w16a, cx.rws, so); });
        wx[5] = ld128c<0>(cx.w16b, cx.rws, so);
    }
    VMWAIT(6);
    BARRIER();
    st64c(epoch, flag_vo, cx.rws, OFF_F3 + tb * 8);
    poll(OFF_F2 + (16 * (rt >> 1) + (lane < 14 ? lane : 0)) * 8, cx);
    sfor<6>([&](auto i) { tie(wx[i]); });
    STAMP2(9);
    f4 acc2[5][4];
    v2u xo[5], bo[5], plv3;
    v4u sv[3];
    const unsigned xvo = (16 * w + (lane & 15)) * (SD * 2) + 8 * (lane >> 4), xso = 64 * rt * (SD * 2) + 160 * ct;
    {
        unsigned hv[4];
        sfor<4>([&](auto t) { hv[t] = (4 * rt + ((t + w) & 3)) * (NKS * 1024) + lane * 16; });
        constexpr int WIN = 4;
        const unsigned hsa = OFF_H + k0s * 1024, hsb = hsa - nk * 1024, hsl = w < 2 ? OFF_H + klast * 1024 : WS_USED;
        v4u hf[WIN][4];
        auto ldh = [&](auto s_) { constexpr int s = s_; const unsigned so = slot_off<s, 1024>(hsa, hsb, hsl, cx.thr); sfor<4>([&](auto t) { hf[s % WIN][t] = ld128c<0>(hv[t], cx.rws, so); }); };
        sfor<WIN - 1>([&](auto s) { ldh(s); });
        const rsrc_t rb2 = __builtin_amdgcn_make_buffer_rsrc((void*)p.b2, 0, SD * 2, 0x00020000);
        sfor<14>([&](auto s_) {
            constexpr int s = s_;
            if constexpr (s + WIN - 1 < 14) ldh(std::integral_constant<int, s + WIN - 1>{});
            if constexpr (s == 5) plv3 = ld64c<0>((lane & 31) * 8, cx.rws, OFF_F3 + ct * 256);
            constexpr int later = (s + WIN - 1 < 14 ? WIN - 1 : 13 - s) * 4;
            constexpr int extra = (s >= 5 && s <= 7) ? 1 : (s >= 9 && s <= 11) ? 13 : 0;
            VMWAIT(later + extra);
            sfor<4>([&](auto t) { tie(hf[s % WIN][t]); });
            if constexpr (s == 8) {
                tie(plv3);
                if (!flags_match(plv3, cx)) poll(OFF_F3 + ct * 256 + (lane & 31) * 8, cx);
                sfor<3>([&](auto ii) {
                    const unsigned P = tid + 256 * ii;
                    sv[ii] = ld128c<0>(P < 640 ? (P >> 3) * 4096 + (P & 7) * 16 : OOB, cx.rws, OFF_ST + 80 * ct * 4096 + 128 * rt);
                });
                sfor<5>([&](auto c) { xo[c] = ld64<c * 32>(xvo, rx, xso); });
                sfor<5>([&](auto c) { bo[c] = ld64<c * 32>(8 * (lane >> 4), rb2, 160 * ct); });
            }
            PIN();
            sfor<5>([&](auto c_) {
                constexpr int c = c_, f = 5 * s + c;
                sfor<4>([&](auto t) {
                    if constexpr (f >= 64) mfma16(acc2[c][t], wx[f - 64], hf[s % WIN][t]);
                    else if constexpr (s == 0) mfma16az<4 * f>(acc2[c][t], hf[s % WIN][t]);
                    else mfma16a<4 * f>(acc2[c][t], hf[s % WIN][t]);
                });
            });
            PIN();
        });
    }
    STAMP2(10);
    sfor<3>([&](auto ii) { tie(sv[ii]); });
    sfor<5>([&](auto c) { tie(xo[c]); tie(bo[c]); });
    sfor<3>([&](auto ii) { const unsigned P = tid + 256 * ii; if (P < 640) lds_st<v4u>(L_SIMG + (P >> 3) * SIMG_ROW + (P & 7) * 16, sv[ii]); });
    MFMA_DRAIN();
    sfor<5>([&](auto c) { sfor<4>([&](auto t) { tie(acc2[c][t]); }); });
    sfor<3>([&](auto t1) {
        const unsigned a = L_RED + ((w + t1 + 1) & 3) * 15360 + t1 * 5120 + lane * 16;
        sfor<5>([&](auto c) { lds_st<f4>(a + c * 1024, acc2[c][t1 + 1]); });
    });
    BARRIER();
    {
        const rsrc_t ro = __builtin_amdgcn_make_buffer_rsrc((void*)p.out, 0, SL * SD * 2, 0x00020000);
        const unsigned a = L_RED + w * 15360 + lane * 16;
        const unsigned sa = L_SIMG + (4 * (lane >> 4) + ((lane & 15) >> 2)) * SIMG_ROW + 32 * w + 8 * (lane & 3);
        sfor<5>([&](auto c) {
            const f4 z4 = (acc2[c][0] + lds_ld<f4>(a + c * 1024)) + (lds_ld<f4>(a + 5120 + c * 1024) + lds_ld<f4>(a + 10240 + c * 1024));
            const v2u s2 = lds_tr(sa + c * 16 * SIMG_ROW);
            const float bz[4] = {blo(bo[c].x), bhi(bo[c].x), blo(bo[c].y), bhi(bo[c].y)};
            const float sz[4] = {blo(s2.x), bhi(s2.x), blo(s2.y), bhi(s2.y)};
            const float xz[4] = {blo(xo[c].x), bhi(xo[c].x), blo(xo[c].y), bhi(xo[c].y)};
            float o[4];
            sfor<4>([&](auto e) {
                const float z = z4[e + 0] + bz[e];
                o[e] = xz[e] + sz[e] * (z * __builtin_amdgcn_rcpf(1.f + __builtin_amdgcn_exp2f(z * -1.44269504088896f)));
            });
            st64<c * 32>(v2u{pk(o[0], o[1]), pk(o[2], o[3])}, xvo, ro, xso);
        });
    }
    if (b == 0) st64c(epoch, flag_vo, cx.rws, OFF_W);
}

extern "C" void kernel_launch(void* const* d_in, const int* in_sizes, int n_in, void* d_out, int out_size, void* d_ws, size_t ws_size, hipStream_t stream) {
    Params p;
    p.x = (const u16*)d_in[0]; p.lar = (const u16*)d_in[1]; p.aim = (const u16*)d_in[2]; p.cr = (const u16*)d_in[3]; p.ci = (const u16*)d_in[4];
    p.ldt = (const u16*)d_in[5]; p.dsk = (const u16*)d_in[6]; p.w1 = (const u16*)d_in[8]; p.b1 = (const u16*)d_in[9];
    p.w2 = (const u16*)d_in[10]; p.b2 = (const u16*)d_in[11]; p.out = (u16*)d_out; p.ws = (char*)d_ws;
    (void)hipFuncSetAttribute((const void*)s4d_mixer_kernel, hipFuncAttributeMaxDynamicSharedMemorySize, LDS_BYTES);
    s4d_mixer_kernel<<<256, 256, LDS_BYTES, stream>>>(p);
}
```

```cpp
#include <hip/hip_runtime.h>
#include <stdint.h>
#include <utility>

typedef unsigned short u16;
typedef unsigned long long u64;
typedef unsigned v4u __attribute__((ext_vector_type(4)));
typedef unsigned v2u __attribute__((ext_vector_type(2)));
typedef float f2 __attribute__((ext_vector_type(2)));
typedef float f4 __attribute__((ext_vector_type(4)));
typedef float f16v __attribute__((ext_vector_type(16)));
typedef short s4v __attribute__((ext_vector_type(4)));
typedef __bf16 bf2v __attribute__((ext_vector_type(2)));
typedef __amdgpu_buffer_rsrc_t rsrc_t;
#define LAS __attribute__((address_space(3)))
#define CAS __attribute__((address_space(4)))
#define DI __device__ __forceinline__
#define PIN() __builtin_amdgcn_sched_barrier(0)
#define BARRIER() do { PIN(); asm volatile("s_waitcnt lgkmcnt(0)\n\ts_barrier" ::: "memory"); PIN(); } while (0)
#define VMWAIT(n) do { PIN(); asm volatile("s_waitcnt vmcnt(%0)" :: "i"(n) : "memory"); PIN(); } while (0)

#define FSTR 32
#define STAMPS 0

constexpr int SL = 2048, SD = 640, SF = 1728, NKS = SF / 32;
constexpr unsigned OOB = 0x40000000u;
constexpr unsigned OFF_W = 0, OFF_TMO = 128, OFF_F1 = 4096, OFF_F2 = OFF_F1 + 256 * FSTR, OFF_F3 = OFF_F2 + 256 * FSTR, OFF_DBG = 0x1000000;
constexpr unsigned OFF_STATS = 114688, OFF_XNB = 131072, OFF_W2T = OFF_XNB + SL * SD * 2, OFF_ST = OFF_W2T + SF * SD * 2;
constexpr unsigned OFF_H = OFF_ST + SL * SD * 2, WS_USED = OFF_H + SL * SF * 2;
constexpr unsigned CODE_LINES = 256;
static_assert(OFF_F3 + 256 * FSTR <= OFF_STATS, "workspace map");
constexpr unsigned SPIN_CAP = 1u << 16;

constexpr unsigned L_AUX = 131072, L_LNT = L_AUX, L_W2S = L_AUX + 10240, L_CS = L_AUX + 22528, L_B1 = L_CS + 512;
constexpr unsigned L_XY = 65536, XY_ROW = 528, XY_X = 32 * XY_ROW, XY_CH = 28160;
constexpr unsigned L_KP = 0, KP_CH = 4608, L_RC = L_KP + 2 * KP_CH, RC_COPY = 4160, RC_CH = 4 * RC_COPY;
constexpr unsigned L_U = L_RC + 2 * RC_CH, U_CH = 4608, L_RED2 = L_XY, L_R0 = L_XY + 2 * XY_CH;
constexpr unsigned L_U6 = L_XY + 16384, XY_BIAS = 38000;
constexpr unsigned L_SIMG = L_AUX, SIMG_ROW = 144, L_RED = 0;
constexpr unsigned LDS_BYTES = 163840;
static_assert(L_U + 2 * U_CH <= L_XY && L_R0 + 6 * RC_COPY <= LDS_BYTES && L_R0 <= L_AUX, "LDS map");

struct Params {
    const u16 *x, *lar, *aim, *cr, *ci, *ldt, *dsk, *w1, *b1, *w2, *b2;
    u16* out;
    char* ws;
};

template <int... Is, class Fn> DI void sfor_impl(std::integer_sequence<int, Is...>, Fn&& fn) { (fn(std::integral_constant<int, Is>{}), ...); }
template <int N, class Fn> DI void sfor(Fn&& fn) { sfor_impl(std::make_integer_sequence<int, N>{}, fn); }

DI unsigned rfl(unsigned v) { return __builtin_amdgcn_readfirstlane(v); }
DI unsigned sel(bool c, unsigned a, unsigned b) { return c ? a : b; }
DI unsigned opaque(unsigned v) { asm volatile("" : "+v"(v)); return v; }
DI float blo(unsigned d) { return __uint_as_float(d << 16); }
DI float bhi(unsigned d) { return __uint_as_float(d & 0xffff0000u); }
DI unsigned pk(float lo, float hi) { f2 v = {lo, hi}; return __builtin_bit_cast(unsigned, __builtin_convertvector(v, bf2v)); }
DI float rbf(float v) { return blo(pk(v, v)); }
DI float dot2(unsigned a, unsigned b, float c) { return __builtin_amdgcn_fdot2_f32_bf16(__builtin_bit_cast(bf2v, a), __builtin_bit_cast(bf2v, b), c, false); }
template <class T> DI T lds_ld(unsigned a) { return *(const LAS T*)(size_t)a; }
template <class T> DI void lds_st(unsigned a, T v) { *(LAS T*)(size_t)a = v; }
DI v2u lds_tr(unsigned a) { return __builtin_bit_cast(v2u, __builtin_amdgcn_ds_read_tr16_b64_v4i16((LAS s4v*)(size_t)a)); }

template <int CTRL> DI float dpp_add(float v) {
    return v + __builtin_bit_cast(float, __builtin_amdgcn_update_dpp(0, __builtin_bit_cast(int, v), CTRL, 0xf, 0xf, true));
}
DI float pair_sum(float a, float b) {
    const auto r = __builtin_amdgcn_permlane32_swap(__float_as_uint(a), __float_as_uint(b), false, false);
    float v = __uint_as_float(r[0]) + __uint_as_float(r[1]);
    v = dpp_add<0xB1>(v); v = dpp_add<0x4E>(v); v = dpp_add<0x141>(v); v = dpp_add<0x140>(v);
    return dpp_add<0x142>(v);
}
template <int LANE> DI float rdl(float v) { return __builtin_bit_cast(float, __builtin_amdgcn_readlane(__builtin_bit_cast(int, v), LANE)); }
DI float inv_sd(float var) { return __builtin_amdgcn_rcpf(rbf(__builtin_amdgcn_sqrtf(rbf(rbf(fmaxf(var, 0.f)) + 1e-5f)))); }

template <int IMM> DI void dma(unsigned vo, rsrc_t rs, unsigned so) {
    asm volatile("buffer_load_dwordx4 %0, %1, %2 offen offset:%3 lds" :: "v"(vo), "s"(rs), "s"(so), "i"(IMM));
}
DI void set_m0(unsigned v) { asm volatile("s_mov_b32 m0, %0\n\ts_nop 0" :: "s"(v)); }
template <int IMM> DI unsigned ld32(unsigned vo, rsrc_t rs, unsigned so) { unsigned r; asm volatile("buffer_load_dword %0, %1, %2, %3 offen offset:%4" : "=v"(r) : "v"(vo), "s"(rs), "s"(so), "i"(IMM)); return r; }
DI unsigned ld16(unsigned vo, rsrc_t rs, unsigned so) { unsigned r; asm volatile("buffer_load_ushort %0, %1, %2, %3 offen" : "=v"(r) : "v"(vo), "s"(rs), "s"(so)); return r; }
template <int IMM> DI v2u ld64(unsigned vo, rsrc_t rs, unsigned so) { v2u r; asm volatile("buffer_load_dwordx2 %0, %1, %2, %3 offen offset:%4" : "=v"(r) : "v"(vo), "s"(rs), "s"(so), "i"(IMM)); return r; }
template <int IMM> DI v2u ld64c(unsigned vo, rsrc_t rs, unsigned so) { v2u r; asm volatile("buffer_load_dwordx2 %0, %1, %2, %3 offen offset:%4 sc1" : "=v"(r) : "v"(vo), "s"(rs), "s"(so), "i"(IMM)); return r; }
template <int IMM> DI v4u ld128c(unsigned vo, rsrc_t rs, unsigned so) { v4u r; asm volatile("buffer_load_dwordx4 %0, %1, %2, %3 offen offset:%4 sc1" : "=v"(r) : "v"(vo), "s"(rs), "s"(so), "i"(IMM)); return r; }
template <int A, int IMM> DI void ld128a(unsigned vo, rsrc_t rs, unsigned so) {
    asm volatile("buffer_load_dwordx4 a[%c3:%c4], %0, %1, %2 offen offset:%5 sc1" :: "v"(vo), "s"(rs), "s"(so), "i"(A), "i"(A + 3), "i"(IMM));
}
template <int IMM> DI void st128c(v4u v, unsigned vo, rsrc_t rs, unsigned so) { asm volatile("buffer_store_dwordx4 %0, %1, %2, %3 offen offset:%4 sc1\n\ts_nop 1" :: "v"(v), "v"(vo), "s"(rs), "s"(so), "i"(IMM)); }
DI void st64c(v2u v, unsigned vo, rsrc_t rs, unsigned so) { asm volatile("buffer_store_dwordx2 %0, %1, %2, %3 offen sc1" :: "v"(v), "v"(vo), "s"(rs), "s"(so)); }
template <int IMM> DI void st64(v2u v, unsigned vo, rsrc_t rs, unsigned so) { asm volatile("buffer_store_dwordx2 %0, %1, %2, %3 offen offset:%4" :: "v"(v), "v"(vo), "s"(rs), "s"(so), "i"(IMM)); }
template <int IMM> DI void lds_st_tid(unsigned v) { asm volatile("ds_write_addtid_b32 %0 offset:%1" :: "v"(v), "i"(IMM)); }
DI void l2_touch(unsigned vo, rsrc_t rs, unsigned so) { asm volatile("buffer_load_dword a255, %0, %1, %2 offen" :: "v"(vo), "s"(rs), "s"(so)); }
template <class T> DI void tie(T& v) { asm volatile("" : "+v"(v)); }

DI void mfma32(f16v& c, v4u a, v4u b) { asm volatile("v_mfma_f32_32x32x16_bf16 %0, %1, %2, %0" : "+v"(c) : "v"(a), "v"(b)); }
DI void mfma16(f4& c, v4u a, v4u b) { asm volatile("v_mfma_f32_16x16x32_bf16 %0, %1, %2, %0" : "+v"(c) : "v"(a), "v"(b)); }
DI void mfma16z(f4& c, v4u a, v4u b) { asm volatile("v_mfma_f32_16x16x32_bf16 %0, %1, %2, 0" : "=&v"(c) : "v"(a), "v"(b)); }
DI void mfma32z(f16v& c, v4u a, v4u b) { asm volatile("v_mfma_f32_32x32x16_bf16 %0, %1, %2, 0" : "=&v"(c) : "v"(a), "v"(b)); }
template <int A> DI void mfma16a(f4& c, v4u b) { asm volatile("v_mfma_f32_16x16x32_bf16 %0, a[%c2:%c3], %1, %0" : "+v"(c) : "v"(b), "i"(A), "i"(A + 3)); }
template <int A> DI void mfma16az(f4& c, v4u b) { asm volatile("v_mfma_f32_16x16x32_bf16 %0, a[%c2:%c3], %1, 0" : "=&v"(c) : "v"(b), "i"(A), "i"(A + 3)); }
#define MFMA_DRAIN() do { PIN(); asm volatile("s_nop 15" ::: "memory"); PIN(); } while (0)

DI f2 cmul(f2 p, f2 w) {
    f2 r;
    asm volatile("v_pk_mul_f32 %0, %1, %2 op_sel:[0,0] op_sel_hi:[0,1]\n\tv_pk_fma_f32 %0, %1, %2, %0 op_sel:[1,1,0] op_sel_hi:[1,0,1] neg_lo:[0,1,0]" : "=&v"(r) : "v"(p), "v"(w));
    return r;
}
DI f2 cmul_s(f2 p, f2 w) {
    float a, b;
    asm volatile("v_mul_f32 %0, %2, %4\n\tv_mul_f32 %1, %2, %5\n\tv_fma_f32 %0, -%3, %5, %0\n\tv_fma_f32 %1, %3, %4, %1" : "=&v"(a), "=&v"(b) : "v"(p.x), "v"(p.y), "v"(w.x), "v"(w.y));
    return f2{a, b};
}
DI float fmul_s(float a, float b) { float r; asm("v_mul_f32 %0, %1, %2" : "=v"(r) : "v"(a), "v"(b)); return r; }
DI f2 cexp_at(float er, float rev, float p) {
    const float m = __builtin_amdgcn_exp2f(er * p), fr = __builtin_amdgcn_fractf(rev * p);
    const float c = __builtin_amdgcn_cosf(fr) * m;
    float s = __builtin_amdgcn_sinf(fr);
    asm volatile("" : "+v"(s));
    return f2{c, s * m};
}

#define ACLOB_16(n) "a" #n "0", "a" #n "1", "a" #n "2", "a" #n "3", "a" #n "4", "a" #n "5", "a" #n "6", "a" #n "7", "a" #n "8", "a" #n "9"
#define ACLOB_ALL "a0", "a1", "a2", "a3", "a4", "a5", "a6", "a7", "a8", "a9", ACLOB_16(1), ACLOB_16(2), ACLOB_16(3), ACLOB_16(4), ACLOB_16(5), ACLOB_16(6), \
    ACLOB_16(7), ACLOB_16(8), ACLOB_16(9), ACLOB_16(10), ACLOB_16(11), ACLOB_16(12), ACLOB_16(13), ACLOB_16(14), ACLOB_16(15), ACLOB_16(16), ACLOB_16(17), \
    ACLOB_16(18), ACLOB_16(19), ACLOB_16(20), ACLOB_16(21), ACLOB_16(22), ACLOB_16(23), ACLOB_16(24), "a250", "a251", "a252", "a253", "a254", "a255"

struct Ctx {
    rsrc_t rws;
    unsigned e_lo, e_hi;
    unsigned tid, lane, w, w16a, w16b, flag_vo;
    unsigned thr, w2a, w2b, w2l, w2so;
};
#define STAMP(i) do {} while (0)
#define CYC(i) do {} while (0)
template <int N> using ic = std::integral_constant<int, N>;

DI bool flags_match(v2u v, const Ctx& cx) { return __all(v.x == cx.e_lo && v.y == cx.e_hi); }
DI void publish(const Ctx& cx, unsigned so) { st64c(v2u{cx.e_lo, cx.e_hi}, cx.flag_vo, cx.rws, so); }
template <int SLEEP> DI void poll(unsigned vo, const Ctx& cx) {
#pragma unroll 1
    for (unsigned spins = 0;; ++spins) {
        v2u v = ld64c<0>(vo, cx.rws, 0);
        VMWAIT(0); tie(v);
        if (flags_match(v, cx)) break;
        if (spins > SPIN_CAP) { st64c(v2u{1u, 1u}, cx.lane == 0 ? OFF_TMO : OOB, cx.rws, 0); break; }
        __builtin_amdgcn_s_sleep(SLEEP);
    }
}
template <int S, int STRIDE> DI unsigned slot_off(unsigned base, unsigned wrapped, unsigned last, unsigned thr) {
    if constexpr (S == 13) return last;
    unsigned r;
    asm volatile("s_cmp_le_u32 %3, %4\n\ts_cselect_b32 %0, %2, %1\n\ts_add_u32 %0, %0, %5" : "=&s"(r) : "s"(base), "s"(wrapped), "s"(thr), "i"(S), "i"(S * STRIDE) : "scc");
    return r;
}
template <int LO, int HI> DI void w2_prefetch(Ctx& cx) {
    sfor<HI - LO>([&](auto ii) {
        constexpr int f = LO + ii, s = f / 5, c = f % 5;
        if constexpr (c == 0) cx.w2so = slot_off<s, 5120>(cx.w2a, cx.w2b, cx.w2l, cx.thr);
        if constexpr (c < 4) ld128a<4 * f, c * 1024>(cx.w16a, cx.rws, cx.w2so); else ld128a<4 * f, 0>(cx.w16b, cx.rws, cx.w2so);
    });
}

template <class H1, class H2> DI f2 layernorm_rows(const unsigned (&xa)[5], const unsigned (&xb)[5], const Ctx& cx, H1&& hook1, H2&& hook2) {
    constexpr unsigned ONES = 0x3f803f80u;
    float sa = 0.f, sb = 0.f;
    sfor<5>([&](auto i) { sa = dot2(xa[i], ONES, sa); sb = dot2(xb[i], ONES, sb); });
    const float mean = pair_sum(sa, sb) * (1.f / SD), mr = rbf(mean), dm = mean - mr;
    const float mra = rdl<31>(mr), mrb = rdl<63>(mr);
    PIN(); hook1(); PIN();
    unsigned ta[5], tb[5];
    f2 qa = {0.f, 0.f}, qb = {0.f, 0.f};
    sfor<5>([&](auto i) {
        const f2 da = f2{blo(xa[i]), bhi(xa[i])} - mra, db = f2{blo(xb[i]), bhi(xb[i])} - mrb;
        qa = da * da + qa; qb = db * db + qb;
        ta[i] = pk(da.x, da.y); tb[i] = pk(db.x, db.y);
    });
    const float r1 = inv_sd(pair_sum(qa.x + qa.y, qb.x + qb.y) * (1.f / SD) - dm * dm);
    const float ra = rdl<31>(r1), rb = rdl<63>(r1);
    PIN(); hook2(); PIN();
    float s0 = 0.f, s1 = 0.f, s2 = 0.f, s3 = 0.f;
    unsigned o[10];
    sfor<5>([&](auto i) {
        const f2 na = f2{blo(ta[i]), bhi(ta[i])} * ra, nb = f2{blo(tb[i]), bhi(tb[i])} * rb;
        const unsigned ua = pk(na.x, na.y), ub = pk(nb.x, nb.y);
        s0 = dot2(ua, ONES, s0); s1 = dot2(ua, ua, s1); s2 = dot2(ub, ONES, s2); s3 = dot2(ub, ub, s3);
        o[2 * i] = __builtin_amdgcn_perm(ub, ua, 0x05040100u); o[2 * i + 1] = __builtin_amdgcn_perm(ub, ua, 0x07060302u);
    });
    const unsigned la = opaque(L_LNT + cx.w * 2560 + cx.lane * 16), lb = opaque(L_LNT + cx.w * 2560 + 2048 + cx.lane * 8);
    lds_st<v4u>(la, v4u{o[0], o[1], o[2], o[3]});
    lds_st<v4u>(la + 1024, v4u{o[4], o[5], o[6], o[7]});
    lds_st<v2u>(lb, v2u{o[8], o[9]});
    const float m2 = pair_sum(s0, s2) * (1.f / SD), r2 = inv_sd(pair_sum(s1, s3) * (1.f / SD) - m2 * m2);
    return f2{r1 * r2, -(mr * r1 + rbf(m2)) * r2};
}
DI void store_xn(const Ctx& cx, f2 st, unsigned tb) {
    const unsigned a = opaque(L_LNT + cx.tid * 4);
    v4u v[3];
    sfor<3>([&](auto ii) { v[ii] = v4u{lds_ld<unsigned>(a + ii * 1024), lds_ld<unsigned>(a + ii * 1024 + 2560), lds_ld<unsigned>(a + ii * 1024 + 5120), lds_ld<unsigned>(a + ii * 1024 + 7680)}; });
    const unsigned so = OFF_XNB + tb * 10240, vo = cx.tid * 16;
    st128c<0>(v[0], vo, cx.rws, so);
    st128c<0>(v[1], vo, cx.rws, so + 4096);
    st128c<0>(v[2], cx.w < 2 ? vo : OOB, cx.rws, so + 8192);
    st64c(v2u{__float_as_uint(st.x), __float_as_uint(st.y)}, sel((cx.lane & 31) == 31, (cx.lane >> 5) * 8, OOB), cx.rws, OFF_STATS + (8 * tb + 2 * cx.w) * 8);
}

struct Kgen {
    unsigned la, ai, cr, ci, ldt;
    float dt, er, rev;
    f2 w8, w32, v1, v2, v256, y[4], x[8], last;
    unsigned row[53];
};
constexpr int KG_STEPS = 57;
constexpr int kg_row(int I) { return I == 1 ? 32 : I == 4 ? 1 : I == 5 ? 4 : I < 7 ? -1 : I < 27 ? I + 26 : I < 29 ? I - 25 : I < 56 ? I - 24 : -1; }
template <int I, bool BESIDE_MFMA> DI void kg_step(Kgen& k) {
    auto mul = [](f2 a, f2 b) { if constexpr (BESIDE_MFMA) return cmul_s(a, b); else return cmul(a, b); };
    constexpr float LOG2E = 1.44269504088896f, INV2PI = 0.159154943091895f;
    const f2 prev = k.last;
    if constexpr (I == 0) { k.dt = rbf(__builtin_amdgcn_exp2f(blo(k.ldt) * LOG2E)); k.er = rbf(__builtin_amdgcn_exp2f(blo(k.la) * LOG2E)); tie(k.dt); tie(k.er); k.row[0] = 0x3f80u; }
    else if constexpr (I == 1) { k.er = (-k.er * k.dt) * LOG2E; k.rev = (blo(k.ai) * k.dt) * INV2PI; k.x[0] = f2{fmul_s(blo(k.cr), k.dt) * 2.f, fmul_s(blo(k.ci), -2.f * k.dt)}; tie(k.er); tie(k.rev); tie(k.x[0]); k.last = k.x[0]; }
    else if constexpr (I == 2) { k.v1 = cexp_at(k.er, -k.rev, 1.f); tie(k.v1); }
    else if constexpr (I == 3) { k.v256 = cexp_at(k.er, -k.rev, 256.f); tie(k.v256); }
    else if constexpr (I == 4) { k.w8 = cexp_at(k.er, k.rev, 8.f); tie(k.w8); k.last = k.w8; }
    else if constexpr (I == 5) { k.w32 = cexp_at(k.er, k.rev, 32.f); tie(k.w32); k.last = k.w32; }
    else if constexpr (I == 6) { k.v2 = mul(k.v1, k.v1); }
    else if constexpr (I == 7) { k.last = k.x[1] = mul(k.x[0], k.v1); }
    else if constexpr (I < 14) { constexpr int i = I - 6; k.last = k.x[i] = mul(k.x[i - 2], k.v2); }
    else if constexpr (I < 18) { constexpr int i = 2 * (I - 14); k.last = k.x[i] = mul(k.x[i], k.v256); }
    else if constexpr (I < 22) { constexpr int i = 4 * (I & 1); k.last = k.x[i] = mul(k.x[i], k.v256); }
    else if constexpr (I < 27) { k.last = k.x[0] = mul(k.x[0], k.v256); }
    else if constexpr (I == 27) { k.y[0] = k.w32; k.y[1] = k.w8; k.last = k.y[2] = mul(k.w8, k.w8); }
    else if constexpr (I == 28) { k.last = k.y[3] = mul(k.y[2], k.w8); }
    else if constexpr (I < 56) { constexpr int j = (I - 24) & 3; k.last = k.y[j] = mul(k.y[j], k.w32); }
    if constexpr (I > 0 && kg_row(I - 1) >= 0) {
        k.row[kg_row(I - 1)] = pk(prev.x, prev.y); tie(k.row[kg_row(I - 1)]);
        if constexpr (!BESIDE_MFMA) lds_st_tid<XY_BIAS + kg_row(I - 1) * XY_ROW>(k.row[kg_row(I - 1)]);
    }
    if constexpr (!BESIDE_MFMA && I == 2) lds_st_tid<XY_BIAS>(k.row[0]);
}
template <int LO, int HI> DI void kg_write(const Kgen& k) {
    sfor<HI - LO>([&](auto ii) { constexpr int r = LO + ii; if constexpr (r < 53) lds_st_tid<XY_BIAS + r * XY_ROW>(k.row[r]); });
}
DI void kg_set_base(const Ctx& cx) { set_m0(L_XY - XY_BIAS + (cx.w >> 1) * XY_CH + (cx.w & 1) * 256); }

DI void kernel_table(const Ctx& cx) {
    const unsigned cidx = cx.w >> 1, half = cx.w & 1, r = cx.lane & 15, g4 = cx.lane >> 4;
    const unsigned xa = opaque(L_XY + cidx * XY_CH + XY_X + r * XY_ROW + g4 * 16), ya = opaque(xa - XY_X + half * (16 * XY_ROW));
    f4 k0, k1;
    v4u a[8], b0[8], b1[8];
    sfor<8>([&](auto ks) { a[ks] = lds_ld<v4u>(ya + ks * 64); b0[ks] = lds_ld<v4u>(xa + ks * 64); b1[ks] = lds_ld<v4u>(xa + 16 * XY_ROW + ks * 64); PIN(); });
    const unsigned i0 = sel(r < 8, r, sel(r < 12, 240 + 2 * r, 128 * (r & 14) - 1024 + 4 * (r & 1)));
    const unsigned i1 = sel(r <= 4, 1024 + 256 * r, 2049);
    const unsigned kp = L_KP + cidx * KP_CH + 256 * half + 64 * g4, a0 = opaque(kp + 2 * i0), a1 = opaque(kp + 2 * i1);
    PIN();
    sfor<8>([&](auto ks) { if constexpr (ks == 0) { mfma16z(k0, a[ks], b0[ks]); mfma16z(k1, a[ks], b1[ks]); } else { mfma16(k0, a[ks], b0[ks]); mfma16(k1, a[ks], b1[ks]); } });
    PIN(); asm volatile("s_nop 7" : "+v"(k0), "+v"(k1)); PIN();
    sfor<2>([&](auto e2) {
        const unsigned v0 = pk(k0[2 * e2], k0[2 * e2 + 1]), v1 = pk(k1[2 * e2], k1[2 * e2 + 1]);
        lds_st<u16>(a0 + 32 * e2, (u16)v0); lds_st<u16>(a0 + 32 * e2 + 16, (u16)(v0 >> 16));
        lds_st<u16>(a1 + 32 * e2, (u16)v1); lds_st<u16>(a1 + 32 * e2 + 16, (u16)(v1 >> 16));
    });
}
DI void expand_taps(const Ctx& cx, unsigned r0) {
    const unsigned m = 64 * (cx.w & 1) + cx.lane, kp = opaque(L_KP + (cx.w >> 1) * KP_CH + 16 * m);
    const v4u d = lds_ld<v4u>(kp);
    const unsigned d4 = lds_ld<unsigned>(kp + 16), e0 = lds_ld<unsigned>(kp + 2048), e4 = lds_ld<unsigned>(kp + 2048 + 16);
    const bool s1 = m < 32, s3 = cx.w & 1;
    const unsigned sel0 = s1 ? 0x01000302u : 0x01000100u, sel1 = s1 ? 0x01000302u : 0x01000504u, sel3 = s3 ? 0x05040504u : sel1;
    v4u o0, o1;
    o0.w = __builtin_amdgcn_perm(d.x, d.x, sel0);
    o0.z = __builtin_amdgcn_perm(d.z, s3 ? d.x : d.y, sel1);
    o0.y = __builtin_amdgcn_perm(d.z, d.z, sel0);
    o0.x = __builtin_amdgcn_perm(d4, d.w, sel3);
    o1.w = __builtin_amdgcn_perm(e0, e0, 0x01000100u);
    o1.z = o1.w;
    o1.y = __builtin_amdgcn_perm(e4, e0, (m & 1) ? 0x05040504u : 0x01000504u);
    o1.x = __builtin_amdgcn_perm(e4, e4, 0x01000100u);
    const unsigned a = opaque(r0 + 16 * (127 - m));
    lds_st<v4u>(a + 2048, o0);
    lds_st<v4u>(a, o1);
    lds_st<v4u>(r0 + 4096 + 16 * (m & 1), v4u{0, 0, 0, 0});
}
template <bool COPY0> DI void shift_taps(const Ctx& cx, unsigned r0) {
    const unsigned m = 64 * (cx.w & 1) + cx.lane, src = opaque(r0 + 16 * m), dst = opaque(L_RC + (cx.w >> 1) * RC_CH + 16 * m);
    sfor<2>([&](auto it) {
        const v4u d = lds_ld<v4u>(src + it * 2048);
        const v2u n = lds_ld<v2u>(src + it * 2048 + 16);
        const unsigned q0 = __builtin_amdgcn_alignbyte(d.y, d.x, 2), q1 = __builtin_amdgcn_alignbyte(d.z, d.y, 2), q2 = __builtin_amdgcn_alignbyte(d.w, d.z, 2);
        const unsigned q3 = __builtin_amdgcn_alignbyte(n.x, d.w, 2), q4 = __builtin_amdgcn_alignbyte(n.y, n.x, 2);
        if constexpr (COPY0) lds_st<v4u>(dst + it * 2048, d);
        lds_st<v4u>(dst + it * 2048 + RC_COPY, v4u{q0, q1, q2, q3});
        lds_st<v4u>(dst + it * 2048 + 2 * RC_COPY, v4u{d.y, d.z, d.w, n.x});
        lds_st<v4u>(dst + it * 2048 + 3 * RC_COPY, v4u{q1, q2, q3, q4});
    });
    lds_st<v2u>(L_RC + ((cx.lane & 31) >> 2) * RC_COPY + 4096 + (cx.lane & 3) * 8, v2u{0, 0});
}
template <class HOOK, class POST> DI void conv_pair(Ctx& cx, unsigned ch, unsigned ubase, float dskip, HOOK&& hook, POST&& post) {
    const unsigned cidx = cx.w >> 1, half = cx.w & 1, r = cx.lane & 15, g4 = cx.lane >> 4;
    f4 ca[8];
    {
        const unsigned aa0 = L_RC + cidx * RC_CH + ((~r) & 3) * RC_COPY + 2 * (2028 - 4 * (r >> 2) + 8 * g4) - 3584 - 256 * half - 192;
        const unsigned aa = opaque(aa0), ab = opaque(aa0 + 8), bb = opaque(ubase + 448 + 32 * r + 16 * g4 - 256 * half - 192);
        v2u al[2][8], ah[2][8];
        v4u fb[2][8];
        auto rd = [&](auto d_, auto i_) {
            constexpr int d = d_, i = i_, e = i / 3, o = (3 - d) * 64;
            if constexpr (i % 3 == 0) al[d & 1][e] = lds_ld<v2u>(aa + (7 - e) * 512 + o);
            else if constexpr (i % 3 == 1) ah[d & 1][e] = lds_ld<v2u>(ab + (7 - e) * 512 + o);
            else fb[d & 1][e] = lds_ld<v4u>(bb + e * 512 + o);
            PIN();
        };
        sfor<12>([&](auto i) { rd(ic<0>{}, i); });
        sfor<4>([&](auto d_) {
            constexpr int d = d_;
            sfor<8>([&](auto n_) {
                constexpr int n = n_;
                sfor<n + 1>([&](auto e_) {
                    constexpr int e = e_, idx = n * (n + 1) / 2 + e;
                    const v4u a = {al[d & 1][e].x, al[d & 1][e].y, ah[d & 1][e].x, ah[d & 1][e].y};
                    if constexpr (d == 0 && e == 0) mfma16z(ca[n], a, fb[d & 1][n - e]); else mfma16(ca[n], a, fb[d & 1][n - e]);
                    PIN();
                    if constexpr (d == 0) { if constexpr (idx < 6) { rd(ic<0>{}, ic<12 + 2 * idx>{}); rd(ic<0>{}, ic<13 + 2 * idx>{}); } else if constexpr (idx >= 12) rd(ic<1>{}, ic<idx - 12>{}); }
                    else if constexpr (d < 3 && idx % 3 != 2) rd(ic<d + 1>{}, ic<idx - idx / 3>{});
                });
            });
            hook(d_);
        });
    }
    STAMP(7);
    post();
    PIN(); asm volatile("s_nop 7"); PIN();
    sfor<8>([&](auto n) { tie(ca[n]); });
    const unsigned ra = opaque(L_RED2 + cx.lane * 16 + cx.w * 4096), rb = opaque(L_RED2 + cx.lane * 16 + (cx.w ^ 1) * 4096);
    if (half == 0) { sfor<4>([&](auto i) { lds_st<f4>(ra + i * 1024, ca[4 + i]); }); }
    else { sfor<4>([&](auto i) { lds_st<f4>(ra + i * 1024, ca[i]); ca[i] = ca[4 + i]; }); }
    const unsigned uaddr = opaque(ubase + 480 + 2 * (1024 * half + 16 * r + 4 * g4));
    f4 uf[4];
    sfor<4>([&](auto i) { const v2u u = lds_ld<v2u>(uaddr + i * 512); uf[i] = f4{blo(u.x), bhi(u.x), blo(u.y), bhi(u.y)} * dskip + ca[i]; });
    BARRIER();
    v2u pkd[4];
    sfor<4>([&](auto i) { const f4 y = uf[i] + lds_ld<f4>(rb + i * 1024); pkd[i] = v2u{pk(y[0], y[1]), pk(y[2], y[3])}; });
    const unsigned vo = 2 * (256 * (g4 & 1) + 16 * r + 8 * (g4 >> 1)), so = OFF_ST + ch * 4096 + 2048 * half;
    sfor<2>([&](auto i2) {
        const auto sx = __builtin_amdgcn_permlane16_swap(pkd[2 * i2].x, pkd[2 * i2 + 1].x, false, false);
        const auto sy = __builtin_amdgcn_permlane16_swap(pkd[2 * i2].y, pkd[2 * i2 + 1].y, false, false);
        st128c<i2 * 1024>(v4u{sx[0], sy[0], sx[1], sy[1]}, vo, cx.rws, so);
    });
}

__global__ void __launch_bounds__(256, 1) s4d_mixer_kernel(Params p) {
    extern __shared__ __attribute__((aligned(16))) char smem[];
    const unsigned tid = threadIdx.x, lane = tid & 63, w = rfl(tid >> 6);
    const unsigned b = blockIdx.x, g = b & 7, q = b >> 3;
    {
        u64 pc;
        asm volatile("s_getpc_b64 %0" : "=s"(pc) :: ACLOB_ALL);
        const unsigned line = (q & 7) * (CODE_LINES / 8) + w * (CODE_LINES / 32) + (lane & (CODE_LINES / 32 - 1));
        asm volatile("global_load_dword a255, %0, %1" :: "v"(line * 128u), "s"(pc & ~127ull));
    }
    asm volatile("" :: "v"((unsigned)(size_t)(LAS char*)smem));
    Ctx cx;
    STAMP(0);
    const unsigned tb = 32 * g + q;
    const rsrc_t rx = __builtin_amdgcn_make_buffer_rsrc((void*)p.x, 0, SL * SD * 2, 0x00020000);
    const rsrc_t rw1 = __builtin_amdgcn_make_buffer_rsrc((void*)p.w1, 0, SD * SF * 2, 0x00020000);
    unsigned xa[5], xb[5];
    v2u xa0, xa1, xb0, xb1;
    {
        const unsigned so = (8 * tb + 2 * w) * (SD * 2);
        asm volatile("s_nop 4");
        xa0 = ld64<0>(lane * 8, rx, so); xa1 = ld64<512>(lane * 8, rx, so); xa[4] = ld32<1024>(lane * 4, rx, so);
        xb0 = ld64<1280>(lane * 8, rx, so); xb1 = ld64<1280 + 512>(lane * 8, rx, so); xb[4] = ld32<1280 + 1024>(lane * 4, rx, so);
    }
    const unsigned tx = (512 * (g >> 1) + 16 * q + (lane & 15)) * 1280 + (lane >> 4) * 128;
    unsigned tw;
    {
        const unsigned st = lane >= 28, i = lane - 28 * st, hi = i >= 14;
        tw = sel(lane < 56, (64 * st + 2 * q + hi) * (SF * 2) + (g & 1) * 1792 + (i - 14 * hi) * 128, OOB);
    }
    if (w == 1) l2_touch(tx, rx, 0); else l2_touch(tw, rw1, sel(w == 0, 0, 2 * w - 2) * (64 * SF * 2));
    auto touch2 = [&]() {
        if (w == 0 || w == 3) l2_touch(sel(w == 0 || lane < 32, tx, OOB), rx, sel(w == 0, 4, 8) * 128);
        else l2_touch(tw, rw1, (4 + 2 * w) * (64 * SF * 2));
    };
    cx.rws = __builtin_amdgcn_make_buffer_rsrc((void*)p.ws, 0, WS_USED, 0x00020000);
    cx.tid = tid; cx.lane = lane; cx.w = w;
    v2u wv = ld64c<0>(opaque(OFF_W), cx.rws, 0);
    const bool spare = q >= 28;
    const unsigned ch0 = 80 * g + sel(spare, 6 * q - 112, 2 * q), cidx = w >> 1;
    const rsrc_t rla = __builtin_amdgcn_make_buffer_rsrc((void*)p.lar, 0, SD * 256, 0x00020000);
    const rsrc_t rai = __builtin_amdgcn_make_buffer_rsrc((void*)p.aim, 0, SD * 256, 0x00020000);
    const rsrc_t rcr = __builtin_amdgcn_make_buffer_rsrc((void*)p.cr, 0, SD * 256, 0x00020000);
    const rsrc_t rci = __builtin_amdgcn_make_buffer_rsrc((void*)p.ci, 0, SD * 256, 0x00020000);
    const unsigned pvo = w * 128 + lane * 2;
    Kgen kg;
    kg.last = f2{0.f, 0.f};
    kg.la = ld16(pvo, rla, ch0 * 256); kg.ai = ld16(pvo, rai, ch0 * 256); kg.cr = ld16(pvo, rcr, ch0 * 256); kg.ci = ld16(pvo, rci, ch0 * 256);
    const unsigned rt = 4 * g + (q >> 3), ct = q & 7;
    const unsigned nk = 14 - (w >> 1), rot = (ct * 13) >> 3, k0s = 14 * w - (w == 3 ? 1 : 0) + rot, klast = k0s + 13 - (rot ? 14 : 0);
    auto late_setup = [&]() {
        sfor<2>([&](auto i) { tie(xa0); tie(xa1); tie(xb0); tie(xb1); });
        tie(xa[4]); tie(xb[4]);
        xa[0] = xa0.x; xa[1] = xa0.y; xa[2] = xa1.x; xa[3] = xa1.y; xb[0] = xb0.x; xb[1] = xb0.y; xb[2] = xb1.x; xb[3] = xb1.y;
    };
    auto take_epoch = [&]() {
        tie(wv); tie(kg.la); tie(kg.ai); tie(kg.cr); tie(kg.ci);
        const u64 e = ((u64)rfl(wv.y) << 32 | rfl(wv.x)) + 0x9E3779B97F4A7C15ull;
        cx.e_lo = (unsigned)e; cx.e_hi = (unsigned)(e >> 32);
        cx.flag_vo = tid == 0 ? 0u : OOB;
        cx.w16a = lane * 16; cx.w16b = lane * 16 + 4096;
        cx.thr = nk - rot; cx.w2a = OFF_W2T + (ct * NKS + k0s) * 5120; cx.w2b = cx.w2a - nk * 5120;
        cx.w2l = sel(w < 2, OFF_W2T + (ct * NKS + klast) * 5120, WS_USED); cx.w2so = 0;
    };
    auto half16 = [&](const u16* base, unsigned ch) { const unsigned v = *(const CAS unsigned*)(base + ch); return (v >> (16 * cidx)) & 0xffffu; };

    const unsigned ti = 4 * (g >> 1) + q / 7, tj = 7 * (g & 1) + q % 7, wf = w >> 1, wt = w & 1;
    unsigned gx[4], gw[4];
    sfor<4>([&](auto ii) {
        gx[ii] = (32 * w + 8 * ii + (lane >> 3)) * 1280 + 16 * ((lane & 7) ^ (4 * (ii & 1) + (lane >> 4))) - ii * 1024;
        gw[ii] = (16 * w + 4 * ii + (lane >> 4)) * (SF * 2) + 16 * ((lane & 15) ^ (((lane >> 4) << 2) | ii)) - ii * 1024;
    });
    const unsigned sx0 = ti * (128 * 1280), sw0 = tj * 256;
    auto piece = [&](auto s_, auto pi_) {
        constexpr int s = s_, pi = pi_, ii = pi & 3;
        if constexpr (ii == 0) set_m0((s & 3) * 32768 + (pi >> 2) * 16384 + w * 4096);
        if constexpr (pi < 4) dma<ii * 1024>(gx[ii], rx, sx0 + s * 128); else dma<ii * 1024>(gw[ii], rw1, sw0 + s * (64 * SF * 2));
    };
    auto stage = [&](auto s_) { sfor<8>([&](auto pi) { piece(s_, pi); }); };
    const unsigned sid = 27 * g + sel(q < 26, q, 26), sks = sid >> 2, sqq = sid & 3;
    const rsrc_t rb1 = __builtin_amdgcn_make_buffer_rsrc((void*)p.b1, 0, SF * 2, 0x00020000);
    unsigned b1v = ld16((tid & 127) * 2, rb1, tj * 256);
    if (!spare) {
        const rsrc_t rw2 = __builtin_amdgcn_make_buffer_rsrc((void*)p.w2, 0, SD * SF * 2, 0x00020000);
        set_m0(L_W2S + w * 3072);
        sfor<3>([&](auto ii) {
            const unsigned P = 64 * (3 * w + ii) + lane, row = (P * 3277) >> 16, vo = row * (1280 - 320) + P * 16 - ii * 1024;
            dma<ii * 1024>(sel(ii == 0 || w < 3, vo, OOB), rw2, sks * (32 * 1280) + sqq * 320);
        });
        VMWAIT(10);
    } else VMWAIT(7);
    late_setup();
    STAMP(1);
    const f2 st = layernorm_rows(xa, xb, cx, [&]() { if (!spare) stage(ic<0>{}); }, [&]() { touch2(); if (!spare) stage(ic<1>{}); });
    if (!spare) VMWAIT(17); else VMWAIT(1);
    take_epoch(); tie(b1v);
    BARRIER();
    store_xn(cx, st, tb);
    unsigned ubase;

    if (!spare) {
        {
            const unsigned g4 = lane >> 4, a0 = opaque(L_W2S + (16 * (g4 >> 1) + 4 * (g4 & 1) + ((lane & 15) >> 2)) * 320 + 8 * (lane & 3) + w * 96);
            v4u fr[3];
            sfor<3>([&](auto ii) { const v2u lo = lds_tr(a0 + ii * 32), hi = lds_tr(a0 + ii * 32 + 8 * 320); fr[ii] = v4u{lo.x, lo.y, hi.x, hi.y}; });
            sfor<3>([&](auto ii) {
                const unsigned fi = 3 * w + ii;
                st128c<0>(fr[ii], cx.w16a, cx.rws, sel(fi < 10, OFF_W2T + (2 * sqq * NKS + sks) * 5120 + fi * 1024 + sel(fi >= 5, (NKS - 1) * 5120, 0), WS_USED));
            });
        }
        stage(ic<2>{});
        kg.ldt = half16(p.ldt, ch0);
        STAMP(2);
        unsigned ax[2][4], aw[2][4];
        {
            const unsigned row = 64 * wt + (lane & 31), hh = lane >> 5, blk = (lane >> 4) & 1, qr = (lane & 15) >> 2, pp = lane & 3;
            sfor<4>([&](auto ks) { ax[0][ks] = opaque(row * 128 + 16 * ((2 * ks + hh) ^ ((row >> 1) & 7))); ax[1][ks] = opaque(ax[0][ks] + 65536); });
            sfor<4>([&](auto i) {
                constexpr int mf = i >> 1, t = i & 1;
                const unsigned r = 8 * hh + 4 * t + qr, chn = 4 * (2 * wf + (mf ^ wt)) + 2 * blk + (pp >> 1);
                aw[0][i] = opaque(16384 + 256 * r + 16 * (chn ^ ((qr << 2) | ((2 * hh + t) & 3))) + 8 * (pp & 1)); aw[1][i] = opaque(aw[0][i] + 65536);
            });
        }
        const unsigned onesel = ((lane ^ (lane >> 4)) & 1) ? 0u : 0x3f803f80u;
        v4u ones = {onesel, onesel, onesel, onesel};
        tie(ones);
        constexpr int W7 = 4, W8 = 4, W9 = 4, WG0 = 12, WG1 = 28;
        f16v acc[2][2];
        f4 csum;
        v2u fa[2][4], plv, st0, st1;
        v4u fb[2][2], ub0, ub1;
        auto rd = [&](auto n_, auto i_) {
            constexpr int n = n_, i = i_, s = n >> 2, ks = n & 3, bf = n & 1, hi = (s & 3) >> 1;
            constexpr unsigned so = (s & 1) * 32768;
            if constexpr (n < 40) {
                if constexpr (i == 0) fb[bf][0] = lds_ld<v4u>(ax[hi][ks] + so);
                else if constexpr (i == 3) fb[bf][1] = lds_ld<v4u>(ax[hi][ks] + so + 4096);
                else fa[bf][i < 3 ? i - 1 : i - 2] = lds_tr(aw[hi][i < 3 ? i - 1 : i - 2] + so + ks * 4096);
            }
            PIN();
        };
        VMWAIT(24);
        BARRIER();
        sfor<6>([&](auto i) { rd(ic<0>{}, i); });
        piece(ic<3>{}, ic<0>{}); piece(ic<3>{}, ic<1>{});
        sfor<40>([&](auto n0_) {
            constexpr int n = n0_ + 1, s = n >> 2, ks = n & 3, bf = (n - 1) & 1;
            constexpr bool first = n == 1;
            if constexpr (ks == 0 && n < 40) {
                constexpr int WAITS[10] = {0, 23, 16, 17, 17, 16, 17, 16, 8 + 2 + W7, W7 + W8};
                VMWAIT(WAITS[s]);
                if constexpr (s == 7) { tie(plv); if (!flags_match(plv, cx)) poll<2>(OFF_F1 + tid * FSTR, cx); }
                BARRIER();
                if constexpr (s == 2) publish(cx, OFF_F1 + tb * FSTR);
                if constexpr (s == 5) plv = ld64c<0>(tid * FSTR, cx.rws, OFF_F1);
                if constexpr (s == 7) {
                    const unsigned vo = (64 * wt + (lane & 31)) * 8;
                    st0 = ld64c<0>(vo, cx.rws, OFF_STATS + ti * 1024); st1 = ld64c<256>(vo, cx.rws, OFF_STATS + ti * 1024);
                }
                if constexpr (s == 8) kg_set_base(cx);
                PIN();
            }
            auto vm = [&](auto m_) {
                constexpr int pi = 2 * ks + m_;
                if constexpr (s <= 6) piece(ic<s + 3>{}, ic<pi>{});
                else if constexpr (s <= 9 && m_ == 0) w2_prefetch<(s - 7) * 4 + ks, (s - 7) * 4 + ks + 1>(cx);
                PIN();
            };
            auto side = [&](auto m_) {
                constexpr int m = m_;
                if constexpr (n <= 6) { if constexpr (m == 0) kg_step<n - 1, true>(kg); }
                else if constexpr (n < 32) { kg_step<6 + 2 * (n - 7) + m, true>(kg); if constexpr (n == 31 && m == 1) kg_step<56, true>(kg); }
                else if constexpr (n < 40) kg_write<(n - 32) * 7 + 4 * m, (n - 32) * 7 + 4 + 3 * m>(kg);
                PIN();
            };
            const v4u a0 = {fa[bf][0].x, fa[bf][0].y, fa[bf][1].x, fa[bf][1].y}, a1 = {fa[bf][2].x, fa[bf][2].y, fa[bf][3].x, fa[bf][3].y};
            if constexpr (first) mfma32z(acc[0][0], a0, fb[bf][0]); else mfma32(acc[0][0], a0, fb[bf][0]);
            PIN(); rd(ic<n>{}, ic<0>{}); rd(ic<n>{}, ic<1>{}); rd(ic<n>{}, ic<2>{}); rd(ic<n>{}, ic<3>{});
            if constexpr (first) mfma32z(acc[0][1], a0, fb[bf][1]); else mfma32(acc[0][1], a0, fb[bf][1]);
            PIN(); rd(ic<n>{}, ic<4>{}); rd(ic<n>{}, ic<5>{}); vm(ic<0>{});
            if constexpr (first) mfma16z(csum, a0, ones); else mfma16(csum, a0, ones);
            if constexpr (first) mfma32z(acc[1][0], a1, fb[bf][0]); else mfma32(acc[1][0], a1, fb[bf][0]);
            PIN(); side(ic<0>{}); vm(ic<1>{});
            if constexpr (first) mfma32z(acc[1][1], a1, fb[bf][1]); else mfma32(acc[1][1], a1, fb[bf][1]);
            PIN(); side(ic<1>{});
        });
        STAMP(3);
        tie(csum);
        lds_st<f4>(L_CS + (64 * wf + 32 * wt + 16 * (lane & 1) + 4 * (lane >> 4)) * 4, csum);
        if (w < 2) lds_st<float>(L_B1 + tid * 4, blo(b1v));
        tie(st0); tie(st1);
        {
            const unsigned uvo = (tid >> 1) * 10240 + (tid & 1) * 16;
            ub0 = ld128c<0>(uvo, cx.rws, OFF_XNB + ch0 * 16); ub1 = ld128c<0>(uvo, cx.rws, OFF_XNB + 128 * 10240 + ch0 * 16);
        }
        BARRIER();
        MFMA_DRAIN();
        {
            const unsigned hh = lane >> 5;
            const unsigned hvo = sel(tj == 13 && wf == 1, OOB, ((lane >> 4) & 1) * (NKS * 1024) + (hh * 16 + (lane & 15)) * 16);
            const unsigned hso = OFF_H + ((8 * ti + 4 * wt) * NKS + 4 * tj + 2 * wf) * 1024;
            const unsigned cam[2] = {opaque(L_CS + (64 * wf + 32 * wt + 4 * hh) * 4), opaque(L_CS + (64 * wf + 32 * (wt ^ 1) + 4 * hh) * 4)};
            constexpr float C1 = -2.302208198f, C2 = -0.1029432397f;
            sfor<2>([&](auto mf_) {
                constexpr int mf = mf_;
                f4 cs[4], bb[4];
                sfor<4>([&](auto ga) { cs[ga] = lds_ld<f4>(cam[mf] + 32 * ga); bb[ga] = lds_ld<f4>(cam[mf] + 512 + 32 * ga); });
                sfor<2>([&](auto nt_) {
                    constexpr int nt = nt_;
                    tie(acc[mf][nt]);
                    const v2u sv = nt ? st1 : st0;
                    const float sa = __uint_as_float(sv.x), sb = __uint_as_float(sv.y);
                    sfor<2>([&](auto chk) {
                        f2 z[4], e[4];
                        sfor<4>([&](auto i) {
                            constexpr int ga = 2 * chk + (i >> 1), e2 = 2 * (i & 1);
                            z[i] = f2{acc[mf][nt][4 * ga + e2], acc[mf][nt][4 * ga + e2 + 1]} * sa + (f2{cs[ga][e2], cs[ga][e2 + 1]} * sb + f2{bb[ga][e2], bb[ga][e2 + 1]});
                        });
                        sfor<4>([&](auto i) { e[i] = z[i] * (z[i] * z[i] * C2 + C1); });
                        sfor<4>([&](auto i) { e[i] = f2{__builtin_amdgcn_exp2f(e[i].x), __builtin_amdgcn_exp2f(e[i].y)}; });
                        sfor<4>([&](auto i) { e[i] = e[i] + 1.f; });
                        sfor<4>([&](auto i) { e[i] = f2{__builtin_amdgcn_rcpf(e[i].x), __builtin_amdgcn_rcpf(e[i].y)}; });
                        unsigned o[4];
                        sfor<4>([&](auto i) { const f2 r = z[i] * e[i]; o[i] = pk(r.x, r.y); });
                        constexpr int idx = 4 * mf + 2 * nt + chk, f0 = WG0 + (WG1 - WG0) * idx / 8, f1 = WG0 + (WG1 - WG0) * (idx + 1) / 8;
                        w2_prefetch<f0, f1>(cx);
                        st128c<chk * 512>(v4u{o[0], o[1], o[2], o[3]}, hvo, cx.rws, hso + (2 * nt * NKS + (mf ^ wt)) * 1024);
                        PIN();
                    });
                });
            });
        }
        STAMP(4);
        w2_prefetch<WG1, WG1 + 3>(cx);
        kernel_table(cx);
        w2_prefetch<WG1 + 3, WG1 + 6>(cx);
        BARRIER();
        const unsigned r0 = L_RC + cidx * RC_CH;
        expand_taps(cx, r0);
        VMWAIT(6);
        tie(ub0); tie(ub1);
        {
            const unsigned ua = opaque(L_U + 480 + (tid & 1) * U_CH + (tid >> 1) * 16);
            lds_st<v4u>(ua, ub0); lds_st<v4u>(ua + 2048, ub1);
            lds_st<v2u>(L_U + (tid >> 7) * U_CH + sel((tid & 127) < 60, tid & 127, 59) * 8, v2u{0, 0});
        }
        BARRIER();
        publish(cx, OFF_F2 + (16 * ti + tj) * FSTR);
        STAMP(5);
        w2_prefetch<WG1 + 6, 40>(cx);
        shift_taps<false>(cx, r0);
        ubase = L_U + cidx * U_CH;
    } else {
        STAMP(2);
        kg_set_base(cx);
#pragma unroll 1
        for (unsigned round = 0; round < 3; ++round) {
            CYC(0);
            kg.ldt = half16(p.ldt, ch0 + 2 * round);
            sfor<2>([&](auto i) { kg_step<i, false>(kg); });
            CYC(1);
            PIN();
            const unsigned cn = (ch0 + 2 * round + 2) * 256;
            kg.la = ld16(pvo, rla, cn); kg.ai = ld16(pvo, rai, cn); kg.cr = ld16(pvo, rcr, cn); kg.ci = ld16(pvo, rci, cn);
            PIN();
            sfor<4>([&](auto i) { kg_step<i + 2, false>(kg); });
            CYC(2);
            sfor<KG_STEPS - 6>([&](auto i) { kg_step<i + 6, false>(kg); });
            CYC(3);
            VMWAIT(0);
            CYC(4);
            tie(kg.la); tie(kg.ai); tie(kg.cr); tie(kg.ci);
            BARRIER();
            CYC(5);
            if (round == 0) publish(cx, OFF_F1 + tb * FSTR);
            kernel_table(cx);
            CYC(6);
            BARRIER();
            CYC(7);
            expand_taps(cx, L_R0 + (2 * round + cidx) * RC_COPY);
            CYC(8);
        }
        STAMP(3);
        poll<4>(OFF_F1 + tid * FSTR, cx);
        BARRIER();
        STAMP(4);
        {
            v4u ub[6];
            unsigned ua[6];
            sfor<6>([&](auto i) {
                const unsigned P = tid + 256 * i, blk = (P * 10923) >> 16, c = P - 6 * blk;
                ub[i] = ld128c<0>(blk * 10240 + c * 16, cx.rws, OFF_XNB + ch0 * 16);
                ua[i] = L_U6 + 480 + c * U_CH + blk * 16;
            });
            w2_prefetch<0, 4>(cx);
            shift_taps<true>(cx, L_R0 + cidx * RC_COPY);
            sfor<3>([&](auto i) { lds_st<v2u>(L_U6 + (2 * i + (tid >> 7)) * U_CH + sel((tid & 127) < 60, tid & 127, 59) * 8, v2u{0, 0}); });
            VMWAIT(4);
            sfor<6>([&](auto i) { tie(ub[i]); lds_st<v4u>(ua[i], ub[i]); });
        }
        STAMP(5);
#pragma unroll 1
        for (unsigned round = 0; round < 2; ++round) {
            const float dsk = blo(half16(p.dsk, ch0 + 2 * round));
            BARRIER();
            conv_pair(cx, ch0 + 2 * round + cidx, L_U6 + (2 * round + cidx) * U_CH, dsk, [&](auto d) {
                if (round == 0) w2_prefetch<4 + 9 * d / 2, 4 + 9 * (d + 1) / 2>(cx); else w2_prefetch<22 + 9 * d / 2, 22 + 9 * (d + 1) / 2>(cx);
            }, [&]() {});
            shift_taps<true>(cx, L_R0 + (2 * round + 2 + cidx) * RC_COPY);
        }
        ubase = L_U6 + (4 + cidx) * U_CH;
    }
    cx.w2so = rfl(cx.w2so); cx.w2l = rfl(cx.w2l); cx.w2a = rfl(cx.w2a); cx.w2b = rfl(cx.w2b); cx.thr = rfl(cx.thr); cx.e_lo = rfl(cx.e_lo); cx.e_hi = rfl(cx.e_hi);
    ubase = rfl(ubase);
    const unsigned chl = rfl(ch0 + sel(spare, 4, 0));
    const float dskip = blo(half16(p.dsk, chl));
    BARRIER();
    STAMP(6);

    constexpr int WIN = 4;
    v4u wx[6], hf[WIN][4];
    unsigned hv[4], hso;
    sfor<4>([&](auto t) { hv[t] = (4 * rt + ((t + w) & 3)) * (NKS * 1024) + lane * 16; });
    const unsigned hsa = OFF_H + k0s * 1024, hsb = hsa - nk * 1024, hsl = sel(w < 2, OFF_H + klast * 1024, WS_USED);
    auto ldh = [&](auto s_, auto t_) {
        constexpr int s = s_, t = t_;
        if constexpr (s < 14) {
            if constexpr (t == 0) hso = slot_off<s, 1024>(hsa, hsb, hsl, cx.thr);
            hf[s % WIN][t] = ld128c<0>(hv[t], cx.rws, hso);
        }
        PIN();
    };
    {
        const unsigned f2vo = OFF_F2 + (16 * (rt >> 1) + sel(lane < 14, lane, 0)) * FSTR;
        v2u plv2;
        conv_pair(cx, chl + cidx, ubase, dskip,
            [&](auto d) {
                if constexpr (d == 2) plv2 = ld64c<0>(f2vo, cx.rws, 0);
                if constexpr (d < 3) w2_prefetch<40 + 8 * d, 48 + 8 * d>(cx);
            },
            [&]() {
                VMWAIT(8);
                tie(plv2);
                if (!flags_match(plv2, cx)) poll<1>(f2vo, cx);
                sfor<WIN - 1>([&](auto s) { sfor<4>([&](auto t) { ldh(s, t); }); });
                wx[0] = ld128c<0>(cx.w16b, cx.rws, cx.w2so);
                sfor<4>([&](auto c) { wx[1 + c] = ld128c<c * 1024>(cx.w16a, cx.rws, cx.w2l); });
                wx[5] = ld128c<0>(cx.w16b, cx.rws, cx.w2l);
                STAMP(8);
            });
    }
    STAMP(9);
    f4 acc2[5][4];
    v2u xo[5], bo[5], plv3;
    v4u sv[3];
    const unsigned xvo = (16 * w + (lane & 15)) * (SD * 2) + 8 * (lane >> 4), xso = 64 * rt * (SD * 2) + 160 * ct;
    {
        const unsigned f3so = rfl(OFF_F3 + tb * FSTR);
        const rsrc_t rb2 = __builtin_amdgcn_make_buffer_rsrc((void*)p.b2, 0, SD * 2, 0x00020000);
        const unsigned svo = (tid >> 3) * 4096 + (tid & 7) * 16;
        sfor<14>([&](auto s_) {
            constexpr int s = s_;
            constexpr int later = (s + WIN - 2 < 14 ? WIN - 2 : 13 - s) * 4;
            constexpr int extra = s <= 2 ? 8 : (s == 4 || s == 5 || s == 7 || s == 8) ? 1 : (s == 10 || s == 11) ? 13 : 0;
            VMWAIT(later + extra);
            sfor<4>([&](auto t) { tie(hf[s % WIN][t]); });
            if constexpr (s == 3) { sfor<6>([&](auto i) { tie(wx[i]); }); BARRIER(); publish(cx, f3so); }
            if constexpr (s == 6) plv3 = ld64c<0>((lane & 31) * FSTR, cx.rws, OFF_F3 + ct * (32 * FSTR));
            if constexpr (s == 9) {
                tie(plv3);
                if (!flags_match(plv3, cx)) poll<1>(OFF_F3 + ct * (32 * FSTR) + (lane & 31) * FSTR, cx);
                const unsigned so = OFF_ST + 80 * ct * 4096 + 128 * rt;
                sv[0] = ld128c<0>(svo, cx.rws, so); sv[1] = ld128c<0>(svo, cx.rws, so + 32 * 4096); sv[2] = ld128c<0>(svo, cx.rws, sel(w < 2, so + 64 * 4096, WS_USED));
                sfor<5>([&](auto c) { xo[c] = ld64<c * 32>(xvo, rx, xso); });
                sfor<5>([&](auto c) { bo[c] = ld64<c * 32>(8 * (lane >> 4), rb2, 160 * ct); });
            }
            PIN();
            sfor<5>([&](auto c_) {
                constexpr int c = c_, f = 5 * s + c;
                sfor<4>([&](auto t) {
                    if constexpr (f >= 64) mfma16(acc2[c][t], wx[f - 64], hf[s % WIN][t]);
                    else if constexpr (s == 0) mfma16az<4 * f>(acc2[c][t], hf[s % WIN][t]);
                    else mfma16a<4 * f>(acc2[c][t], hf[s % WIN][t]);
                });
                PIN();
                if constexpr (c < 4) ldh(ic<s + WIN - 1>{}, c_);
            });
            if constexpr (s == 12) {
                sfor<3>([&](auto ii) { tie(sv[ii]); });
                const unsigned sa = opaque(L_SIMG + (tid >> 3) * SIMG_ROW + (tid & 7) * 16);
                lds_st<v4u>(sa, sv[0]); lds_st<v4u>(sa + 32 * SIMG_ROW, sv[1]); lds_st<v4u>(sa + 64 * SIMG_ROW, sv[2]);
                PIN();
            }
        });
    }
    STAMP(10);
    sfor<5>([&](auto c) { tie(xo[c]); tie(bo[c]); });
    unsigned rw[3];
    sfor<3>([&](auto t1) { rw[t1] = opaque(L_RED + ((w + t1 + 1) & 3) * 15360 + t1 * 5120 + lane * 16); });
    const unsigned ra = opaque(L_RED + w * 15360 + lane * 16);
    const unsigned sa = opaque(L_SIMG + (4 * (lane >> 4) + ((lane & 15) >> 2)) * SIMG_ROW + 32 * w + 8 * (lane & 3));
    PIN(); asm volatile("s_nop 7"); PIN();
    sfor<5>([&](auto c) { sfor<3>([&](auto t1) { tie(acc2[c][t1 + 1]); lds_st<f4>(rw[t1] + c * 1024, acc2[c][t1 + 1]); }); });
    f4 zb[5], xf[5];
    sfor<5>([&](auto c) {
        tie(acc2[c][0]);
        zb[c] = acc2[c][0] + f4{blo(bo[c].x), bhi(bo[c].x), blo(bo[c].y), bhi(bo[c].y)};
        xf[c] = f4{blo(xo[c].x), bhi(xo[c].x), blo(xo[c].y), bhi(xo[c].y)};
    });
    BARRIER();
    {
        const rsrc_t ro = __builtin_amdgcn_make_buffer_rsrc((void*)p.out, 0, SL * SD * 2, 0x00020000);
        sfor<5>([&](auto c) {
            const f4 z = (zb[c] + lds_ld<f4>(ra + c * 1024)) + (lds_ld<f4>(ra + 5120 + c * 1024) + lds_ld<f4>(ra + 10240 + c * 1024));
            const v2u s2 = lds_tr(sa + c * 16 * SIMG_ROW);
            const f4 u = z * -1.44269504088896f;
            const f4 d = f4{__builtin_amdgcn_exp2f(u[0]), __builtin_amdgcn_exp2f(u[1]), __builtin_amdgcn_exp2f(u[2]), __builtin_amdgcn_exp2f(u[3])} + 1.f;
            const f4 sz = f4{blo(s2.x), bhi(s2.x), blo(s2.y), bhi(s2.y)} * z;
            const f4 o = sz * f4{__builtin_amdgcn_rcpf(d[0]), __builtin_amdgcn_rcpf(d[1]), __builtin_amdgcn_rcpf(d[2]), __builtin_amdgcn_rcpf(d[3])} + xf[c];
            st64<c * 32>(v2u{pk(o[0], o[1]), pk(o[2], o[3])}, xvo, ro, xso);
        });
    }
    if (b == 0) publish(cx, OFF_W);
}

extern "C" void kernel_launch(void* const* d_in, const int* in_sizes, int n_in, void* d_out, int out_size, void* d_ws, size_t ws_size, hipStream_t stream) {
    Params p;
    p.x = (const u16*)d_in[0]; p.lar = (const u16*)d_in[1]; p.aim = (const u16*)d_in[2]; p.cr = (const u16*)d_in[3]; p.ci = (const u16*)d_in[4];
    p.ldt = (const u16*)d_in[5]; p.dsk = (const u16*)d_in[6]; p.w1 = (const u16*)d_in[8]; p.b1 = (const u16*)d_in[9];
    p.w2 = (const u16*)d_in[10]; p.b2 = (const u16*)d_in[11]; p.out = (u16*)d_out; p.ws = (char*)d_ws;
    (void)hipFuncSetAttribute((const void*)s4d_mixer_kernel, hipFuncAttributeMaxDynamicSharedMemorySize, LDS_BYTES);
    s4d_mixer_kernel<<<256, 256, LDS_BYTES, stream>>>(p);
}
```

```cpp
#define PROBE_KIND 0
#define PROBE_MUL 1
#define PROBE_WG 255
#include <hip/hip_runtime.h>
#include <stdint.h>
#include <utility>

typedef unsigned short u16;
typedef unsigned long long u64;
typedef unsigned v4u __attribute__((ext_vector_type(4)));
typedef unsigned v2u __attribute__((ext_vector_type(2)));
typedef float f2 __attribute__((ext_vector_type(2)));
typedef float f4 __attribute__((ext_vector_type(4)));
typedef float f16v __attribute__((ext_vector_type(16)));
typedef short s4v __attribute__((ext_vector_type(4)));
typedef __bf16 bf2v __attribute__((ext_vector_type(2)));
typedef __amdgpu_buffer_rsrc_t rsrc_t;
#define LAS __attribute__((address_space(3)))
#define CAS __attribute__((address_space(4)))
#define DI __device__ __forceinline__
#define PIN() __builtin_amdgcn_sched_barrier(0)
#define BARRIER() do { PIN(); asm volatile("s_waitcnt lgkmcnt(0)\n\ts_barrier" ::: "memory"); PIN(); } while (0)
#define VMWAIT(n) do { PIN(); asm volatile("s_waitcnt vmcnt(%0)" :: "i"(n) : "memory"); PIN(); } while (0)

#define FSTR 32
#define STAMPS 0

constexpr int SL = 2048, SD = 640, SF = 1728, NKS = SF / 32;
constexpr unsigned OOB = 0x40000000u;
constexpr unsigned OFF_W = 0, OFF_TMO = 128, OFF_F1 = 4096, OFF_F2 = OFF_F1 + 256 * FSTR, OFF_F3 = OFF_F2 + 256 * FSTR, OFF_DBG = 0x1000000;
constexpr unsigned OFF_STATS = 114688, OFF_XNB = 131072, OFF_W2T = OFF_XNB + SL * SD * 2, OFF_ST = OFF_W2T + SF * SD * 2;
constexpr unsigned OFF_H = OFF_ST + SL * SD * 2, WS_USED = OFF_H + SL * SF * 2;
constexpr unsigned CODE_LINES = 256;
static_assert(OFF_F3 + 256 * FSTR <= OFF_STATS, "workspace map");
constexpr unsigned SPIN_CAP = 1u << 16;

constexpr unsigned L_AUX = 131072, L_LNT = L_AUX, L_W2S = L_AUX + 10240, L_CS = L_AUX + 22528, L_B1 = L_CS + 512;
constexpr unsigned L_XY = 65536, XY_ROW = 528, XY_X = 32 * XY_ROW, XY_CH = 28160;
constexpr unsigned L_KP = 0, KP_CH = 4608, L_RC = L_KP + 2 * KP_CH, RC_COPY = 4160, RC_CH = 4 * RC_COPY;
constexpr unsigned L_U = L_RC + 2 * RC_CH, U_CH = 4608, L_RED2 = L_XY, L_R0 = L_XY + 2 * XY_CH;
constexpr unsigned L_U6 = L_XY + 16384, XY_BIAS = 38000;
constexpr unsigned L_SIMG = L_AUX, SIMG_ROW = 144, L_RED = 0;
constexpr unsigned LDS_BYTES = 163840;
static_assert(L_U + 2 * U_CH <= L_XY && L_R0 + 6 * RC_COPY <= LDS_BYTES && L_R0 <= L_AUX, "LDS map");

struct Params {
    const u16 *x, *lar, *aim, *cr, *ci, *ldt, *dsk, *w1, *b1, *w2, *b2;
    u16* out;
    char* ws;
};

template <int... Is, class Fn> DI void sfor_impl(std::integer_sequence<int, Is...>, Fn&& fn) { (fn(std::integral_constant<int, Is>{}), ...); }
template <int N, class Fn> DI void sfor(Fn&& fn) { sfor_impl(std::make_integer_sequence<int, N>{}, fn); }

DI unsigned rfl(unsigned v) { return __builtin_amdgcn_readfirstlane(v); }
DI unsigned sel(bool c, unsigned a, unsigned b) { return c ? a : b; }
DI unsigned opaque(unsigned v) { asm volatile("" : "+v"(v)); return v; }
DI float blo(unsigned d) { return __uint_as_float(d << 16); }
DI float bhi(unsigned d) { return __uint_as_float(d & 0xffff0000u); }
DI unsigned pk(float lo, float hi) { f2 v = {lo, hi}; return __builtin_bit_cast(unsigned, __builtin_convertvector(v, bf2v)); }
DI float rbf(float v) { return blo(pk(v, v)); }
DI float dot2(unsigned a, unsigned b, float c) { return __builtin_amdgcn_fdot2_f32_bf16(__builtin_bit_cast(bf2v, a), __builtin_bit_cast(bf2v, b), c, false); }
template <class T> DI T lds_ld(unsigned a) { return *(const LAS T*)(size_t)a; }
template <class T> DI void lds_st(unsigned a, T v) { *(LAS T*)(size_t)a = v; }
DI v2u lds_tr(unsigned a) { return __builtin_bit_cast(v2u, __builtin_amdgcn_ds_read_tr16_b64_v4i16((LAS s4v*)(size_t)a)); }

template <int CTRL> DI float dpp_add(float v) {
    return v + __builtin_bit_cast(float, __builtin_amdgcn_update_dpp(0, __builtin_bit_cast(int, v), CTRL, 0xf, 0xf, true));
}
DI float pair_sum(float a, float b) {
    const auto r = __builtin_amdgcn_permlane32_swap(__float_as_uint(a), __float_as_uint(b), false, false);
    float v = __uint_as_float(r[0]) + __uint_as_float(r[1]);
    v = dpp_add<0xB1>(v); v = dpp_add<0x4E>(v); v = dpp_add<0x141>(v); v = dpp_add<0x140>(v);
    return dpp_add<0x142>(v);
}
template <int LANE> DI float rdl(float v) { return __builtin_bit_cast(float, __builtin_amdgcn_readlane(__builtin_bit_cast(int, v), LANE)); }
DI float inv_sd(float var) { return __builtin_amdgcn_rcpf(rbf(__builtin_amdgcn_sqrtf(rbf(rbf(fmaxf(var, 0.f)) + 1e-5f)))); }

template <int IMM> DI void dma(unsigned vo, rsrc_t rs, unsigned so) {
    asm volatile("buffer_load_dwordx4 %0, %1, %2 offen offset:%3 lds" :: "v"(vo), "s"(rs), "s"(so), "i"(IMM));
}
DI void set_m0(unsigned v) { asm volatile("s_mov_b32 m0, %0\n\ts_nop 0" :: "s"(v)); }
template <int IMM> DI unsigned ld32(unsigned vo, rsrc_t rs, unsigned so) { unsigned r; asm volatile("buffer_load_dword %0, %1, %2, %3 offen offset:%4" : "=v"(r) : "v"(vo), "s"(rs), "s"(so), "i"(IMM)); return r; }
DI unsigned ld16(unsigned vo, rsrc_t rs, unsigned so) { unsigned r; asm volatile("buffer_load_ushort %0, %1, %2, %3 offen" : "=v"(r) : "v"(vo), "s"(rs), "s"(so)); return r; }
template <int IMM> DI v2u ld64(unsigned vo, rsrc_t rs, unsigned so) { v2u r; asm volatile("buffer_load_dwordx2 %0, %1, %2, %3 offen offset:%4" : "=v"(r) : "v"(vo), "s"(rs), "s"(so), "i"(IMM)); return r; }
template <int IMM> DI v2u ld64c(unsigned vo, rsrc_t rs, unsigned so) { v2u r; asm volatile("buffer_load_dwordx2 %0, %1, %2, %3 offen offset:%4 sc1" : "=v"(r) : "v"(vo), "s"(rs), "s"(so), "i"(IMM)); return r; }
template <int IMM> DI v4u ld128c(unsigned vo, rsrc_t rs, unsigned so) { v4u r; asm volatile("buffer_load_dwordx4 %0, %1, %2, %3 offen offset:%4 sc1" : "=v"(r) : "v"(vo), "s"(rs), "s"(so), "i"(IMM)); return r; }
template <int A, int IMM> DI void ld128a(unsigned vo, rsrc_t rs, unsigned so) {
    asm volatile("buffer_load_dwordx4 a[%c3:%c4], %0, %1, %2 offen offset:%5 sc1" :: "v"(vo), "s"(rs), "s"(so), "i"(A), "i"(A + 3), "i"(IMM));
}
template <int IMM> DI void st128c(v4u v, unsigned vo, rsrc_t rs, unsigned so) { asm volatile("buffer_store_dwordx4 %0, %1, %2, %3 offen offset:%4 sc1\n\ts_nop 1" :: "v"(v), "v"(vo), "s"(rs), "s"(so), "i"(IMM)); }
DI void st64c(v2u v, unsigned vo, rsrc_t rs, unsigned so) { asm volatile("buffer_store_dwordx2 %0, %1, %2, %3 offen sc1" :: "v"(v), "v"(vo), "s"(rs), "s"(so)); }
template <int IMM> DI void st64(v2u v, unsigned vo, rsrc_t rs, unsigned so) { asm volatile("buffer_store_dwordx2 %0, %1, %2, %3 offen offset:%4" :: "v"(v), "v"(vo), "s"(rs), "s"(so), "i"(IMM)); }
template <int IMM> DI void lds_st_tid(unsigned v) { asm volatile("ds_write_addtid_b32 %0 offset:%1" :: "v"(v), "i"(IMM)); }
DI void l2_touch(unsigned vo, rsrc_t rs, unsigned so) { asm volatile("buffer_load_dword a255, %0, %1, %2 offen" :: "v"(vo), "s"(rs), "s"(so)); }
template <class T> DI void tie(T& v) { asm volatile("" : "+v"(v)); }

DI void mfma32(f16v& c, v4u a, v4u b) { asm volatile("v_mfma_f32_32x32x16_bf16 %0, %1, %2, %0" : "+v"(c) : "v"(a), "v"(b)); }
DI void mfma16(f4& c, v4u a, v4u b) { asm volatile("v_mfma_f32_16x16x32_bf16 %0, %1, %2, %0" : "+v"(c) : "v"(a), "v"(b)); }
DI void mfma16z(f4& c, v4u a, v4u b) { asm volatile("v_mfma_f32_16x16x32_bf16 %0, %1, %2, 0" : "=&v"(c) : "v"(a), "v"(b)); }
DI void mfma32z(f16v& c, v4u a, v4u b) { asm volatile("v_mfma_f32_32x32x16_bf16 %0, %1, %2, 0" : "=&v"(c) : "v"(a), "v"(b)); }
template <int A> DI void mfma16a(f4& c, v4u b) { asm volatile("v_mfma_f32_16x16x32_bf16 %0, a[%c2:%c3], %1, %0" : "+v"(c) : "v"(b), "i"(A), "i"(A + 3)); }
template <int A> DI void mfma16az(f4& c, v4u b) { asm volatile("v_mfma_f32_16x16x32_bf16 %0, a[%c2:%c3], %1, 0" : "=&v"(c) : "v"(b), "i"(A), "i"(A + 3)); }
#define MFMA_DRAIN() do { PIN(); asm volatile("s_nop 15" ::: "memory"); PIN(); } while (0)

DI f2 cmul(f2 p, f2 w) {
    f2 r;
    asm volatile("v_pk_mul_f32 %0, %1, %2 op_sel:[0,0] op_sel_hi:[0,1]\n\tv_pk_fma_f32 %0, %1, %2, %0 op_sel:[1,1,0] op_sel_hi:[1,0,1] neg_lo:[0,1,0]" : "=&v"(r) : "v"(p), "v"(w));
    return r;
}
DI f2 cmul_s(f2 p, f2 w) {
    float a, b;
    asm volatile("v_mul_f32 %0, %2, %4\n\tv_mul_f32 %1, %2, %5\n\tv_fma_f32 %0, -%3, %5, %0\n\tv_fma_f32 %1, %3, %4, %1" : "=&v"(a), "=&v"(b) : "v"(p.x), "v"(p.y), "v"(w.x), "v"(w.y));
    return f2{a, b};
}
DI float fmul_s(float a, float b) { float r; asm("v_mul_f32 %0, %1, %2" : "=v"(r) : "v"(a), "v"(b)); return r; }
DI f2 cexp_at(float er, float rev, float p) {
    const float m = __builtin_amdgcn_exp2f(er * p), fr = __builtin_amdgcn_fractf(rev * p);
    const float c = __builtin_amdgcn_cosf(fr) * m;
    float s = __builtin_amdgcn_sinf(fr);
    asm volatile("" : "+v"(s));
    return f2{c, s * m};
}

#define ACLOB_16(n) "a" #n "0", "a" #n "1", "a" #n "2", "a" #n "3", "a" #n "4", "a" #n "5", "a" #n "6", "a" #n "7", "a" #n "8", "a" #n "9"
#define ACLOB_ALL "a0", "a1", "a2", "a3", "a4", "a5", "a6", "a7", "a8", "a9", ACLOB_16(1), ACLOB_16(2), ACLOB_16(3), ACLOB_16(4), ACLOB_16(5), ACLOB_16(6), \
    ACLOB_16(7), ACLOB_16(8), ACLOB_16(9), ACLOB_16(10), ACLOB_16(11), ACLOB_16(12), ACLOB_16(13), ACLOB_16(14), ACLOB_16(15), ACLOB_16(16), ACLOB_16(17), \
    ACLOB_16(18), ACLOB_16(19), ACLOB_16(20), ACLOB_16(21), ACLOB_16(22), ACLOB_16(23), ACLOB_16(24), "a250", "a251", "a252", "a253", "a254", "a255"

struct Ctx {
    rsrc_t rws;
    unsigned e_lo, e_hi;
    unsigned tid, lane, w, w16a, w16b, flag_vo;
    unsigned thr, w2a, w2b, w2l, w2so;
};
#define STAMP(i) do {} while (0)
#define CYC(i) do {} while (0)
template <int N> using ic = std::integral_constant<int, N>;

DI bool flags_match(v2u v, const Ctx& cx) { return __all(v.x == cx.e_lo && v.y == cx.e_hi); }
DI void publish(const Ctx& cx, unsigned so) { st64c(v2u{cx.e_lo, cx.e_hi}, cx.flag_vo, cx.rws, so); }
template <int SLEEP> DI void poll(unsigned vo, const Ctx& cx) {
#pragma unroll 1
    for (unsigned spins = 0;; ++spins) {
        v2u v = ld64c<0>(vo, cx.rws, 0);
        VMWAIT(0); tie(v);
        if (flags_match(v, cx)) break;
        if (spins > SPIN_CAP) { st64c(v2u{1u, 1u}, cx.lane == 0 ? OFF_TMO : OOB, cx.rws, 0); break; }
        __builtin_amdgcn_s_sleep(SLEEP);
    }
}
template <int S, int STRIDE> DI unsigned slot_off(unsigned base, unsigned wrapped, unsigned last, unsigned thr) {
    if constexpr (S == 13) return last;
    unsigned r;
    asm volatile("s_cmp_le_u32 %3, %4\n\ts_cselect_b32 %0, %2, %1\n\ts_add_u32 %0, %0, %5" : "=&s"(r) : "s"(base), "s"(wrapped), "s"(thr), "i"(S), "i"(S * STRIDE) : "scc");
    return r;
}
template <int LO, int HI> DI void w2_prefetch(Ctx& cx) {
    sfor<HI - LO>([&](auto ii) {
        constexpr int f = LO + ii, s = f / 5, c = f % 5;
        if constexpr (c == 0) cx.w2so = slot_off<s, 5120>(cx.w2a, cx.w2b, cx.w2l, cx.thr);
        if constexpr (c < 4) ld128a<4 * f, c * 1024>(cx.w16a, cx.rws, cx.w2so); else ld128a<4 * f, 0>(cx.w16b, cx.rws, cx.w2so);
    });
}

template <class H1, class H2> DI f2 layernorm_rows(const unsigned (&xa)[5], const unsigned (&xb)[5], const Ctx& cx, H1&& hook1, H2&& hook2) {
    constexpr unsigned ONES = 0x3f803f80u;
    float sa = 0.f, sb = 0.f;
    sfor<5>([&](auto i) { sa = dot2(xa[i], ONES, sa); sb = dot2(xb[i], ONES, sb); });
    const float mean = pair_sum(sa, sb) * (1.f / SD), mr = rbf(mean), dm = mean - mr;
    const float mra = rdl<31>(mr), mrb = rdl<63>(mr);
    PIN(); hook1(); PIN();
    unsigned ta[5], tb[5];
    f2 qa = {0.f, 0.f}, qb = {0.f, 0.f};
    sfor<5>([&](auto i) {
        const f2 da = f2{blo(xa[i]), bhi(xa[i])} - mra, db = f2{blo(xb[i]), bhi(xb[i])} - mrb;
        qa = da * da + qa; qb = db * db + qb;
        ta[i] = pk(da.x, da.y); tb[i] = pk(db.x, db.y);
    });
    const float r1 = inv_sd(pair_sum(qa.x + qa.y, qb.x + qb.y) * (1.f / SD) - dm * dm);
    const float ra = rdl<31>(r1), rb = rdl<63>(r1);
    PIN(); hook2(); PIN();
    float s0 = 0.f, s1 = 0.f, s2 = 0.f, s3 = 0.f;
    unsigned o[10];
    sfor<5>([&](auto i) {
        const f2 na = f2{blo(ta[i]), bhi(ta[i])} * ra, nb = f2{blo(tb[i]), bhi(tb[i])} * rb;
        const unsigned ua = pk(na.x, na.y), ub = pk(nb.x, nb.y);
        s0 = dot2(ua, ONES, s0); s1 = dot2(ua, ua, s1); s2 = dot2(ub, ONES, s2); s3 = dot2(ub, ub, s3);
        o[2 * i] = __builtin_amdgcn_perm(ub, ua, 0x05040100u); o[2 * i + 1] = __builtin_amdgcn_perm(ub, ua, 0x07060302u);
    });
    const unsigned la = opaque(L_LNT + cx.w * 2560 + cx.lane * 16), lb = opaque(L_LNT + cx.w * 2560 + 2048 + cx.lane * 8);
    lds_st<v4u>(la, v4u{o[0], o[1], o[2], o[3]});
    lds_st<v4u>(la + 1024, v4u{o[4], o[5], o[6], o[7]});
    lds_st<v2u>(lb, v2u{o[8], o[9]});
    const float m2 = pair_sum(s0, s2) * (1.f / SD), r2 = inv_sd(pair_sum(s1, s3) * (1.f / SD) - m2 * m2);
    return f2{r1 * r2, -(mr * r1 + rbf(m2)) * r2};
}
DI void store_xn(const Ctx& cx, f2 st, unsigned tb) {
    const unsigned a = opaque(L_LNT + cx.tid * 4);
    v4u v[3];
    sfor<3>([&](auto ii) { v[ii] = v4u{lds_ld<unsigned>(a + ii * 1024), lds_ld<unsigned>(a + ii * 1024 + 2560), lds_ld<unsigned>(a + ii * 1024 + 5120), lds_ld<unsigned>(a + ii * 1024 + 7680)}; });
    const unsigned so = OFF_XNB + tb * 10240, vo = cx.tid * 16;
    st128c<0>(v[0], vo, cx.rws, so);
    st128c<0>(v[1], vo, cx.rws, so + 4096);
    st128c<0>(v[2], cx.w < 2 ? vo : OOB, cx.rws, so + 8192);
    st64c(v2u{__float_as_uint(st.x), __float_as_uint(st.y)}, sel((cx.lane & 31) == 31, (cx.lane >> 5) * 8, OOB), cx.rws, OFF_STATS + (8 * tb + 2 * cx.w) * 8);
}

struct Kgen {
    unsigned la, ai, cr, ci, ldt;
    float dt, er, rev;
    f2 w8, w32, v1, v2, v256, y[4], x[8], last;
    unsigned row[53];
};
constexpr int KG_STEPS = 57;
constexpr int kg_row(int I) { return I == 1 ? 32 : I == 4 ? 1 : I == 5 ? 4 : I < 7 ? -1 : I < 27 ? I + 26 : I < 29 ? I - 25 : I < 56 ? I - 24 : -1; }
template <int I, bool BESIDE_MFMA> DI void kg_step(Kgen& k) {
    auto mul = [](f2 a, f2 b) { if constexpr (BESIDE_MFMA) return cmul_s(a, b); else return cmul(a, b); };
    constexpr float LOG2E = 1.44269504088896f, INV2PI = 0.159154943091895f;
    const f2 prev = k.last;
    if constexpr (I == 0) { k.dt = rbf(__builtin_amdgcn_exp2f(blo(k.ldt) * LOG2E)); k.er = rbf(__builtin_amdgcn_exp2f(blo(k.la) * LOG2E)); tie(k.dt); tie(k.er); k.row[0] = 0x3f80u; }
    else if constexpr (I == 1) { k.er = (-k.er * k.dt) * LOG2E; k.rev = (blo(k.ai) * k.dt) * INV2PI; k.x[0] = f2{fmul_s(blo(k.cr), k.dt) * 2.f, fmul_s(blo(k.ci), -2.f * k.dt)}; tie(k.er); tie(k.rev); tie(k.x[0]); k.last = k.x[0]; }
    else if constexpr (I == 2) { k.v1 = cexp_at(k.er, -k.rev, 1.f); tie(k.v1); }
    else if constexpr (I == 3) { k.v256 = cexp_at(k.er, -k.rev, 256.f); tie(k.v256); }
    else if constexpr (I == 4) { k.w8 = cexp_at(k.er, k.rev, 8.f); tie(k.w8); k.last = k.w8; }
    else if constexpr (I == 5) { k.w32 = cexp_at(k.er, k.rev, 32.f); tie(k.w32); k.last = k.w32; }
    else if constexpr (I == 6) { k.v2 = mul(k.v1, k.v1); }
    else if constexpr (I == 7) { k.last = k.x[1] = mul(k.x[0], k.v1); }
    else if constexpr (I < 14) { constexpr int i = I - 6; k.last = k.x[i] = mul(k.x[i - 2], k.v2); }
    else if constexpr (I < 18) { constexpr int i = 2 * (I - 14); k.last = k.x[i] = mul(k.x[i], k.v256); }
    else if constexpr (I < 22) { constexpr int i = 4 * (I & 1); k.last = k.x[i] = mul(k.x[i], k.v256); }
    else if constexpr (I < 27) { k.last = k.x[0] = mul(k.x[0], k.v256); }
    else if constexpr (I == 27) { k.y[0] = k.w32; k.y[1] = k.w8; k.last = k.y[2] = mul(k.w8, k.w8); }
    else if constexpr (I == 28) { k.last = k.y[3] = mul(k.y[2], k.w8); }
    else if constexpr (I < 56) { constexpr int j = (I - 24) & 3; k.last = k.y[j] = mul(k.y[j], k.w32); }
    if constexpr (I > 0 && kg_row(I - 1) >= 0) {
        k.row[kg_row(I - 1)] = pk(prev.x, prev.y); tie(k.row[kg_row(I - 1)]);
        if constexpr (!BESIDE_MFMA) lds_st_tid<XY_BIAS + kg_row(I - 1) * XY_ROW>(k.row[kg_row(I - 1)]);
    }
    if constexpr (!BESIDE_MFMA && I == 2) lds_st_tid<XY_BIAS>(k.row[0]);
}
template <int LO, int HI> DI void kg_write(const Kgen& k) {
    sfor<HI - LO>([&](auto ii) { constexpr int r = LO + ii; if constexpr (r < 53) lds_st_tid<XY_BIAS + r * XY_ROW>(k.row[r]); });
}
DI void kg_set_base(const Ctx& cx) { set_m0(L_XY - XY_BIAS + (cx.w >> 1) * XY_CH + (cx.w & 1) * 256); }

DI void kernel_table(const Ctx& cx) {
    const unsigned cidx = cx.w >> 1, half = cx.w & 1, r = cx.lane & 15, g4 = cx.lane >> 4;
    const unsigned xa = opaque(L_XY + cidx * XY_CH + XY_X + r * XY_ROW + g4 * 16), ya = opaque(xa - XY_X + half * (16 * XY_ROW));
    f4 k0, k1;
    v4u a[8], b0[8], b1[8];
    sfor<8>([&](auto ks) { a[ks] = lds_ld<v4u>(ya + ks * 64); b0[ks] = lds_ld<v4u>(xa + ks * 64); b1[ks] = lds_ld<v4u>(xa + 16 * XY_ROW + ks * 64); PIN(); });
    const unsigned i0 = sel(r < 8, r, sel(r < 12, 240 + 2 * r, 128 * (r & 14) - 1024 + 4 * (r & 1)));
    const unsigned i1 = sel(r <= 4, 1024 + 256 * r, 2049);
    const unsigned kp = L_KP + cidx * KP_CH + 256 * half + 64 * g4, a0 = opaque(kp + 2 * i0), a1 = opaque(kp + 2 * i1);
    PIN();
    sfor<8>([&](auto ks) { if constexpr (ks == 0) { mfma16z(k0, a[ks], b0[ks]); mfma16z(k1, a[ks], b1[ks]); } else { mfma16(k0, a[ks], b0[ks]); mfma16(k1, a[ks], b1[ks]); } });
    PIN(); asm volatile("s_nop 7" : "+v"(k0), "+v"(k1)); PIN();
    sfor<2>([&](auto e2) {
        const unsigned v0 = pk(k0[2 * e2], k0[2 * e2 + 1]), v1 = pk(k1[2 * e2], k1[2 * e2 + 1]);
        lds_st<u16>(a0 + 32 * e2, (u16)v0); lds_st<u16>(a0 + 32 * e2 + 16, (u16)(v0 >> 16));
        lds_st<u16>(a1 + 32 * e2, (u16)v1); lds_st<u16>(a1 + 32 * e2 + 16, (u16)(v1 >> 16));
    });
}
DI void expand_taps(const Ctx& cx, unsigned r0) {
    const unsigned m = 64 * (cx.w & 1) + cx.lane, kp = opaque(L_KP + (cx.w >> 1) * KP_CH + 16 * m);
    const v4u d = lds_ld<v4u>(kp);
    const unsigned d4 = lds_ld<unsigned>(kp + 16), e0 = lds_ld<unsigned>(kp + 2048), e4 = lds_ld<unsigned>(kp + 2048 + 16);
    const bool s1 = m < 32, s3 = cx.w & 1;
    const unsigned sel0 = s1 ? 0x01000302u : 0x01000100u, sel1 = s1 ? 0x01000302u : 0x01000504u, sel3 = s3 ? 0x05040504u : sel1;
    v4u o0, o1;
    o0.w = __builtin_amdgcn_perm(d.x, d.x, sel0);
    o0.z = __builtin_amdgcn_perm(d.z, s3 ? d.x : d.y, sel1);
    o0.y = __builtin_amdgcn_perm(d.z, d.z, sel0);
    o0.x = __builtin_amdgcn_perm(d4, d.w, sel3);
    o1.w = __builtin_amdgcn_perm(e0, e0, 0x01000100u);
    o1.z = o1.w;
    o1.y = __builtin_amdgcn_perm(e4, e0, (m & 1) ? 0x05040504u : 0x01000504u);
    o1.x = __builtin_amdgcn_perm(e4, e4, 0x01000100u);
    const unsigned a = opaque(r0 + 16 * (127 - m));
    lds_st<v4u>(a + 2048, o0);
    lds_st<v4u>(a, o1);
    lds_st<v4u>(r0 + 4096 + 16 * (m & 1), v4u{0, 0, 0, 0});
}
template <bool COPY0> DI void shift_taps(const Ctx& cx, unsigned r0) {
    const unsigned m = 64 * (cx.w & 1) + cx.lane, src = opaque(r0 + 16 * m), dst = opaque(L_RC + (cx.w >> 1) * RC_CH + 16 * m);
    sfor<2>([&](auto it) {
        const v4u d = lds_ld<v4u>(src + it * 2048);
        const v2u n = lds_ld<v2u>(src + it * 2048 + 16);
        const unsigned q0 = __builtin_amdgcn_alignbyte(d.y, d.x, 2), q1 = __builtin_amdgcn_alignbyte(d.z, d.y, 2), q2 = __builtin_amdgcn_alignbyte(d.w, d.z, 2);
        const unsigned q3 = __builtin_amdgcn_alignbyte(n.x, d.w, 2), q4 = __builtin_amdgcn_alignbyte(n.y, n.x, 2);
        if constexpr (COPY0) lds_st<v4u>(dst + it * 2048, d);
        lds_st<v4u>(dst + it * 2048 + RC_COPY, v4u{q0, q1, q2, q3});
        lds_st<v4u>(dst + it * 2048 + 2 * RC_COPY, v4u{d.y, d.z, d.w, n.x});
        lds_st<v4u>(dst + it * 2048 + 3 * RC_COPY, v4u{q1, q2, q3, q4});
    });
    lds_st<v2u>(L_RC + ((cx.lane & 31) >> 2) * RC_COPY + 4096 + (cx.lane & 3) * 8, v2u{0, 0});
}
template <class HOOK, class POST> DI void conv_pair(Ctx& cx, unsigned ch, unsigned ubase, float dskip, HOOK&& hook, POST&& post) {
    const unsigned cidx = cx.w >> 1, half = cx.w & 1, r = cx.lane & 15, g4 = cx.lane >> 4;
    f4 ca[8];
    {
        const unsigned aa0 = L_RC + cidx * RC_CH + ((~r) & 3) * RC_COPY + 2 * (2028 - 4 * (r >> 2) + 8 * g4) - 3584 - 256 * half - 192;
        const unsigned aa = opaque(aa0), ab = opaque(aa0 + 8), bb = opaque(ubase + 448 + 32 * r + 16 * g4 - 256 * half - 192);
        v2u al[2][8], ah[2][8];
        v4u fb[2][8];
        auto rd = [&](auto d_, auto i_) {
            constexpr int d = d_, i = i_, e = i / 3, o = (3 - d) * 64;
            if constexpr (i % 3 == 0) al[d & 1][e] = lds_ld<v2u>(aa + (7 - e) * 512 + o);
            else if constexpr (i % 3 == 1) ah[d & 1][e] = lds_ld<v2u>(ab + (7 - e) * 512 + o);
            else fb[d & 1][e] = lds_ld<v4u>(bb + e * 512 + o);
            PIN();
        };
        sfor<12>([&](auto i) { rd(ic<0>{}, i); });
        sfor<4>([&](auto d_) {
            constexpr int d = d_;
            sfor<8>([&](auto n_) {
                constexpr int n = n_;
                sfor<n + 1>([&](auto e_) {
                    constexpr int e = e_, idx = n * (n + 1) / 2 + e;
                    const v4u a = {al[d & 1][e].x, al[d & 1][e].y, ah[d & 1][e].x, ah[d & 1][e].y};
                    if constexpr (d == 0 && e == 0) mfma16z(ca[n], a, fb[d & 1][n - e]); else mfma16(ca[n], a, fb[d & 1][n - e]);
                    PIN();
                    if constexpr (d == 0) { if constexpr (idx < 6) { rd(ic<0>{}, ic<12 + 2 * idx>{}); rd(ic<0>{}, ic<13 + 2 * idx>{}); } else if constexpr (idx >= 12) rd(ic<1>{}, ic<idx - 12>{}); }
                    else if constexpr (d < 3 && idx % 3 != 2) rd(ic<d + 1>{}, ic<idx - idx / 3>{});
                });
            });
            hook(d_);
        });
    }
    STAMP(7);
    post();
    PIN(); asm volatile("s_nop 7"); PIN();
    sfor<8>([&](auto n) { tie(ca[n]); });
    const unsigned ra = opaque(L_RED2 + cx.lane * 16 + cx.w * 4096), rb = opaque(L_RED2 + cx.lane * 16 + (cx.w ^ 1) * 4096);
    if (half == 0) { sfor<4>([&](auto i) { lds_st<f4>(ra + i * 1024, ca[4 + i]); }); }
    else { sfor<4>([&](auto i) { lds_st<f4>(ra + i * 1024, ca[i]); ca[i] = ca[4 + i]; }); }
    const unsigned uaddr = opaque(ubase + 480 + 2 * (1024 * half + 16 * r + 4 * g4));
    f4 uf[4];
    sfor<4>([&](auto i) { const v2u u = lds_ld<v2u>(uaddr + i * 512); uf[i] = f4{blo(u.x), bhi(u.x), blo(u.y), bhi(u.y)} * dskip + ca[i]; });
    BARRIER();
    v2u pkd[4];
    sfor<4>([&](auto i) { const f4 y = uf[i] + lds_ld<f4>(rb + i * 1024); pkd[i] = v2u{pk(y[0], y[1]), pk(y[2], y[3])}; });
    const unsigned vo = 2 * (256 * (g4 & 1) + 16 * r + 8 * (g4 >> 1)), so = OFF_ST + ch * 4096 + 2048 * half;
    sfor<2>([&](auto i2) {
        const auto sx = __builtin_amdgcn_permlane16_swap(pkd[2 * i2].x, pkd[2 * i2 + 1].x, false, false);
        const auto sy = __builtin_amdgcn_permlane16_swap(pkd[2 * i2].y, pkd[2 * i2 + 1].y, false, false);
        st128c<i2 * 1024>(v4u{sx[0], sy[0], sx[1], sy[1]}, vo, cx.rws, so);
    });
}

__global__ void __launch_bounds__(256, 1) s4d_mixer_kernel(Params p) {
    extern __shared__ __attribute__((aligned(16))) char smem[];
    const unsigned tid = threadIdx.x, lane = tid & 63, w = rfl(tid >> 6);
    const unsigned b = blockIdx.x, g = b & 7, q = b >> 3;
    {
        u64 pc;
        asm volatile("s_getpc_b64 %0" : "=s"(pc) :: ACLOB_ALL);
        const unsigned line = (q & 7) * (CODE_LINES / 8) + w * (CODE_LINES / 32) + (lane & (CODE_LINES / 32 - 1));
        asm volatile("global_load_dword a255, %0, %1" :: "v"(line * 128u), "s"(pc & ~127ull));
    }
    asm volatile("" :: "v"((unsigned)(size_t)(LAS char*)smem));
    Ctx cx;
    STAMP(0);
    u64 pr0, pr1 = 0;
    asm volatile("s_memrealtime %0\n\ts_waitcnt lgkmcnt(0)" : "=s"(pr0));
    const unsigned tb = 32 * g + q;
    const rsrc_t rx = __builtin_amdgcn_make_buffer_rsrc((void*)p.x, 0, SL * SD * 2, 0x00020000);
    const rsrc_t rw1 = __builtin_amdgcn_make_buffer_rsrc((void*)p.w1, 0, SD * SF * 2, 0x00020000);
    unsigned xa[5], xb[5];
    v2u xa0, xa1, xb0, xb1;
    {
        const unsigned so = (8 * tb + 2 * w) * (SD * 2);
        asm volatile("s_nop 4");
        xa0 = ld64<0>(lane * 8, rx, so); xa1 = ld64<512>(lane * 8, rx, so); xa[4] = ld32<1024>(lane * 4, rx, so);
        xb0 = ld64<1280>(lane * 8, rx, so); xb1 = ld64<1280 + 512>(lane * 8, rx, so); xb[4] = ld32<1280 + 1024>(lane * 4, rx, so);
    }
    const unsigned tx = (512 * (g >> 1) + 16 * q + (lane & 15)) * 1280 + (lane >> 4) * 128;
    unsigned tw;
    {
        const unsigned st = lane >= 28, i = lane - 28 * st, hi = i >= 14;
        tw = sel(lane < 56, (64 * st + 2 * q + hi) * (SF * 2) + (g & 1) * 1792 + (i - 14 * hi) * 128, OOB);
    }
    if (w == 1) l2_touch(tx, rx, 0); else l2_touch(tw, rw1, sel(w == 0, 0, 2 * w - 2) * (64 * SF * 2));
    auto touch2 = [&]() {
        if (w == 0 || w == 3) l2_touch(sel(w == 0 || lane < 32, tx, OOB), rx, sel(w == 0, 4, 8) * 128);
        else l2_touch(tw, rw1, (4 + 2 * w) * (64 * SF * 2));
    };
    cx.rws = __builtin_amdgcn_make_buffer_rsrc((void*)p.ws, 0, WS_USED, 0x00020000);
    cx.tid = tid; cx.lane = lane; cx.w = w;
    v2u wv = ld64c<0>(opaque(OFF_W), cx.rws, 0);
    const bool spare = q >= 28;
    const unsigned ch0 = 80 * g + sel(spare, 6 * q - 112, 2 * q), cidx = w >> 1;
    const rsrc_t rla = __builtin_amdgcn_make_buffer_rsrc((void*)p.lar, 0, SD * 256, 0x00020000);
    const rsrc_t rai = __builtin_amdgcn_make_buffer_rsrc((void*)p.aim, 0, SD * 256, 0x00020000);
    const rsrc_t rcr = __builtin_amdgcn_make_buffer_rsrc((void*)p.cr, 0, SD * 256, 0x00020000);
    const rsrc_t rci = __builtin_amdgcn_make_buffer_rsrc((void*)p.ci, 0, SD * 256, 0x00020000);
    const unsigned pvo = w * 128 + lane * 2;
    Kgen kg;
    kg.last = f2{0.f, 0.f};
    kg.la = ld16(pvo, rla, ch0 * 256); kg.ai = ld16(pvo, rai, ch0 * 256); kg.cr = ld16(pvo, rcr, ch0 * 256); kg.ci = ld16(pvo, rci, ch0 * 256);
    const unsigned rt = 4 * g + (q >> 3), ct = q & 7;
    const unsigned nk = 14 - (w >> 1), rot = (ct * 13) >> 3, k0s = 14 * w - (w == 3 ? 1 : 0) + rot, klast = k0s + 13 - (rot ? 14 : 0);
    auto late_setup = [&]() {
        sfor<2>([&](auto i) { tie(xa0); tie(xa1); tie(xb0); tie(xb1); });
        tie(xa[4]); tie(xb[4]);
        xa[0] = xa0.x; xa[1] = xa0.y; xa[2] = xa1.x; xa[3] = xa1.y; xb[0] = xb0.x; xb[1] = xb0.y; xb[2] = xb1.x; xb[3] = xb1.y;
    };
    auto take_epoch = [&]() {
        tie(wv); tie(kg.la); tie(kg.ai); tie(kg.cr); tie(kg.ci);
        const u64 e = ((u64)rfl(wv.y) << 32 | rfl(wv.x)) + 0x9E3779B97F4A7C15ull;
        cx.e_lo = (unsigned)e; cx.e_hi = (unsigned)(e >> 32);
        cx.flag_vo = tid == 0 ? 0u : OOB;
        cx.w16a = lane * 16; cx.w16b = lane * 16 + 4096;
        cx.thr = nk - rot; cx.w2a = OFF_W2T + (ct * NKS + k0s) * 5120; cx.w2b = cx.w2a - nk * 5120;
        cx.w2l = sel(w < 2, OFF_W2T + (ct * NKS + klast) * 5120, WS_USED); cx.w2so = 0;
    };
    auto half16 = [&](const u16* base, unsigned ch) { const unsigned v = *(const CAS unsigned*)(base + ch); return (v >> (16 * cidx)) & 0xffffu; };

    const unsigned ti = 4 * (g >> 1) + q / 7, tj = 7 * (g & 1) + q % 7, wf = w >> 1, wt = w & 1;
    unsigned gx[4], gw[4];
    sfor<4>([&](auto ii) {
        gx[ii] = (32 * w + 8 * ii + (lane >> 3)) * 1280 + 16 * ((lane & 7) ^ (4 * (ii & 1) + (lane >> 4))) - ii * 1024;
        gw[ii] = (16 * w + 4 * ii + (lane >> 4)) * (SF * 2) + 16 * ((lane & 15) ^ (((lane >> 4) << 2) | ii)) - ii * 1024;
    });
    const unsigned sx0 = ti * (128 * 1280), sw0 = tj * 256;
    auto piece = [&](auto s_, auto pi_) {
        constexpr int s = s_, pi = pi_, ii = pi & 3;
        if constexpr (ii == 0) set_m0((s & 3) * 32768 + (pi >> 2) * 16384 + w * 4096);
        if constexpr (pi < 4) dma<ii * 1024>(gx[ii], rx, sx0 + s * 128); else dma<ii * 1024>(gw[ii], rw1, sw0 + s * (64 * SF * 2));
    };
    auto stage = [&](auto s_) { sfor<8>([&](auto pi) { piece(s_, pi); }); };
    const unsigned sid = 27 * g + sel(q < 26, q, 26), sks = sid >> 2, sqq = sid & 3;
    const rsrc_t rb1 = __builtin_amdgcn_make_buffer_rsrc((void*)p.b1, 0, SF * 2, 0x00020000);
    unsigned b1v = ld16((tid & 127) * 2, rb1, tj * 256);
    if (!spare) {
        const rsrc_t rw2 = __builtin_amdgcn_make_buffer_rsrc((void*)p.w2, 0, SD * SF * 2, 0x00020000);
        set_m0(L_W2S + w * 3072);
        sfor<3>([&](auto ii) {
            const unsigned P = 64 * (3 * w + ii) + lane, row = (P * 3277) >> 16, vo = row * (1280 - 320) + P * 16 - ii * 1024;
            dma<ii * 1024>(sel(ii == 0 || w < 3, vo, OOB), rw2, sks * (32 * 1280) + sqq * 320);
        });
        VMWAIT(10);
    } else VMWAIT(7);
    late_setup();
    STAMP(1);
    const f2 st = layernorm_rows(xa, xb, cx, [&]() { if (!spare) stage(ic<0>{}); }, [&]() { touch2(); if (!spare) stage(ic<1>{}); });
    if (!spare) VMWAIT(17); else VMWAIT(1);
    take_epoch(); tie(b1v);
    BARRIER();
    store_xn(cx, st, tb);
    unsigned ubase;

    if (!spare) {
        {
            const unsigned g4 = lane >> 4, a0 = opaque(L_W2S + (16 * (g4 >> 1) + 4 * (g4 & 1) + ((lane & 15) >> 2)) * 320 + 8 * (lane & 3) + w * 96);
            v4u fr[3];
            sfor<3>([&](auto ii) { const v2u lo = lds_tr(a0 + ii * 32), hi = lds_tr(a0 + ii * 32 + 8 * 320); fr[ii] = v4u{lo.x, lo.y, hi.x, hi.y}; });
            sfor<3>([&](auto ii) {
                const unsigned fi = 3 * w + ii;
                st128c<0>(fr[ii], cx.w16a, cx.rws, sel(fi < 10, OFF_W2T + (2 * sqq * NKS + sks) * 5120 + fi * 1024 + sel(fi >= 5, (NKS - 1) * 5120, 0), WS_USED));
            });
        }
        stage(ic<2>{});
        kg.ldt = half16(p.ldt, ch0);
        STAMP(2);
        unsigned ax[2][4], aw[2][4];
        {
            const unsigned row = 64 * wt + (lane & 31), hh = lane >> 5, blk = (lane >> 4) & 1, qr = (lane & 15) >> 2, pp = lane & 3;
            sfor<4>([&](auto ks) { ax[0][ks] = opaque(row * 128 + 16 * ((2 * ks + hh) ^ ((row >> 1) & 7))); ax[1][ks] = opaque(ax[0][ks] + 65536); });
            sfor<4>([&](auto i) {
                constexpr int mf = i >> 1, t = i & 1;
                const unsigned r = 8 * hh + 4 * t + qr, chn = 4 * (2 * wf + (mf ^ wt)) + 2 * blk + (pp >> 1);
                aw[0][i] = opaque(16384 + 256 * r + 16 * (chn ^ ((qr << 2) | ((2 * hh + t) & 3))) + 8 * (pp & 1)); aw[1][i] = opaque(aw[0][i] + 65536);
            });
        }
        const unsigned onesel = ((lane ^ (lane >> 4)) & 1) ? 0u : 0x3f803f80u;
        v4u ones = {onesel, onesel, onesel, onesel};
        tie(ones);
        constexpr int W7 = 4, W8 = 4, W9 = 4, WG0 = 12, WG1 = 28;
        f16v acc[2][2];
        f4 csum;
        v2u fa[2][4], plv, st0, st1;
        v4u fb[2][2], ub0, ub1;
        auto rd = [&](auto n_, auto i_) {
            constexpr int n = n_, i = i_, s = n >> 2, ks = n & 3, bf = n & 1, hi = (s & 3) >> 1;
            constexpr unsigned so = (s & 1) * 32768;
            if constexpr (n < 40) {
                if constexpr (i == 0) fb[bf][0] = lds_ld<v4u>(ax[hi][ks] + so);
                else if constexpr (i == 3) fb[bf][1] = lds_ld<v4u>(ax[hi][ks] + so + 4096);
                else fa[bf][i < 3 ? i - 1 : i - 2] = lds_tr(aw[hi][i < 3 ? i - 1 : i - 2] + so + ks * 4096);
            }
            PIN();
        };
        VMWAIT(24);
        BARRIER();
        sfor<6>([&](auto i) { rd(ic<0>{}, i); });
        piece(ic<3>{}, ic<0>{}); piece(ic<3>{}, ic<1>{});
        sfor<40>([&](auto n0_) {
            constexpr int n = n0_ + 1, s = n >> 2, ks = n & 3, bf = (n - 1) & 1;
            constexpr bool first = n == 1;
            if constexpr (ks == 0 && n < 40) {
                constexpr int WAITS[10] = {0, 23, 16, 17, 17, 16, 17, 16, 8 + 2 + W7, W7 + W8};
                VMWAIT(WAITS[s]);
                if constexpr (s == 7) { tie(plv); if (!flags_match(plv, cx)) poll<2>(OFF_F1 + tid * FSTR, cx); }
                BARRIER();
                if constexpr (s == 2) publish(cx, OFF_F1 + tb * FSTR);
                if constexpr (s == 5) plv = ld64c<0>(tid * FSTR, cx.rws, OFF_F1);
                if constexpr (s == 7) {
                    const unsigned vo = (64 * wt + (lane & 31)) * 8;
                    st0 = ld64c<0>(vo, cx.rws, OFF_STATS + ti * 1024); st1 = ld64c<256>(vo, cx.rws, OFF_STATS + ti * 1024);
                }
                if constexpr (s == 8) kg_set_base(cx);
                PIN();
            }
            auto vm = [&](auto m_) {
                constexpr int pi = 2 * ks + m_;
                if constexpr (s <= 6) piece(ic<s + 3>{}, ic<pi>{});
                else if constexpr (s <= 9 && m_ == 0) w2_prefetch<(s - 7) * 4 + ks, (s - 7) * 4 + ks + 1>(cx);
                PIN();
            };
            auto side = [&](auto m_) {
                constexpr int m = m_;
                if constexpr (n <= 6) { if constexpr (m == 0) kg_step<n - 1, true>(kg); }
                else if constexpr (n < 32) { kg_step<6 + 2 * (n - 7) + m, true>(kg); if constexpr (n == 31 && m == 1) kg_step<56, true>(kg); }
                else if constexpr (n < 40) kg_write<(n - 32) * 7 + 4 * m, (n - 32) * 7 + 4 + 3 * m>(kg);
                PIN();
            };
            const v4u a0 = {fa[bf][0].x, fa[bf][0].y, fa[bf][1].x, fa[bf][1].y}, a1 = {fa[bf][2].x, fa[bf][2].y, fa[bf][3].x, fa[bf][3].y};
            if constexpr (first) mfma32z(acc[0][0], a0, fb[bf][0]); else mfma32(acc[0][0], a0, fb[bf][0]);
            PIN(); rd(ic<n>{}, ic<0>{}); rd(ic<n>{}, ic<1>{}); rd(ic<n>{}, ic<2>{}); rd(ic<n>{}, ic<3>{});
            if constexpr (first) mfma32z(acc[0][1], a0, fb[bf][1]); else mfma32(acc[0][1], a0, fb[bf][1]);
            PIN(); rd(ic<n>{}, ic<4>{}); rd(ic<n>{}, ic<5>{}); vm(ic<0>{});
            if constexpr (first) mfma16z(csum, a0, ones); else mfma16(csum, a0, ones);
            if constexpr (first) mfma32z(acc[1][0], a1, fb[bf][0]); else mfma32(acc[1][0], a1, fb[bf][0]);
            PIN(); side(ic<0>{}); vm(ic<1>{});
            if constexpr (first) mfma32z(acc[1][1], a1, fb[bf][1]); else mfma32(acc[1][1], a1, fb[bf][1]);
            PIN(); side(ic<1>{});
        });
        asm volatile("s_memrealtime %0\n\ts_waitcnt lgkmcnt(0)" : "=s"(pr1));
        tie(csum);
        lds_st<f4>(L_CS + (64 * wf + 32 * wt + 16 * (lane & 1) + 4 * (lane >> 4)) * 4, csum);
        if (w < 2) lds_st<float>(L_B1 + tid * 4, blo(b1v));
        tie(st0); tie(st1);
        {
            const unsigned uvo = (tid >> 1) * 10240 + (tid & 1) * 16;
            ub0 = ld128c<0>(uvo, cx.rws, OFF_XNB + ch0 * 16); ub1 = ld128c<0>(uvo, cx.rws, OFF_XNB + 128 * 10240 + ch0 * 16);
        }
        BARRIER();
        MFMA_DRAIN();
        {
            const unsigned hh = lane >> 5;
            const unsigned hvo = sel(tj == 13 && wf == 1, OOB, ((lane >> 4) & 1) * (NKS * 1024) + (hh * 16 + (lane & 15)) * 16);
            const unsigned hso = OFF_H + ((8 * ti + 4 * wt) * NKS + 4 * tj + 2 * wf) * 1024;
            const unsigned cam[2] = {opaque(L_CS + (64 * wf + 32 * wt + 4 * hh) * 4), opaque(L_CS + (64 * wf + 32 * (wt ^ 1) + 4 * hh) * 4)};
            constexpr float C1 = -2.302208198f, C2 = -0.1029432397f;
            sfor<2>([&](auto mf_) {
                constexpr int mf = mf_;
                f4 cs[4], bb[4];
                sfor<4>([&](auto ga) { cs[ga] = lds_ld<f4>(cam[mf] + 32 * ga); bb[ga] = lds_ld<f4>(cam[mf] + 512 + 32 * ga); });
                sfor<2>([&](auto nt_) {
                    constexpr int nt = nt_;
                    tie(acc[mf][nt]);
                    const v2u sv = nt ? st1 : st0;
                    const float sa = __uint_as_float(sv.x), sb = __uint_as_float(sv.y);
                    sfor<2>([&](auto chk) {
                        f2 z[4], e[4];
                        sfor<4>([&](auto i) {
                            constexpr int ga = 2 * chk + (i >> 1), e2 = 2 * (i & 1);
                            z[i] = f2{acc[mf][nt][4 * ga + e2], acc[mf][nt][4 * ga + e2 + 1]} * sa + (f2{cs[ga][e2], cs[ga][e2 + 1]} * sb + f2{bb[ga][e2], bb[ga][e2 + 1]});
                        });
                        sfor<4>([&](auto i) { e[i] = z[i] * (z[i] * z[i] * C2 + C1); });
                        sfor<4>([&](auto i) { e[i] = f2{__builtin_amdgcn_exp2f(e[i].x), __builtin_amdgcn_exp2f(e[i].y)}; });
                        sfor<4>([&](auto i) { e[i] = e[i] + 1.f; });
                        sfor<4>([&](auto i) { e[i] = f2{__builtin_amdgcn_rcpf(e[i].x), __builtin_amdgcn_rcpf(e[i].y)}; });
                        unsigned o[4];
                        sfor<4>([&](auto i) { const f2 r = z[i] * e[i]; o[i] = pk(r.x, r.y); });
                        constexpr int idx = 4 * mf + 2 * nt + chk, f0 = WG0 + (WG1 - WG0) * idx / 8, f1 = WG0 + (WG1 - WG0) * (idx + 1) / 8;
                        w2_prefetch<f0, f1>(cx);
                        st128c<chk * 512>(v4u{o[0], o[1], o[2], o[3]}, hvo, cx.rws, hso + (2 * nt * NKS + (mf ^ wt)) * 1024);
                        PIN();
                    });
                });
            });
        }
        STAMP(4);
        w2_prefetch<WG1, WG1 + 3>(cx);
        kernel_table(cx);
        w2_prefetch<WG1 + 3, WG1 + 6>(cx);
        BARRIER();
        const unsigned r0 = L_RC + cidx * RC_CH;
        expand_taps(cx, r0);
        VMWAIT(6);
        tie(ub0); tie(ub1);
        {
            const unsigned ua = opaque(L_U + 480 + (tid & 1) * U_CH + (tid >> 1) * 16);
            lds_st<v4u>(ua, ub0); lds_st<v4u>(ua + 2048, ub1);
            lds_st<v2u>(L_U + (tid >> 7) * U_CH + sel((tid & 127) < 60, tid & 127, 59) * 8, v2u{0, 0});
        }
        BARRIER();
        publish(cx, OFF_F2 + (16 * ti + tj) * FSTR);
        STAMP(5);
        w2_prefetch<WG1 + 6, 40>(cx);
        shift_taps<false>(cx, r0);
        ubase = L_U + cidx * U_CH;
    } else {
        STAMP(2);
        kg_set_base(cx);
#pragma unroll 1
        for (unsigned round = 0; round < 3; ++round) {
            CYC(0);
            kg.ldt = half16(p.ldt, ch0 + 2 * round);
            sfor<2>([&](auto i) { kg_step<i, false>(kg); });
            CYC(1);
            PIN();
            const unsigned cn = (ch0 + 2 * round + 2) * 256;
            kg.la = ld16(pvo, rla, cn); kg.ai = ld16(pvo, rai, cn); kg.cr = ld16(pvo, rcr, cn); kg.ci = ld16(pvo, rci, cn);
            PIN();
            sfor<4>([&](auto i) { kg_step<i + 2, false>(kg); });
            CYC(2);
            sfor<KG_STEPS - 6>([&](auto i) { kg_step<i + 6, false>(kg); });
            CYC(3);
            VMWAIT(0);
            CYC(4);
            tie(kg.la); tie(kg.ai); tie(kg.cr); tie(kg.ci);
            BARRIER();
            CYC(5);
            if (round == 0) publish(cx, OFF_F1 + tb * FSTR);
            kernel_table(cx);
            CYC(6);
            BARRIER();
            CYC(7);
            expand_taps(cx, L_R0 + (2 * round + cidx) * RC_COPY);
            CYC(8);
        }
        STAMP(3);
        poll<4>(OFF_F1 + tid * FSTR, cx);
        BARRIER();
        STAMP(4);
        {
            v4u ub[6];
            unsigned ua[6];
            sfor<6>([&](auto i) {
                const unsigned P = tid + 256 * i, blk = (P * 10923) >> 16, c = P - 6 * blk;
                ub[i] = ld128c<0>(blk * 10240 + c * 16, cx.rws, OFF_XNB + ch0 * 16);
                ua[i] = L_U6 + 480 + c * U_CH + blk * 16;
            });
            w2_prefetch<0, 4>(cx);
            shift_taps<true>(cx, L_R0 + cidx * RC_COPY);
            sfor<3>([&](auto i) { lds_st<v2u>(L_U6 + (2 * i + (tid >> 7)) * U_CH + sel((tid & 127) < 60, tid & 127, 59) * 8, v2u{0, 0}); });
            VMWAIT(4);
            sfor<6>([&](auto i) { tie(ub[i]); lds_st<v4u>(ua[i], ub[i]); });
        }
        STAMP(5);
#pragma unroll 1
        for (unsigned round = 0; round < 2; ++round) {
            const float dsk = blo(half16(p.dsk, ch0 + 2 * round));
            BARRIER();
            conv_pair(cx, ch0 + 2 * round + cidx, L_U6 + (2 * round + cidx) * U_CH, dsk, [&](auto d) {
                if (round == 0) w2_prefetch<4 + 9 * d / 2, 4 + 9 * (d + 1) / 2>(cx); else w2_prefetch<22 + 9 * d / 2, 22 + 9 * (d + 1) / 2>(cx);
            }, [&]() {});
            shift_taps<true>(cx, L_R0 + (2 * round + 2 + cidx) * RC_COPY);
        }
        ubase = L_U6 + (4 + cidx) * U_CH;
    }
    cx.w2so = rfl(cx.w2so); cx.w2l = rfl(cx.w2l); cx.w2a = rfl(cx.w2a); cx.w2b = rfl(cx.w2b); cx.thr = rfl(cx.thr); cx.e_lo = rfl(cx.e_lo); cx.e_hi = rfl(cx.e_hi);
    ubase = rfl(ubase);
    const unsigned chl = rfl(ch0 + sel(spare, 4, 0));
    const float dskip = blo(half16(p.dsk, chl));
    BARRIER();
    STAMP(6);

    constexpr int WIN = 4;
    v4u wx[6], hf[WIN][4];
    unsigned hv[4], hso;
    sfor<4>([&](auto t) { hv[t] = (4 * rt + ((t + w) & 3)) * (NKS * 1024) + lane * 16; });
    const unsigned hsa = OFF_H + k0s * 1024, hsb = hsa - nk * 1024, hsl = sel(w < 2, OFF_H + klast * 1024, WS_USED);
    auto ldh = [&](auto s_, auto t_) {
        constexpr int s = s_, t = t_;
        if constexpr (s < 14) {
            if constexpr (t == 0) hso = slot_off<s, 1024>(hsa, hsb, hsl, cx.thr);
            hf[s % WIN][t] = ld128c<0>(hv[t], cx.rws, hso);
        }
        PIN();
    };
    {
        const unsigned f2vo = OFF_F2 + (16 * (rt >> 1) + sel(lane < 14, lane, 0)) * FSTR;
        v2u plv2;
        conv_pair(cx, chl + cidx, ubase, dskip,
            [&](auto d) {
                if constexpr (d == 2) plv2 = ld64c<0>(f2vo, cx.rws, 0);
                if constexpr (d < 3) w2_prefetch<40 + 8 * d, 48 + 8 * d>(cx);
            },
            [&]() {
                VMWAIT(8);
                tie(plv2);
                if (!flags_match(plv2, cx)) poll<1>(f2vo, cx);
                sfor<WIN - 1>([&](auto s) { sfor<4>([&](auto t) { ldh(s, t); }); });
                wx[0] = ld128c<0>(cx.w16b, cx.rws, cx.w2so);
                sfor<4>([&](auto c) { wx[1 + c] = ld128c<c * 1024>(cx.w16a, cx.rws, cx.w2l); });
                wx[5] = ld128c<0>(cx.w16b, cx.rws, cx.w2l);
                STAMP(8);
            });
    }
    STAMP(9);
    f4 acc2[5][4];
    v2u xo[5], bo[5], plv3;
    v4u sv[3];
    const unsigned xvo = (16 * w + (lane & 15)) * (SD * 2) + 8 * (lane >> 4), xso = 64 * rt * (SD * 2) + 160 * ct;
    {
        const unsigned f3so = rfl(OFF_F3 + tb * FSTR);
        const rsrc_t rb2 = __builtin_amdgcn_make_buffer_rsrc((void*)p.b2, 0, SD * 2, 0x00020000);
        const unsigned svo = (tid >> 3) * 4096 + (tid & 7) * 16;
        sfor<14>([&](auto s_) {
            constexpr int s = s_;
            constexpr int later = (s + WIN - 2 < 14 ? WIN - 2 : 13 - s) * 4;
            constexpr int extra = s <= 2 ? 8 : (s == 4 || s == 5 || s == 7 || s == 8) ? 1 : (s == 10 || s == 11) ? 13 : 0;
            VMWAIT(later + extra);
            sfor<4>([&](auto t) { tie(hf[s % WIN][t]); });
            if constexpr (s == 3) { sfor<6>([&](auto i) { tie(wx[i]); }); BARRIER(); publish(cx, f3so); }
            if constexpr (s == 6) plv3 = ld64c<0>((lane & 31) * FSTR, cx.rws, OFF_F3 + ct * (32 * FSTR));
            if constexpr (s == 9) {
                tie(plv3);
                if (!flags_match(plv3, cx)) poll<1>(OFF_F3 + ct * (32 * FSTR) + (lane & 31) * FSTR, cx);
                const unsigned so = OFF_ST + 80 * ct * 4096 + 128 * rt;
                sv[0] = ld128c<0>(svo, cx.rws, so); sv[1] = ld128c<0>(svo, cx.rws, so + 32 * 4096); sv[2] = ld128c<0>(svo, cx.rws, sel(w < 2, so + 64 * 4096, WS_USED));
                sfor<5>([&](auto c) { xo[c] = ld64<c * 32>(xvo, rx, xso); });
                sfor<5>([&](auto c) { bo[c] = ld64<c * 32>(8 * (lane >> 4), rb2, 160 * ct); });
            }
            PIN();
            sfor<5>([&](auto c_) {
                constexpr int c = c_, f = 5 * s + c;
                sfor<4>([&](auto t) {
                    if constexpr (f >= 64) mfma16(acc2[c][t], wx[f - 64], hf[s % WIN][t]);
                    else if constexpr (s == 0) mfma16az<4 * f>(acc2[c][t], hf[s % WIN][t]);
                    else mfma16a<4 * f>(acc2[c][t], hf[s % WIN][t]);
                });
                PIN();
                if constexpr (c < 4) ldh(ic<s + WIN - 1>{}, c_);
            });
            if constexpr (s == 12) {
                sfor<3>([&](auto ii) { tie(sv[ii]); });
                const unsigned sa = opaque(L_SIMG + (tid >> 3) * SIMG_ROW + (tid & 7) * 16);
                lds_st<v4u>(sa, sv[0]); lds_st<v4u>(sa + 32 * SIMG_ROW, sv[1]); lds_st<v4u>(sa + 64 * SIMG_ROW, sv[2]);
                PIN();
            }
        });
    }
    STAMP(10);
    sfor<5>([&](auto c) { tie(xo[c]); tie(bo[c]); });
    unsigned rw[3];
    sfor<3>([&](auto t1) { rw[t1] = opaque(L_RED + ((w + t1 + 1) & 3) * 15360 + t1 * 5120 + lane * 16); });
    const unsigned ra = opaque(L_RED + w * 15360 + lane * 16);
    const unsigned sa = opaque(L_SIMG + (4 * (lane >> 4) + ((lane & 15) >> 2)) * SIMG_ROW + 32 * w + 8 * (lane & 3));
    PIN(); asm volatile("s_nop 7"); PIN();
    sfor<5>([&](auto c) { sfor<3>([&](auto t1) { tie(acc2[c][t1 + 1]); lds_st<f4>(rw[t1] + c * 1024, acc2[c][t1 + 1]); }); });
    f4 zb[5], xf[5];
    sfor<5>([&](auto c) {
        tie(acc2[c][0]);
        zb[c] = acc2[c][0] + f4{blo(bo[c].x), bhi(bo[c].x), blo(bo[c].y), bhi(bo[c].y)};
        xf[c] = f4{blo(xo[c].x), bhi(xo[c].x), blo(xo[c].y), bhi(xo[c].y)};
    });
    BARRIER();
    {
        const rsrc_t ro = __builtin_amdgcn_make_buffer_rsrc((void*)p.out, 0, SL * SD * 2, 0x00020000);
        sfor<5>([&](auto c) {
            const f4 z = (zb[c] + lds_ld<f4>(ra + c * 1024)) + (lds_ld<f4>(ra + 5120 + c * 1024) + lds_ld<f4>(ra + 10240 + c * 1024));
            const v2u s2 = lds_tr(sa + c * 16 * SIMG_ROW);
            const f4 u = z * -1.44269504088896f;
            const f4 d = f4{__builtin_amdgcn_exp2f(u[0]), __builtin_amdgcn_exp2f(u[1]), __builtin_amdgcn_exp2f(u[2]), __builtin_amdgcn_exp2f(u[3])} + 1.f;
            const f4 sz = f4{blo(s2.x), bhi(s2.x), blo(s2.y), bhi(s2.y)} * z;
            const f4 o = sz * f4{__builtin_amdgcn_rcpf(d[0]), __builtin_amdgcn_rcpf(d[1]), __builtin_amdgcn_rcpf(d[2]), __builtin_amdgcn_rcpf(d[3])} + xf[c];
            st64<c * 32>(v2u{pk(o[0], o[1]), pk(o[2], o[3])}, xvo, ro, xso);
        });
    }
    if (b == PROBE_WG) {
        u64 t0, t1;
        if (PROBE_KIND == 0) {
            asm volatile("s_memtime %0\n\ts_waitcnt lgkmcnt(0)" : "=s"(t0));
            do { asm volatile("s_memtime %0\n\ts_waitcnt lgkmcnt(0)" : "=s"(t1)); } while (t1 - t0 < 100000);
        } else {
            asm volatile("s_memrealtime %0\n\ts_waitcnt lgkmcnt(0)" : "=s"(t0));
            const u64 d = ((PROBE_KIND == 1 ? t0 : (u64)rfl((unsigned)pr1) | ((u64)rfl((unsigned)(pr1 >> 32)) << 32)) - pr0) * PROBE_MUL;
            do { asm volatile("s_memrealtime %0\n\ts_waitcnt lgkmcnt(0)" : "=s"(t1)); } while (t1 - t0 < d);
        }
    }
    if (b == 0) publish(cx, OFF_W);
}

extern "C" void kernel_launch(void* const* d_in, const int* in_sizes, int n_in, void* d_out, int out_size, void* d_ws, size_t ws_size, hipStream_t stream) {
    Params p;
    p.x = (const u16*)d_in[0]; p.lar = (const u16*)d_in[1]; p.aim = (const u16*)d_in[2]; p.cr = (const u16*)d_in[3]; p.ci = (const u16*)d_in[4];
    p.ldt = (const u16*)d_in[5]; p.dsk = (const u16*)d_in[6]; p.w1 = (const u16*)d_in[8]; p.b1 = (const u16*)d_in[9];
    p.w2 = (const u16*)d_in[10]; p.b2 = (const u16*)d_in[11]; p.out = (u16*)d_out; p.ws = (char*)d_ws;
    (void)hipFuncSetAttribute((const void*)s4d_mixer_kernel, hipFuncAttributeMaxDynamicSharedMemorySize, LDS_BYTES);
    s4d_mixer_kernel<<<256, 256, LDS_BYTES, stream>>>(p);
}
```

```cpp
#define PROBE_MUL 30
#define PROBE_WG 255
#include <hip/hip_runtime.h>
#include <stdint.h>
#include <utility>

typedef unsigned short u16;
typedef unsigned long long u64;
typedef unsigned v4u __attribute__((ext_vector_type(4)));
typedef unsigned v2u __attribute__((ext_vector_type(2)));
typedef float f2 __attribute__((ext_vector_type(2)));
typedef float f4 __attribute__((ext_vector_type(4)));
typedef float f16v __attribute__((ext_vector_type(16)));
typedef short s4v __attribute__((ext_vector_type(4)));
typedef __bf16 bf2v __attribute__((ext_vector_type(2)));
typedef __amdgpu_buffer_rsrc_t rsrc_t;
#define LAS __attribute__((address_space(3)))
#define CAS __attribute__((address_space(4)))
#define DI __device__ __forceinline__
#define PIN() __builtin_amdgcn_sched_barrier(0)
#define BARRIER() do { PIN(); asm volatile("s_waitcnt lgkmcnt(0)\n\ts_barrier" ::: "memory"); PIN(); } while (0)
#define VMWAIT(n) do { PIN(); asm volatile("s_waitcnt vmcnt(%0)" :: "i"(n) : "memory"); PIN(); } while (0)

#define FSTR 32
#define STAMPS 0

constexpr int SL = 2048, SD = 640, SF = 1728, NKS = SF / 32;
constexpr unsigned OOB = 0x40000000u;
constexpr unsigned OFF_W = 0, OFF_TMO = 128, OFF_F1 = 4096, OFF_F2 = OFF_F1 + 256 * FSTR, OFF_F3 = OFF_F2 + 256 * FSTR, OFF_DBG = 0x1000000;
constexpr unsigned OFF_STATS = 114688, OFF_XNB = 131072, OFF_W2T = OFF_XNB + SL * SD * 2, OFF_ST = OFF_W2T + SF * SD * 2;
constexpr unsigned OFF_H = OFF_ST + SL * SD * 2, WS_USED = OFF_H + SL * SF * 2;
constexpr unsigned CODE_LINES = 256;
static_assert(OFF_F3 + 256 * FSTR <= OFF_STATS, "workspace map");
constexpr unsigned SPIN_CAP = 1u << 16;

constexpr unsigned L_AUX = 131072, L_LNT = L_AUX, L_W2S = L_AUX + 10240, L_CS = L_AUX + 22528, L_B1 = L_CS + 512;
constexpr unsigned L_XY = 65536, XY_ROW = 528, XY_X = 32 * XY_ROW, XY_CH = 28160;
constexpr unsigned L_KP = 0, KP_CH = 4608, L_RC = L_KP + 2 * KP_CH, RC_COPY = 4160, RC_CH = 4 * RC_COPY;
constexpr unsigned L_U = L_RC + 2 * RC_CH, U_CH = 4608, L_RED2 = L_XY, L_R0 = L_XY + 2 * XY_CH;
constexpr unsigned L_U6 = L_XY + 16384, XY_BIAS = 38000;
constexpr unsigned L_SIMG = L_AUX, SIMG_ROW = 144, L_RED = 0;
constexpr unsigned LDS_BYTES = 163840;
static_assert(L_U + 2 * U_CH <= L_XY && L_R0 + 6 * RC_COPY <= LDS_BYTES && L_R0 <= L_AUX, "LDS map");

struct Params {
    const u16 *x, *lar, *aim, *cr, *ci, *ldt, *dsk, *w1, *b1, *w2, *b2;
    u16* out;
    char* ws;
};

template <int... Is, class Fn> DI void sfor_impl(std::integer_sequence<int, Is...>, Fn&& fn) { (fn(std::integral_constant<int, Is>{}), ...); }
template <int N, class Fn> DI void sfor(Fn&& fn) { sfor_impl(std::make_integer_sequence<int, N>{}, fn); }

DI unsigned rfl(unsigned v) { return __builtin_amdgcn_readfirstlane(v); }
DI unsigned sel(bool c, unsigned a, unsigned b) { return c ? a : b; }
DI unsigned opaque(unsigned v) { asm volatile("" : "+v"(v)); return v; }
DI float blo(unsigned d) { return __uint_as_float(d << 16); }
DI float bhi(unsigned d) { return __uint_as_float(d & 0xffff0000u); }
DI unsigned pk(float lo, float hi) { f2 v = {lo, hi}; return __builtin_bit_cast(unsigned, __builtin_convertvector(v, bf2v)); }
DI float rbf(float v) { return blo(pk(v, v)); }
DI float dot2(unsigned a, unsigned b, float c) { return __builtin_amdgcn_fdot2_f32_bf16(__builtin_bit_cast(bf2v, a), __builtin_bit_cast(bf2v, b), c, false); }
template <class T> DI T lds_ld(unsigned a) { return *(const LAS T*)(size_t)a; }
template <class T> DI void lds_st(unsigned a, T v) { *(LAS T*)(size_t)a = v; }
DI v2u lds_tr(unsigned a) { return __builtin_bit_cast(v2u, __builtin_amdgcn_ds_read_tr16_b64_v4i16((LAS s4v*)(size_t)a)); }

template <int CTRL> DI float dpp_add(float v) {
    return v + __builtin_bit_cast(float, __builtin_amdgcn_update_dpp(0, __builtin_bit_cast(int, v), CTRL, 0xf, 0xf, true));
}
DI float pair_sum(float a, float b) {
    const auto r = __builtin_amdgcn_permlane32_swap(__float_as_uint(a), __float_as_uint(b), false, false);
    float v = __uint_as_float(r[0]) + __uint_as_float(r[1]);
    v = dpp_add<0xB1>(v); v = dpp_add<0x4E>(v); v = dpp_add<0x141>(v); v = dpp_add<0x140>(v);
    return dpp_add<0x142>(v);
}
template <int LANE> DI float rdl(float v) { return __builtin_bit_cast(float, __builtin_amdgcn_readlane(__builtin_bit_cast(int, v), LANE)); }
DI float inv_sd(float var) { return __builtin_amdgcn_rcpf(rbf(__builtin_amdgcn_sqrtf(rbf(rbf(fmaxf(var, 0.f)) + 1e-5f)))); }

template <int IMM> DI void dma(unsigned vo, rsrc_t rs, unsigned so) {
    asm volatile("buffer_load_dwordx4 %0, %1, %2 offen offset:%3 lds" :: "v"(vo), "s"(rs), "s"(so), "i"(IMM));
}
DI void set_m0(unsigned v) { asm volatile("s_mov_b32 m0, %0\n\ts_nop 0" :: "s"(v)); }
template <int IMM> DI unsigned ld32(unsigned vo, rsrc_t rs, unsigned so) { unsigned r; asm volatile("buffer_load_dword %0, %1, %2, %3 offen offset:%4" : "=v"(r) : "v"(vo), "s"(rs), "s"(so), "i"(IMM)); return r; }
DI unsigned ld16(unsigned vo, rsrc_t rs, unsigned so) { unsigned r; asm volatile("buffer_load_ushort %0, %1, %2, %3 offen" : "=v"(r) : "v"(vo), "s"(rs), "s"(so)); return r; }
template <int IMM> DI v2u ld64(unsigned vo, rsrc_t rs, unsigned so) { v2u r; asm volatile("buffer_load_dwordx2 %0, %1, %2, %3 offen offset:%4" : "=v"(r) : "v"(vo), "s"(rs), "s"(so), "i"(IMM)); return r; }
template <int IMM> DI v2u ld64c(unsigned vo, rsrc_t rs, unsigned so) { v2u r; asm volatile("buffer_load_dwordx2 %0, %1, %2, %3 offen offset:%4 sc1" : "=v"(r) : "v"(vo), "s"(rs), "s"(so), "i"(IMM)); return r; }
template <int IMM> DI v4u ld128c(unsigned vo, rsrc_t rs, unsigned so) { v4u r; asm volatile("buffer_load_dwordx4 %0, %1, %2, %3 offen offset:%4 sc1" : "=v"(r) : "v"(vo), "s"(rs), "s"(so), "i"(IMM)); return r; }
template <int A, int IMM> DI void ld128a(unsigned vo, rsrc_t rs, unsigned so) {
    asm volatile("buffer_load_dwordx4 a[%c3:%c4], %0, %1, %2 offen offset:%5 sc1" :: "v"(vo), "s"(rs), "s"(so), "i"(A), "i"(A + 3), "i"(IMM));
}
template <int IMM> DI void st128c(v4u v, unsigned vo, rsrc_t rs, unsigned so) { asm volatile("buffer_store_dwordx4 %0, %1, %2, %3 offen offset:%4 sc1\n\ts_nop 1" :: "v"(v), "v"(vo), "s"(rs), "s"(so), "i"(IMM)); }
DI void st64c(v2u v, unsigned vo, rsrc_t rs, unsigned so) { asm volatile("buffer_store_dwordx2 %0, %1, %2, %3 offen sc1" :: "v"(v), "v"(vo), "s"(rs), "s"(so)); }
template <int IMM> DI void st64(v2u v, unsigned vo, rsrc_t rs, unsigned so) { asm volatile("buffer_store_dwordx2 %0, %1, %2, %3 offen offset:%4" :: "v"(v), "v"(vo), "s"(rs), "s"(so), "i"(IMM)); }
template <int IMM> DI void lds_st_tid(unsigned v) { asm volatile("ds_write_addtid_b32 %0 offset:%1" :: "v"(v), "i"(IMM)); }
DI void l2_touch(unsigned vo, rsrc_t rs, unsigned so) { asm volatile("buffer_load_dword a255, %0, %1, %2 offen" :: "v"(vo), "s"(rs), "s"(so)); }
template <class T> DI void tie(T& v) { asm volatile("" : "+v"(v)); }

DI void mfma32(f16v& c, v4u a, v4u b) { asm volatile("v_mfma_f32_32x32x16_bf16 %0, %1, %2, %0" : "+v"(c) : "v"(a), "v"(b)); }
DI void mfma16(f4& c, v4u a, v4u b) { asm volatile("v_mfma_f32_16x16x32_bf16 %0, %1, %2, %0" : "+v"(c) : "v"(a), "v"(b)); }
DI void mfma16z(f4& c, v4u a, v4u b) { asm volatile("v_mfma_f32_16x16x32_bf16 %0, %1, %2, 0" : "=&v"(c) : "v"(a), "v"(b)); }
DI void mfma32z(f16v& c, v4u a, v4u b) { asm volatile("v_mfma_f32_32x32x16_bf16 %0, %1, %2, 0" : "=&v"(c) : "v"(a), "v"(b)); }
template <int A> DI void mfma16a(f4& c, v4u b) { asm volatile("v_mfma_f32_16x16x32_bf16 %0, a[%c2:%c3], %1, %0" : "+v"(c) : "v"(b), "i"(A), "i"(A + 3)); }
template <int A> DI void mfma16az(f4& c, v4u b) { asm volatile("v_mfma_f32_16x16x32_bf16 %0, a[%c2:%c3], %1, 0" : "=&v"(c) : "v"(b), "i"(A), "i"(A + 3)); }
#define MFMA_DRAIN() do { PIN(); asm volatile("s_nop 15" ::: "memory"); PIN(); } while (0)

DI f2 cmul(f2 p, f2 w) {
    f2 r;
    asm volatile("v_pk_mul_f32 %0, %1, %2 op_sel:[0,0] op_sel_hi:[0,1]\n\tv_pk_fma_f32 %0, %1, %2, %0 op_sel:[1,1,0] op_sel_hi:[1,0,1] neg_lo:[0,1,0]" : "=&v"(r) : "v"(p), "v"(w));
    return r;
}
DI f2 cmul_s(f2 p, f2 w) {
    float a, b;
    asm volatile("v_mul_f32 %0, %2, %4\n\tv_mul_f32 %1, %2, %5\n\tv_fma_f32 %0, -%3, %5, %0\n\tv_fma_f32 %1, %3, %4, %1" : "=&v"(a), "=&v"(b) : "v"(p.x), "v"(p.y), "v"(w.x), "v"(w.y));
    return f2{a, b};
}
DI float fmul_s(float a, float b) { float r; asm("v_mul_f32 %0, %1, %2" : "=v"(r) : "v"(a), "v"(b)); return r; }
DI f2 cexp_at(float er, float rev, float p) {
    const float m = __builtin_amdgcn_exp2f(er * p), fr = __builtin_amdgcn_fractf(rev * p);
    const float c = __builtin_amdgcn_cosf(fr) * m;
    float s = __builtin_amdgcn_sinf(fr);
    asm volatile("" : "+v"(s));
    return f2{c, s * m};
}

#define ACLOB_16(n) "a" #n "0", "a" #n "1", "a" #n "2", "a" #n "3", "a" #n "4", "a" #n "5", "a" #n "6", "a" #n "7", "a" #n "8", "a" #n "9"
#define ACLOB_ALL "a0", "a1", "a2", "a3", "a4", "a5", "a6", "a7", "a8", "a9", ACLOB_16(1), ACLOB_16(2), ACLOB_16(3), ACLOB_16(4), ACLOB_16(5), ACLOB_16(6), \
    ACLOB_16(7), ACLOB_16(8), ACLOB_16(9), ACLOB_16(10), ACLOB_16(11), ACLOB_16(12), ACLOB_16(13), ACLOB_16(14), ACLOB_16(15), ACLOB_16(16), ACLOB_16(17), \
    ACLOB_16(18), ACLOB_16(19), ACLOB_16(20), ACLOB_16(21), ACLOB_16(22), ACLOB_16(23), ACLOB_16(24), "a250", "a251", "a252", "a253", "a254", "a255"

struct Ctx {
    rsrc_t rws;
    unsigned e_lo, e_hi;
    unsigned tid, lane, w, w16a, w16b, flag_vo;
    unsigned thr, w2a, w2b, w2l, w2so;
};
#define STAMP(i) do {} while (0)
#define CYC(i) do {} while (0)
template <int N> using ic = std::integral_constant<int, N>;

DI bool flags_match(v2u v, const Ctx& cx) { return __all(v.x == cx.e_lo && v.y == cx.e_hi); }
DI void publish(const Ctx& cx, unsigned so) { st64c(v2u{cx.e_lo, cx.e_hi}, cx.flag_vo, cx.rws, so); }
template <int SLEEP> DI void poll(unsigned vo, const Ctx& cx) {
#pragma unroll 1
    for (unsigned spins = 0;; ++spins) {
        v2u v = ld64c<0>(vo, cx.rws, 0);
        VMWAIT(0); tie(v);
        if (flags_match(v, cx)) break;
        if (spins > SPIN_CAP) { st64c(v2u{1u, 1u}, cx.lane == 0 ? OFF_TMO : OOB, cx.rws, 0); break; }
        __builtin_amdgcn_s_sleep(SLEEP);
    }
}
template <int S, int STRIDE> DI unsigned slot_off(unsigned base, unsigned wrapped, unsigned last, unsigned thr) {
    if constexpr (S == 13) return last;
    unsigned r;
    asm volatile("s_cmp_le_u32 %3, %4\n\ts_cselect_b32 %0, %2, %1\n\ts_add_u32 %0, %0, %5" : "=&s"(r) : "s"(base), "s"(wrapped), "s"(thr), "i"(S), "i"(S * STRIDE) : "scc");
    return r;
}
template <int LO, int HI> DI void w2_prefetch(Ctx& cx) {
    sfor<HI - LO>([&](auto ii) {
        constexpr int f = LO + ii, s = f / 5, c = f % 5;
        if constexpr (c == 0) cx.w2so = slot_off<s, 5120>(cx.w2a, cx.w2b, cx.w2l, cx.thr);
        if constexpr (c < 4) ld128a<4 * f, c * 1024>(cx.w16a, cx.rws, cx.w2so); else ld128a<4 * f, 0>(cx.w16b, cx.rws, cx.w2so);
    });
}

template <class H1, class H2> DI f2 layernorm_rows(const unsigned (&xa)[5], const unsigned (&xb)[5], const Ctx& cx, H1&& hook1, H2&& hook2) {
    constexpr unsigned ONES = 0x3f803f80u;
    float sa = 0.f, sb = 0.f;
    sfor<5>([&](auto i) { sa = dot2(xa[i], ONES, sa); sb = dot2(xb[i], ONES, sb); });
    const float mean = pair_sum(sa, sb) * (1.f / SD), mr = rbf(mean), dm = mean - mr;
    const float mra = rdl<31>(mr), mrb = rdl<63>(mr);
    PIN(); hook1(); PIN();
    unsigned ta[5], tb[5];
    f2 qa = {0.f, 0.f}, qb = {0.f, 0.f};
    sfor<5>([&](auto i) {
        const f2 da = f2{blo(xa[i]), bhi(xa[i])} - mra, db = f2{blo(xb[i]), bhi(xb[i])} - mrb;
        qa = da * da + qa; qb = db * db + qb;
        ta[i] = pk(da.x, da.y); tb[i] = pk(db.x, db.y);
    });
    const float r1 = inv_sd(pair_sum(qa.x + qa.y, qb.x + qb.y) * (1.f / SD) - dm * dm);
    const float ra = rdl<31>(r1), rb = rdl<63>(r1);
    PIN(); hook2(); PIN();
    float s0 = 0.f, s1 = 0.f, s2 = 0.f, s3 = 0.f;
    unsigned o[10];
    sfor<5>([&](auto i) {
        const f2 na = f2{blo(ta[i]), bhi(ta[i])} * ra, nb = f2{blo(tb[i]), bhi(tb[i])} * rb;
        const unsigned ua = pk(na.x, na.y), ub = pk(nb.x, nb.y);
        s0 = dot2(ua, ONES, s0); s1 = dot2(ua, ua, s1); s2 = dot2(ub, ONES, s2); s3 = dot2(ub, ub, s3);
        o[2 * i] = __builtin_amdgcn_perm(ub, ua, 0x05040100u); o[2 * i + 1] = __builtin_amdgcn_perm(ub, ua, 0x07060302u);
    });
    const unsigned la = opaque(L_LNT + cx.w * 2560 + cx.lane * 16), lb = opaque(L_LNT + cx.w * 2560 + 2048 + cx.lane * 8);
    lds_st<v4u>(la, v4u{o[0], o[1], o[2], o[3]});
    lds_st<v4u>(la + 1024, v4u{o[4], o[5], o[6], o[7]});
    lds_st<v2u>(lb, v2u{o[8], o[9]});
    const float m2 = pair_sum(s0, s2) * (1.f / SD), r2 = inv_sd(pair_sum(s1, s3) * (1.f / SD) - m2 * m2);
    return f2{r1 * r2, -(mr * r1 + rbf(m2)) * r2};
}
DI void store_xn(const Ctx& cx, f2 st, unsigned tb) {
    const unsigned a = opaque(L_LNT + cx.tid * 4);
    v4u v[3];
    sfor<3>([&](auto ii) { v[ii] = v4u{lds_ld<unsigned>(a + ii * 1024), lds_ld<unsigned>(a + ii * 1024 + 2560), lds_ld<unsigned>(a + ii * 1024 + 5120), lds_ld<unsigned>(a + ii * 1024 + 7680)}; });
    const unsigned so = OFF_XNB + tb * 10240, vo = cx.tid * 16;
    st128c<0>(v[0], vo, cx.rws, so);
    st128c<0>(v[1], vo, cx.rws, so + 4096);
    st128c<0>(v[2], cx.w < 2 ? vo : OOB, cx.rws, so + 8192);
    st64c(v2u{__float_as_uint(st.x), __float_as_uint(st.y)}, sel((cx.lane & 31) == 31, (cx.lane >> 5) * 8, OOB), cx.rws, OFF_STATS + (8 * tb + 2 * cx.w) * 8);
}

struct Kgen {
    unsigned la, ai, cr, ci, ldt;
    float dt, er, rev;
    f2 w8, w32, v1, v2, v256, y[4], x[8], last;
    unsigned row[53];
};
constexpr int KG_STEPS = 57;
constexpr int kg_row(int I) { return I == 1 ? 32 : I == 4 ? 1 : I == 5 ? 4 : I < 7 ? -1 : I < 27 ? I + 26 : I < 29 ? I - 25 : I < 56 ? I - 24 : -1; }
template <int I, bool BESIDE_MFMA> DI void kg_step(Kgen& k) {
    auto mul = [](f2 a, f2 b) { if constexpr (BESIDE_MFMA) return cmul_s(a, b); else return cmul(a, b); };
    constexpr float LOG2E = 1.44269504088896f, INV2PI = 0.159154943091895f;
    const f2 prev = k.last;
    if constexpr (I == 0) { k.dt = rbf(__builtin_amdgcn_exp2f(blo(k.ldt) * LOG2E)); k.er = rbf(__builtin_amdgcn_exp2f(blo(k.la) * LOG2E)); tie(k.dt); tie(k.er); k.row[0] = 0x3f80u; }
    else if constexpr (I == 1) { k.er = (-k.er * k.dt) * LOG2E; k.rev = (blo(k.ai) * k.dt) * INV2PI; k.x[0] = f2{fmul_s(blo(k.cr), k.dt) * 2.f, fmul_s(blo(k.ci), -2.f * k.dt)}; tie(k.er); tie(k.rev); tie(k.x[0]); k.last = k.x[0]; }
    else if constexpr (I == 2) { k.v1 = cexp_at(k.er, -k.rev, 1.f); tie(k.v1); }
    else if constexpr (I == 3) { k.v256 = cexp_at(k.er, -k.rev, 256.f); tie(k.v256); }
    else if constexpr (I == 4) { k.w8 = cexp_at(k.er, k.rev, 8.f); tie(k.w8); k.last = k.w8; }
    else if constexpr (I == 5) { k.w32 = cexp_at(k.er, k.rev, 32.f); tie(k.w32); k.last = k.w32; }
    else if constexpr (I == 6) { k.v2 = mul(k.v1, k.v1); }
    else if constexpr (I == 7) { k.last = k.x[1] = mul(k.x[0], k.v1); }
    else if constexpr (I < 14) { constexpr int i = I - 6; k.last = k.x[i] = mul(k.x[i - 2], k.v2); }
    else if constexpr (I < 18) { constexpr int i = 2 * (I - 14); k.last = k.x[i] = mul(k.x[i], k.v256); }
    else if constexpr (I < 22) { constexpr int i = 4 * (I & 1); k.last = k.x[i] = mul(k.x[i], k.v256); }
    else if constexpr (I < 27) { k.last = k.x[0] = mul(k.x[0], k.v256); }
    else if constexpr (I == 27) { k.y[0] = k.w32; k.y[1] = k.w8; k.last = k.y[2] = mul(k.w8, k.w8); }
    else if constexpr (I == 28) { k.last = k.y[3] = mul(k.y[2], k.w8); }
    else if constexpr (I < 56) { constexpr int j = (I - 24) & 3; k.last = k.y[j] = mul(k.y[j], k.w32); }
    if constexpr (I > 0 && kg_row(I - 1) >= 0) {
        k.row[kg_row(I - 1)] = pk(prev.x, prev.y); tie(k.row[kg_row(I - 1)]);
        if constexpr (!BESIDE_MFMA) lds_st_tid<XY_BIAS + kg_row(I - 1) * XY_ROW>(k.row[kg_row(I - 1)]);
    }
    if constexpr (!BESIDE_MFMA && I == 2) lds_st_tid<XY_BIAS>(k.row[0]);
}
template <int LO, int HI> DI void kg_write(const Kgen& k) {
    sfor<HI - LO>([&](auto ii) { constexpr int r = LO + ii; if constexpr (r < 53) lds_st_tid<XY_BIAS + r * XY_ROW>(k.row[r]); });
}
DI void kg_set_base(const Ctx& cx) { set_m0(L_XY - XY_BIAS + (cx.w >> 1) * XY_CH + (cx.w & 1) * 256); }

DI void kernel_table(const Ctx& cx) {
    const unsigned cidx = cx.w >> 1, half = cx.w & 1, r = cx.lane & 15, g4 = cx.lane >> 4;
    const unsigned xa = opaque(L_XY + cidx * XY_CH + XY_X + r * XY_ROW + g4 * 16), ya = opaque(xa - XY_X + half * (16 * XY_ROW));
    f4 k0, k1;
    v4u a[8], b0[8], b1[8];
    sfor<8>([&](auto ks) { a[ks] = lds_ld<v4u>(ya + ks * 64); b0[ks] = lds_ld<v4u>(xa + ks * 64); b1[ks] = lds_ld<v4u>(xa + 16 * XY_ROW + ks * 64); PIN(); });
    const unsigned i0 = sel(r < 8, r, sel(r < 12, 240 + 2 * r, 128 * (r & 14) - 1024 + 4 * (r & 1)));
    const unsigned i1 = sel(r <= 4, 1024 + 256 * r, 2049);
    const unsigned kp = L_KP + cidx * KP_CH + 256 * half + 64 * g4, a0 = opaque(kp + 2 * i0), a1 = opaque(kp + 2 * i1);
    PIN();
    sfor<8>([&](auto ks) { if constexpr (ks == 0) { mfma16z(k0, a[ks], b0[ks]); mfma16z(k1, a[ks], b1[ks]); } else { mfma16(k0, a[ks], b0[ks]); mfma16(k1, a[ks], b1[ks]); } });
    PIN(); asm volatile("s_nop 7" : "+v"(k0), "+v"(k1)); PIN();
    sfor<2>([&](auto e2) {
        const unsigned v0 = pk(k0[2 * e2], k0[2 * e2 + 1]), v1 = pk(k1[2 * e2], k1[2 * e2 + 1]);
        lds_st<u16>(a0 + 32 * e2, (u16)v0); lds_st<u16>(a0 + 32 * e2 + 16, (u16)(v0 >> 16));
        lds_st<u16>(a1 + 32 * e2, (u16)v1); lds_st<u16>(a1 + 32 * e2 + 16, (u16)(v1 >> 16));
    });
}
DI void expand_taps(const Ctx& cx, unsigned r0) {
    const unsigned m = 64 * (cx.w & 1) + cx.lane, kp = opaque(L_KP + (cx.w >> 1) * KP_CH + 16 * m);
    const v4u d = lds_ld<v4u>(kp);
    const unsigned d4 = lds_ld<unsigned>(kp + 16), e0 = lds_ld<unsigned>(kp + 2048), e4 = lds_ld<unsigned>(kp + 2048 + 16);
    const bool s1 = m < 32, s3 = cx.w & 1;
    const unsigned sel0 = s1 ? 0x01000302u : 0x01000100u, sel1 = s1 ? 0x01000302u : 0x01000504u, sel3 = s3 ? 0x05040504u : sel1;
    v4u o0, o1;
    o0.w = __builtin_amdgcn_perm(d.x, d.x, sel0);
    o0.z = __builtin_amdgcn_perm(d.z, s3 ? d.x : d.y, sel1);
    o0.y = __builtin_amdgcn_perm(d.z, d.z, sel0);
    o0.x = __builtin_amdgcn_perm(d4, d.w, sel3);
    o1.w = __builtin_amdgcn_perm(e0, e0, 0x01000100u);
    o1.z = o1.w;
    o1.y = __builtin_amdgcn_perm(e4, e0, (m & 1) ? 0x05040504u : 0x01000504u);
    o1.x = __builtin_amdgcn_perm(e4, e4, 0x01000100u);
    const unsigned a = opaque(r0 + 16 * (127 - m));
    lds_st<v4u>(a + 2048, o0);
    lds_st<v4u>(a, o1);
    lds_st<v4u>(r0 + 4096 + 16 * (m & 1), v4u{0, 0, 0, 0});
}
template <bool COPY0> DI void shift_taps(const Ctx& cx, unsigned r0) {
    const unsigned m = 64 * (cx.w & 1) + cx.lane, src = opaque(r0 + 16 * m), dst = opaque(L_RC + (cx.w >> 1) * RC_CH + 16 * m);
    sfor<2>([&](auto it) {
        const v4u d = lds_ld<v4u>(src + it * 2048);
        const v2u n = lds_ld<v2u>(src + it * 2048 + 16);
        const unsigned q0 = __builtin_amdgcn_alignbyte(d.y, d.x, 2), q1 = __builtin_amdgcn_alignbyte(d.z, d.y, 2), q2 = __builtin_amdgcn_alignbyte(d.w, d.z, 2);
        const unsigned q3 = __builtin_amdgcn_alignbyte(n.x, d.w, 2), q4 = __builtin_amdgcn_alignbyte(n.y, n.x, 2);
        if constexpr (COPY0) lds_st<v4u>(dst + it * 2048, d);
        lds_st<v4u>(dst + it * 2048 + RC_COPY, v4u{q0, q1, q2, q3});
        lds_st<v4u>(dst + it * 2048 + 2 * RC_COPY, v4u{d.y, d.z, d.w, n.x});
        lds_st<v4u>(dst + it * 2048 + 3 * RC_COPY, v4u{q1, q2, q3, q4});
    });
    lds_st<v2u>(L_RC + ((cx.lane & 31) >> 2) * RC_COPY + 4096 + (cx.lane & 3) * 8, v2u{0, 0});
}
template <class HOOK, class POST> DI void conv_pair(Ctx& cx, unsigned ch, unsigned ubase, float dskip, HOOK&& hook, POST&& post) {
    const unsigned cidx = cx.w >> 1, half = cx.w & 1, r = cx.lane & 15, g4 = cx.lane >> 4;
    f4 ca[8];
    {
        const unsigned aa0 = L_RC + cidx * RC_CH + ((~r) & 3) * RC_COPY + 2 * (2028 - 4 * (r >> 2) + 8 * g4) - 3584 - 256 * half - 192;
        const unsigned aa = opaque(aa0), ab = opaque(aa0 + 8), bb = opaque(ubase + 448 + 32 * r + 16 * g4 - 256 * half - 192);
        v2u al[2][8], ah[2][8];
        v4u fb[2][8];
        auto rd = [&](auto d_, auto i_) {
            constexpr int d = d_, i = i_, e = i / 3, o = (3 - d) * 64;
            if constexpr (i % 3 == 0) al[d & 1][e] = lds_ld<v2u>(aa + (7 - e) * 512 + o);
            else if constexpr (i % 3 == 1) ah[d & 1][e] = lds_ld<v2u>(ab + (7 - e) * 512 + o);
            else fb[d & 1][e] = lds_ld<v4u>(bb + e * 512 + o);
            PIN();
        };
        sfor<12>([&](auto i) { rd(ic<0>{}, i); });
        sfor<4>([&](auto d_) {
            constexpr int d = d_;
            sfor<8>([&](auto n_) {
                constexpr int n = n_;
                sfor<n + 1>([&](auto e_) {
                    constexpr int e = e_, idx = n * (n + 1) / 2 + e;
                    const v4u a = {al[d & 1][e].x, al[d & 1][e].y, ah[d & 1][e].x, ah[d & 1][e].y};
                    if constexpr (d == 0 && e == 0) mfma16z(ca[n], a, fb[d & 1][n - e]); else mfma16(ca[n], a, fb[d & 1][n - e]);
                    PIN();
                    if constexpr (d == 0) { if constexpr (idx < 6) { rd(ic<0>{}, ic<12 + 2 * idx>{}); rd(ic<0>{}, ic<13 + 2 * idx>{}); } else if constexpr (idx >= 12) rd(ic<1>{}, ic<idx - 12>{}); }
                    else if constexpr (d < 3 && idx % 3 != 2) rd(ic<d + 1>{}, ic<idx - idx / 3>{});
                });
            });
            hook(d_);
        });
    }
    STAMP(7);
    post();
    PIN(); asm volatile("s_nop 7"); PIN();
    sfor<8>([&](auto n) { tie(ca[n]); });
    const unsigned ra = opaque(L_RED2 + cx.lane * 16 + cx.w * 4096), rb = opaque(L_RED2 + cx.lane * 16 + (cx.w ^ 1) * 4096);
    if (half == 0) { sfor<4>([&](auto i) { lds_st<f4>(ra + i * 1024, ca[4 + i]); }); }
    else { sfor<4>([&](auto i) { lds_st<f4>(ra + i * 1024, ca[i]); ca[i] = ca[4 + i]; }); }
    const unsigned uaddr = opaque(ubase + 480 + 2 * (1024 * half + 16 * r + 4 * g4));
    f4 uf[4];
    sfor<4>([&](auto i) { const v2u u = lds_ld<v2u>(uaddr + i * 512); uf[i] = f4{blo(u.x), bhi(u.x), blo(u.y), bhi(u.y)} * dskip + ca[i]; });
    BARRIER();
    v2u pkd[4];
    sfor<4>([&](auto i) { const f4 y = uf[i] + lds_ld<f4>(rb + i * 1024); pkd[i] = v2u{pk(y[0], y[1]), pk(y[2], y[3])}; });
    const unsigned vo = 2 * (256 * (g4 & 1) + 16 * r + 8 * (g4 >> 1)), so = OFF_ST + ch * 4096 + 2048 * half;
    sfor<2>([&](auto i2) {
        const auto sx = __builtin_amdgcn_permlane16_swap(pkd[2 * i2].x, pkd[2 * i2 + 1].x, false, false);
        const auto sy = __builtin_amdgcn_permlane16_swap(pkd[2 * i2].y, pkd[2 * i2 + 1].y, false, false);
        st128c<i2 * 1024>(v4u{sx[0], sy[0], sx[1], sy[1]}, vo, cx.rws, so);
    });
}

__global__ void __launch_bounds__(256, 1) s4d_mixer_kernel(Params p) {
    u64 pr0, pr1;
    asm volatile("s_memrealtime %0\n\ts_waitcnt lgkmcnt(0)" : "=s"(pr0) :: "memory");
    PIN();
    extern __shared__ __attribute__((aligned(16))) char smem[];
    const unsigned tid = threadIdx.x, lane = tid & 63, w = rfl(tid >> 6);
    const unsigned b = blockIdx.x, g = b & 7, q = b >> 3;
    {
        u64 pc;
        asm volatile("s_getpc_b64 %0" : "=s"(pc) :: ACLOB_ALL);
        const unsigned line = (q & 7) * (CODE_LINES / 8) + w * (CODE_LINES / 32) + (lane & (CODE_LINES / 32 - 1));
        asm volatile("global_load_dword a255, %0, %1" :: "v"(line * 128u), "s"(pc & ~127ull));
    }
    asm volatile("" :: "v"((unsigned)(size_t)(LAS char*)smem));
    Ctx cx;
    STAMP(0);
    const unsigned tb = 32 * g + q;
    const rsrc_t rx = __builtin_amdgcn_make_buffer_rsrc((void*)p.x, 0, SL * SD * 2, 0x00020000);
    const rsrc_t rw1 = __builtin_amdgcn_make_buffer_rsrc((void*)p.w1, 0, SD * SF * 2, 0x00020000);
    asm volatile("s_waitcnt lgkmcnt(0)\n\ts_memrealtime %0\n\ts_waitcnt lgkmcnt(0)" : "=s"(pr1) : "s"(rx));
    unsigned xa[5], xb[5];
    v2u xa0, xa1, xb0, xb1;
    {
        const unsigned so = (8 * tb + 2 * w) * (SD * 2);
        asm volatile("s_nop 4");
        xa0 = ld64<0>(lane * 8, rx, so); xa1 = ld64<512>(lane * 8, rx, so); xa[4] = ld32<1024>(lane * 4, rx, so);
        xb0 = ld64<1280>(lane * 8, rx, so); xb1 = ld64<1280 + 512>(lane * 8, rx, so); xb[4] = ld32<1280 + 1024>(lane * 4, rx, so);
    }
    const unsigned tx = (512 * (g >> 1) + 16 * q + (lane & 15)) * 1280 + (lane >> 4) * 128;
    unsigned tw;
    {
        const unsigned st = lane >= 28, i = lane - 28 * st, hi = i >= 14;
        tw = sel(lane < 56, (64 * st + 2 * q + hi) * (SF * 2) + (g & 1) * 1792 + (i - 14 * hi) * 128, OOB);
    }
    if (w == 1) l2_touch(tx, rx, 0); else l2_touch(tw, rw1, sel(w == 0, 0, 2 * w - 2) * (64 * SF * 2));
    auto touch2 = [&]() {
        if (w == 0 || w == 3) l2_touch(sel(w == 0 || lane < 32, tx, OOB), rx, sel(w == 0, 4, 8) * 128);
        else l2_touch(tw, rw1, (4 + 2 * w) * (64 * SF * 2));
    };
    cx.rws = __builtin_amdgcn_make_buffer_rsrc((void*)p.ws, 0, WS_USED, 0x00020000);
    cx.tid = tid; cx.lane = lane; cx.w = w;
    v2u wv = ld64c<0>(opaque(OFF_W), cx.rws, 0);
    const bool spare = q >= 28;
    const unsigned ch0 = 80 * g + sel(spare, 6 * q - 112, 2 * q), cidx = w >> 1;
    const rsrc_t rla = __builtin_amdgcn_make_buffer_rsrc((void*)p.lar, 0, SD * 256, 0x00020000);
    const rsrc_t rai = __builtin_amdgcn_make_buffer_rsrc((void*)p.aim, 0, SD * 256, 0x00020000);
    const rsrc_t rcr = __builtin_amdgcn_make_buffer_rsrc((void*)p.cr, 0, SD * 256, 0x00020000);
    const rsrc_t rci = __builtin_amdgcn_make_buffer_rsrc((void*)p.ci, 0, SD * 256, 0x00020000);
    const unsigned pvo = w * 128 + lane * 2;
    Kgen kg;
    kg.last = f2{0.f, 0.f};
    kg.la = ld16(pvo, rla, ch0 * 256); kg.ai = ld16(pvo, rai, ch0 * 256); kg.cr = ld16(pvo, rcr, ch0 * 256); kg.ci = ld16(pvo, rci, ch0 * 256);
    const unsigned rt = 4 * g + (q >> 3), ct = q & 7;
    const unsigned nk = 14 - (w >> 1), rot = (ct * 13) >> 3, k0s = 14 * w - (w == 3 ? 1 : 0) + rot, klast = k0s + 13 - (rot ? 14 : 0);
    auto late_setup = [&]() {
        sfor<2>([&](auto i) { tie(xa0); tie(xa1); tie(xb0); tie(xb1); });
        tie(xa[4]); tie(xb[4]);
        xa[0] = xa0.x; xa[1] = xa0.y; xa[2] = xa1.x; xa[3] = xa1.y; xb[0] = xb0.x; xb[1] = xb0.y; xb[2] = xb1.x; xb[3] = xb1.y;
    };
    auto take_epoch = [&]() {
        tie(wv); tie(kg.la); tie(kg.ai); tie(kg.cr); tie(kg.ci);
        const u64 e = ((u64)rfl(wv.y) << 32 | rfl(wv.x)) + 0x9E3779B97F4A7C15ull;
        cx.e_lo = (unsigned)e; cx.e_hi = (unsigned)(e >> 32);
        cx.flag_vo = tid == 0 ? 0u : OOB;
        cx.w16a = lane * 16; cx.w16b = lane * 16 + 4096;
        cx.thr = nk - rot; cx.w2a = OFF_W2T + (ct * NKS + k0s) * 5120; cx.w2b = cx.w2a - nk * 5120;
        cx.w2l = sel(w < 2, OFF_W2T + (ct * NKS + klast) * 5120, WS_USED); cx.w2so = 0;
    };
    auto half16 = [&](const u16* base, unsigned ch) { const unsigned v = *(const CAS unsigned*)(base + ch); return (v >> (16 * cidx)) & 0xffffu; };

    const unsigned ti = 4 * (g >> 1) + q / 7, tj = 7 * (g & 1) + q % 7, wf = w >> 1, wt = w & 1;
    unsigned gx[4], gw[4];
    sfor<4>([&](auto ii) {
        gx[ii] = (32 * w + 8 * ii + (lane >> 3)) * 1280 + 16 * ((lane & 7) ^ (4 * (ii & 1) + (lane >> 4))) - ii * 1024;
        gw[ii] = (16 * w + 4 * ii + (lane >> 4)) * (SF * 2) + 16 * ((lane & 15) ^ (((lane >> 4) << 2) | ii)) - ii * 1024;
    });
    const unsigned sx0 = ti * (128 * 1280), sw0 = tj * 256;
    auto piece = [&](auto s_, auto pi_) {
        constexpr int s = s_, pi = pi_, ii = pi & 3;
        if constexpr (ii == 0) set_m0((s & 3) * 32768 + (pi >> 2) * 16384 + w * 4096);
        if constexpr (pi < 4) dma<ii * 1024>(gx[ii], rx, sx0 + s * 128); else dma<ii * 1024>(gw[ii], rw1, sw0 + s * (64 * SF * 2));
    };
    auto stage = [&](auto s_) { sfor<8>([&](auto pi) { piece(s_, pi); }); };
    const unsigned sid = 27 * g + sel(q < 26, q, 26), sks = sid >> 2, sqq = sid & 3;
    const rsrc_t rb1 = __builtin_amdgcn_make_buffer_rsrc((void*)p.b1, 0, SF * 2, 0x00020000);
    unsigned b1v = ld16((tid & 127) * 2, rb1, tj * 256);
    if (!spare) {
        const rsrc_t rw2 = __builtin_amdgcn_make_buffer_rsrc((void*)p.w2, 0, SD * SF * 2, 0x00020000);
        set_m0(L_W2S + w * 3072);
        sfor<3>([&](auto ii) {
            const unsigned P = 64 * (3 * w + ii) + lane, row = (P * 3277) >> 16, vo = row * (1280 - 320) + P * 16 - ii * 1024;
            dma<ii * 1024>(sel(ii == 0 || w < 3, vo, OOB), rw2, sks * (32 * 1280) + sqq * 320);
        });
        VMWAIT(10);
    } else VMWAIT(7);
    late_setup();
    STAMP(1);
    const f2 st = layernorm_rows(xa, xb, cx, [&]() { if (!spare) stage(ic<0>{}); }, [&]() { touch2(); if (!spare) stage(ic<1>{}); });
    if (!spare) VMWAIT(17); else VMWAIT(1);
    take_epoch(); tie(b1v);
    BARRIER();
    store_xn(cx, st, tb);
    unsigned ubase;

    if (!spare) {
        {
            const unsigned g4 = lane >> 4, a0 = opaque(L_W2S + (16 * (g4 >> 1) + 4 * (g4 & 1) + ((lane & 15) >> 2)) * 320 + 8 * (lane & 3) + w * 96);
            v4u fr[3];
            sfor<3>([&](auto ii) { const v2u lo = lds_tr(a0 + ii * 32), hi = lds_tr(a0 + ii * 32 + 8 * 320); fr[ii] = v4u{lo.x, lo.y, hi.x, hi.y}; });
            sfor<3>([&](auto ii) {
                const unsigned fi = 3 * w + ii;
                st128c<0>(fr[ii], cx.w16a, cx.rws, sel(fi < 10, OFF_W2T + (2 * sqq * NKS + sks) * 5120 + fi * 1024 + sel(fi >= 5, (NKS - 1) * 5120, 0), WS_USED));
            });
        }
        stage(ic<2>{});
        kg.ldt = half16(p.ldt, ch0);
        STAMP(2);
        unsigned ax[2][4], aw[2][4];
        {
            const unsigned row = 64 * wt + (lane & 31), hh = lane >> 5, blk = (lane >> 4) & 1, qr = (lane & 15) >> 2, pp = lane & 3;
            sfor<4>([&](auto ks) { ax[0][ks] = opaque(row * 128 + 16 * ((2 * ks + hh) ^ ((row >> 1) & 7))); ax[1][ks] = opaque(ax[0][ks] + 65536); });
            sfor<4>([&](auto i) {
                constexpr int mf = i >> 1, t = i & 1;
                const unsigned r = 8 * hh + 4 * t + qr, chn = 4 * (2 * wf + (mf ^ wt)) + 2 * blk + (pp >> 1);
                aw[0][i] = opaque(16384 + 256 * r + 16 * (chn ^ ((qr << 2) | ((2 * hh + t) & 3))) + 8 * (pp & 1)); aw[1][i] = opaque(aw[0][i] + 65536);
            });
        }
        const unsigned onesel = ((lane ^ (lane >> 4)) & 1) ? 0u : 0x3f803f80u;
        v4u ones = {onesel, onesel, onesel, onesel};
        tie(ones);
        constexpr int W7 = 4, W8 = 4, W9 = 4, WG0 = 12, WG1 = 28;
        f16v acc[2][2];
        f4 csum;
        v2u fa[2][4], plv, st0, st1;
        v4u fb[2][2], ub0, ub1;
        auto rd = [&](auto n_, auto i_) {
            constexpr int n = n_, i = i_, s = n >> 2, ks = n & 3, bf = n & 1, hi = (s & 3) >> 1;
            constexpr unsigned so = (s & 1) * 32768;
            if constexpr (n < 40) {
                if constexpr (i == 0) fb[bf][0] = lds_ld<v4u>(ax[hi][ks] + so);
                else if constexpr (i == 3) fb[bf][1] = lds_ld<v4u>(ax[hi][ks] + so + 4096);
                else fa[bf][i < 3 ? i - 1 : i - 2] = lds_tr(aw[hi][i < 3 ? i - 1 : i - 2] + so + ks * 4096);
            }
            PIN();
        };
        VMWAIT(24);
        BARRIER();
        sfor<6>([&](auto i) { rd(ic<0>{}, i); });
        piece(ic<3>{}, ic<0>{}); piece(ic<3>{}, ic<1>{});
        sfor<40>([&](auto n0_) {
            constexpr int n = n0_ + 1, s = n >> 2, ks = n & 3, bf = (n - 1) & 1;
            constexpr bool first = n == 1;
            if constexpr (ks == 0 && n < 40) {
                constexpr int WAITS[10] = {0, 23, 16, 17, 17, 16, 17, 16, 8 + 2 + W7, W7 + W8};
                VMWAIT(WAITS[s]);
                if constexpr (s == 7) { tie(plv); if (!flags_match(plv, cx)) poll<2>(OFF_F1 + tid * FSTR, cx); }
                BARRIER();
                if constexpr (s == 2) publish(cx, OFF_F1 + tb * FSTR);
                if constexpr (s == 5) plv = ld64c<0>(tid * FSTR, cx.rws, OFF_F1);
                if constexpr (s == 7) {
                    const unsigned vo = (64 * wt + (lane & 31)) * 8;
                    st0 = ld64c<0>(vo, cx.rws, OFF_STATS + ti * 1024); st1 = ld64c<256>(vo, cx.rws, OFF_STATS + ti * 1024);
                }
                if constexpr (s == 8) kg_set_base(cx);
                PIN();
            }
            auto vm = [&](auto m_) {
                constexpr int pi = 2 * ks + m_;
                if constexpr (s <= 6) piece(ic<s + 3>{}, ic<pi>{});
                else if constexpr (s <= 9 && m_ == 0) w2_prefetch<(s - 7) * 4 + ks, (s - 7) * 4 + ks + 1>(cx);
                PIN();
            };
            auto side = [&](auto m_) {
                constexpr int m = m_;
                if constexpr (n <= 6) { if constexpr (m == 0) kg_step<n - 1, true>(kg); }
                else if constexpr (n < 32) { kg_step<6 + 2 * (n - 7) + m, true>(kg); if constexpr (n == 31 && m == 1) kg_step<56, true>(kg); }
                else if constexpr (n < 40) kg_write<(n - 32) * 7 + 4 * m, (n - 32) * 7 + 4 + 3 * m>(kg);
                PIN();
            };
            const v4u a0 = {fa[bf][0].x, fa[bf][0].y, fa[bf][1].x, fa[bf][1].y}, a1 = {fa[bf][2].x, fa[bf][2].y, fa[bf][3].x, fa[bf][3].y};
            if constexpr (first) mfma32z(acc[0][0], a0, fb[bf][0]); else mfma32(acc[0][0], a0, fb[bf][0]);
            PIN(); rd(ic<n>{}, ic<0>{}); rd(ic<n>{}, ic<1>{}); rd(ic<n>{}, ic<2>{}); rd(ic<n>{}, ic<3>{});
            if constexpr (first) mfma32z(acc[0][1], a0, fb[bf][1]); else mfma32(acc[0][1], a0, fb[bf][1]);
            PIN(); rd(ic<n>{}, ic<4>{}); rd(ic<n>{}, ic<5>{}); vm(ic<0>{});
            if constexpr (first) mfma16z(csum, a0, ones); else mfma16(csum, a0, ones);
            if constexpr (first) mfma32z(acc[1][0], a1, fb[bf][0]); else mfma32(acc[1][0], a1, fb[bf][0]);
            PIN(); side(ic<0>{}); vm(ic<1>{});
            if constexpr (first) mfma32z(acc[1][1], a1, fb[bf][1]); else mfma32(acc[1][1], a1, fb[bf][1]);
            PIN(); side(ic<1>{});
        });
        STAMP(3);
        tie(csum);
        lds_st<f4>(L_CS + (64 * wf + 32 * wt + 16 * (lane & 1) + 4 * (lane >> 4)) * 4, csum);
        if (w < 2) lds_st<float>(L_B1 + tid * 4, blo(b1v));
        tie(st0); tie(st1);
        {
            const unsigned uvo = (tid >> 1) * 10240 + (tid & 1) * 16;
            ub0 = ld128c<0>(uvo, cx.rws, OFF_XNB + ch0 * 16); ub1 = ld128c<0>(uvo, cx.rws, OFF_XNB + 128 * 10240 + ch0 * 16);
        }
        BARRIER();
        MFMA_DRAIN();
        {
            const unsigned hh = lane >> 5;
            const unsigned hvo = sel(tj == 13 && wf == 1, OOB, ((lane >> 4) & 1) * (NKS * 1024) + (hh * 16 + (lane & 15)) * 16);
            const unsigned hso = OFF_H + ((8 * ti + 4 * wt) * NKS + 4 * tj + 2 * wf) * 1024;
            const unsigned cam[2] = {opaque(L_CS + (64 * wf + 32 * wt + 4 * hh) * 4), opaque(L_CS + (64 * wf + 32 * (wt ^ 1) + 4 * hh) * 4)};
            constexpr float C1 = -2.302208198f, C2 = -0.1029432397f;
            sfor<2>([&](auto mf_) {
                constexpr int mf = mf_;
                f4 cs[4], bb[4];
                sfor<4>([&](auto ga) { cs[ga] = lds_ld<f4>(cam[mf] + 32 * ga); bb[ga] = lds_ld<f4>(cam[mf] + 512 + 32 * ga); });
                sfor<2>([&](auto nt_) {
                    constexpr int nt = nt_;
                    tie(acc[mf][nt]);
                    const v2u sv = nt ? st1 : st0;
                    const float sa = __uint_as_float(sv.x), sb = __uint_as_float(sv.y);
                    sfor<2>([&](auto chk) {
                        f2 z[4], e[4];
                        sfor<4>([&](auto i) {
                            constexpr int ga = 2 * chk + (i >> 1), e2 = 2 * (i & 1);
                            z[i] = f2{acc[mf][nt][4 * ga + e2], acc[mf][nt][4 * ga + e2 + 1]} * sa + (f2{cs[ga][e2], cs[ga][e2 + 1]} * sb + f2{bb[ga][e2], bb[ga][e2 + 1]});
                        });
                        sfor<4>([&](auto i) { e[i] = z[i] * (z[i] * z[i] * C2 + C1); });
                        sfor<4>([&](auto i) { e[i] = f2{__builtin_amdgcn_exp2f(e[i].x), __builtin_amdgcn_exp2f(e[i].y)}; });
                        sfor<4>([&](auto i) { e[i] = e[i] + 1.f; });
                        sfor<4>([&](auto i) { e[i] = f2{__builtin_amdgcn_rcpf(e[i].x), __builtin_amdgcn_rcpf(e[i].y)}; });
                        unsigned o[4];
                        sfor<4>([&](auto i) { const f2 r = z[i] * e[i]; o[i] = pk(r.x, r.y); });
                        constexpr int idx = 4 * mf + 2 * nt + chk, f0 = WG0 + (WG1 - WG0) * idx / 8, f1 = WG0 + (WG1 - WG0) * (idx + 1) / 8;
                        w2_prefetch<f0, f1>(cx);
                        st128c<chk * 512>(v4u{o[0], o[1], o[2], o[3]}, hvo, cx.rws, hso + (2 * nt * NKS + (mf ^ wt)) * 1024);
                        PIN();
                    });
                });
            });
        }
        STAMP(4);
        w2_prefetch<WG1, WG1 + 3>(cx);
        kernel_table(cx);
        w2_prefetch<WG1 + 3, WG1 + 6>(cx);
        BARRIER();
        const unsigned r0 = L_RC + cidx * RC_CH;
        expand_taps(cx, r0);
        VMWAIT(6);
        tie(ub0); tie(ub1);
        {
            const unsigned ua = opaque(L_U + 480 + (tid & 1) * U_CH + (tid >> 1) * 16);
            lds_st<v4u>(ua, ub0); lds_st<v4u>(ua + 2048, ub1);
            lds_st<v2u>(L_U + (tid >> 7) * U_CH + sel((tid & 127) < 60, tid & 127, 59) * 8, v2u{0, 0});
        }
        BARRIER();
        publish(cx, OFF_F2 + (16 * ti + tj) * FSTR);
        STAMP(5);
        w2_prefetch<WG1 + 6, 40>(cx);
        shift_taps<false>(cx, r0);
        ubase = L_U + cidx * U_CH;
    } else {
        STAMP(2);
        kg_set_base(cx);
#pragma unroll 1
        for (unsigned round = 0; round < 3; ++round) {
            CYC(0);
            kg.ldt = half16(p.ldt, ch0 + 2 * round);
            sfor<2>([&](auto i) { kg_step<i, false>(kg); });
            CYC(1);
            PIN();
            const unsigned cn = (ch0 + 2 * round + 2) * 256;
            kg.la = ld16(pvo, rla, cn); kg.ai = ld16(pvo, rai, cn); kg.cr = ld16(pvo, rcr, cn); kg.ci = ld16(pvo, rci, cn);
            PIN();
            sfor<4>([&](auto i) { kg_step<i + 2, false>(kg); });
            CYC(2);
            sfor<KG_STEPS - 6>([&](auto i) { kg_step<i + 6, false>(kg); });
            CYC(3);
            VMWAIT(0);
            CYC(4);
            tie(kg.la); tie(kg.ai); tie(kg.cr); tie(kg.ci);
            BARRIER();
            CYC(5);
            if (round == 0) publish(cx, OFF_F1 + tb * FSTR);
            kernel_table(cx);
            CYC(6);
            BARRIER();
            CYC(7);
            expand_taps(cx, L_R0 + (2 * round + cidx) * RC_COPY);
            CYC(8);
        }
        STAMP(3);
        poll<4>(OFF_F1 + tid * FSTR, cx);
        BARRIER();
        STAMP(4);
        {
            v4u ub[6];
            unsigned ua[6];
            sfor<6>([&](auto i) {
                const unsigned P = tid + 256 * i, blk = (P * 10923) >> 16, c = P - 6 * blk;
                ub[i] = ld128c<0>(blk * 10240 + c * 16, cx.rws, OFF_XNB + ch0 * 16);
                ua[i] = L_U6 + 480 + c * U_CH + blk * 16;
            });
            w2_prefetch<0, 4>(cx);
            shift_taps<true>(cx, L_R0 + cidx * RC_COPY);
            sfor<3>([&](auto i) { lds_st<v2u>(L_U6 + (2 * i + (tid >> 7)) * U_CH + sel((tid & 127) < 60, tid & 127, 59) * 8, v2u{0, 0}); });
            VMWAIT(4);
            sfor<6>([&](auto i) { tie(ub[i]); lds_st<v4u>(ua[i], ub[i]); });
        }
        STAMP(5);
#pragma unroll 1
        for (unsigned round = 0; round < 2; ++round) {
            const float dsk = blo(half16(p.dsk, ch0 + 2 * round));
            BARRIER();
            conv_pair(cx, ch0 + 2 * round + cidx, L_U6 + (2 * round + cidx) * U_CH, dsk, [&](auto d) {
                if (round == 0) w2_prefetch<4 + 9 * d / 2, 4 + 9 * (d + 1) / 2>(cx); else w2_prefetch<22 + 9 * d / 2, 22 + 9 * (d + 1) / 2>(cx);
            }, [&]() {});
            shift_taps<true>(cx, L_R0 + (2 * round + 2 + cidx) * RC_COPY);
        }
        ubase = L_U6 + (4 + cidx) * U_CH;
    }
    cx.w2so = rfl(cx.w2so); cx.w2l = rfl(cx.w2l); cx.w2a = rfl(cx.w2a); cx.w2b = rfl(cx.w2b); cx.thr = rfl(cx.thr); cx.e_lo = rfl(cx.e_lo); cx.e_hi = rfl(cx.e_hi);
    ubase = rfl(ubase);
    const unsigned chl = rfl(ch0 + sel(spare, 4, 0));
    const float dskip = blo(half16(p.dsk, chl));
    BARRIER();
    STAMP(6);

    constexpr int WIN = 4;
    v4u wx[6], hf[WIN][4];
    unsigned hv[4], hso;
    sfor<4>([&](auto t) { hv[t] = (4 * rt + ((t + w) & 3)) * (NKS * 1024) + lane * 16; });
    const unsigned hsa = OFF_H + k0s * 1024, hsb = hsa - nk * 1024, hsl = sel(w < 2, OFF_H + klast * 1024, WS_USED);
    auto ldh = [&](auto s_, auto t_) {
        constexpr int s = s_, t = t_;
        if constexpr (s < 14) {
            if constexpr (t == 0) hso = slot_off<s, 1024>(hsa, hsb, hsl, cx.thr);
            hf[s % WIN][t] = ld128c<0>(hv[t], cx.rws, hso);
        }
        PIN();
    };
    {
        const unsigned f2vo = OFF_F2 + (16 * (rt >> 1) + sel(lane < 14, lane, 0)) * FSTR;
        v2u plv2;
        conv_pair(cx, chl + cidx, ubase, dskip,
            [&](auto d) {
                if constexpr (d == 2) plv2 = ld64c<0>(f2vo, cx.rws, 0);
                if constexpr (d < 3) w2_prefetch<40 + 8 * d, 48 + 8 * d>(cx);
            },
            [&]() {
                VMWAIT(8);
                tie(plv2);
                if (!flags_match(plv2, cx)) poll<1>(f2vo, cx);
                sfor<WIN - 1>([&](auto s) { sfor<4>([&](auto t) { ldh(s, t); }); });
                wx[0] = ld128c<0>(cx.w16b, cx.rws, cx.w2so);
                sfor<4>([&](auto c) { wx[1 + c] = ld128c<c * 1024>(cx.w16a, cx.rws, cx.w2l); });
                wx[5] = ld128c<0>(cx.w16b, cx.rws, cx.w2l);
                STAMP(8);
            });
    }
    STAMP(9);
    f4 acc2[5][4];
    v2u xo[5], bo[5], plv3;
    v4u sv[3];
    const unsigned xvo = (16 * w + (lane & 15)) * (SD * 2) + 8 * (lane >> 4), xso = 64 * rt * (SD * 2) + 160 * ct;
    {
        const unsigned f3so = rfl(OFF_F3 + tb * FSTR);
        const rsrc_t rb2 = __builtin_amdgcn_make_buffer_rsrc((void*)p.b2, 0, SD * 2, 0x00020000);
        const unsigned svo = (tid >> 3) * 4096 + (tid & 7) * 16;
        sfor<14>([&](auto s_) {
            constexpr int s = s_;
            constexpr int later = (s + WIN - 2 < 14 ? WIN - 2 : 13 - s) * 4;
            constexpr int extra = s <= 2 ? 8 : (s == 4 || s == 5 || s == 7 || s == 8) ? 1 : (s == 10 || s == 11) ? 13 : 0;
            VMWAIT(later + extra);
            sfor<4>([&](auto t) { tie(hf[s % WIN][t]); });
            if constexpr (s == 3) { sfor<6>([&](auto i) { tie(wx[i]); }); BARRIER(); publish(cx, f3so); }
            if constexpr (s == 6) plv3 = ld64c<0>((lane & 31) * FSTR, cx.rws, OFF_F3 + ct * (32 * FSTR));
            if constexpr (s == 9) {
                tie(plv3);
                if (!flags_match(plv3, cx)) poll<1>(OFF_F3 + ct * (32 * FSTR) + (lane & 31) * FSTR, cx);
                const unsigned so = OFF_ST + 80 * ct * 4096 + 128 * rt;
                sv[0] = ld128c<0>(svo, cx.rws, so); sv[1] = ld128c<0>(svo, cx.rws, so + 32 * 4096); sv[2] = ld128c<0>(svo, cx.rws, sel(w < 2, so + 64 * 4096, WS_USED));
                sfor<5>([&](auto c) { xo[c] = ld64<c * 32>(xvo, rx, xso); });
                sfor<5>([&](auto c) { bo[c] = ld64<c * 32>(8 * (lane >> 4), rb2, 160 * ct); });
            }
            PIN();
            sfor<5>([&](auto c_) {
                constexpr int c = c_, f = 5 * s + c;
                sfor<4>([&](auto t) {
                    if constexpr (f >= 64) mfma16(acc2[c][t], wx[f - 64], hf[s % WIN][t]);
                    else if constexpr (s == 0) mfma16az<4 * f>(acc2[c][t], hf[s % WIN][t]);
                    else mfma16a<4 * f>(acc2[c][t], hf[s % WIN][t]);
                });
                PIN();
                if constexpr (c < 4) ldh(ic<s + WIN - 1>{}, c_);
            });
            if constexpr (s == 12) {
                sfor<3>([&](auto ii) { tie(sv[ii]); });
                const unsigned sa = opaque(L_SIMG + (tid >> 3) * SIMG_ROW + (tid & 7) * 16);
                lds_st<v4u>(sa, sv[0]); lds_st<v4u>(sa + 32 * SIMG_ROW, sv[1]); lds_st<v4u>(sa + 64 * SIMG_ROW, sv[2]);
                PIN();
            }
        });
    }
    STAMP(10);
    sfor<5>([&](auto c) { tie(xo[c]); tie(bo[c]); });
    unsigned rw[3];
    sfor<3>([&](auto t1) { rw[t1] = opaque(L_RED + ((w + t1 + 1) & 3) * 15360 + t1 * 5120 + lane * 16); });
    const unsigned ra = opaque(L_RED + w * 15360 + lane * 16);
    const unsigned sa = opaque(L_SIMG + (4 * (lane >> 4) + ((lane & 15) >> 2)) * SIMG_ROW + 32 * w + 8 * (lane & 3));
    PIN(); asm volatile("s_nop 7"); PIN();
    sfor<5>([&](auto c) { sfor<3>([&](auto t1) { tie(acc2[c][t1 + 1]); lds_st<f4>(rw[t1] + c * 1024, acc2[c][t1 + 1]); }); });
    f4 zb[5], xf[5];
    sfor<5>([&](auto c) {
        tie(acc2[c][0]);
        zb[c] = acc2[c][0] + f4{blo(bo[c].x), bhi(bo[c].x), blo(bo[c].y), bhi(bo[c].y)};
        xf[c] = f4{blo(xo[c].x), bhi(xo[c].x), blo(xo[c].y), bhi(xo[c].y)};
    });
    BARRIER();
    {
        const rsrc_t ro = __builtin_amdgcn_make_buffer_rsrc((void*)p.out, 0, SL * SD * 2, 0x00020000);
        sfor<5>([&](auto c) {
            const f4 z = (zb[c] + lds_ld<f4>(ra + c * 1024)) + (lds_ld<f4>(ra + 5120 + c * 1024) + lds_ld<f4>(ra + 10240 + c * 1024));
            const v2u s2 = lds_tr(sa + c * 16 * SIMG_ROW);
            const f4 u = z * -1.44269504088896f;
            const f4 d = f4{__builtin_amdgcn_exp2f(u[0]), __builtin_amdgcn_exp2f(u[1]), __builtin_amdgcn_exp2f(u[2]), __builtin_amdgcn_exp2f(u[3])} + 1.f;
            const f4 sz = f4{blo(s2.x), bhi(s2.x), blo(s2.y), bhi(s2.y)} * z;
            const f4 o = sz * f4{__builtin_amdgcn_rcpf(d[0]), __builtin_amdgcn_rcpf(d[1]), __builtin_amdgcn_rcpf(d[2]), __builtin_amdgcn_rcpf(d[3])} + xf[c];
            st64<c * 32>(v2u{pk(o[0], o[1]), pk(o[2], o[3])}, xvo, ro, xso);
        });
    }
    if (b == PROBE_WG) {
        u64 t0, t1;
        asm volatile("s_memrealtime %0\n\ts_waitcnt lgkmcnt(0)" : "=s"(t0));
        const u64 d = (pr1 - pr0) * PROBE_MUL;
        do { asm volatile("s_memrealtime %0\n\ts_waitcnt lgkmcnt(0)" : "=s"(t1)); } while (t1 - t0 < d);
    }
    if (b == 0) publish(cx, OFF_W);
}

extern "C" void kernel_launch(void* const* d_in, const int* in_sizes, int n_in, void* d_out, int out_size, void* d_ws, size_t ws_size, hipStream_t stream) {
    Params p;
    p.x = (const u16*)d_in[0]; p.lar = (const u16*)d_in[1]; p.aim = (const u16*)d_in[2]; p.cr = (const u16*)d_in[3]; p.ci = (const u16*)d_in[4];
    p.ldt = (const u16*)d_in[5]; p.dsk = (const u16*)d_in[6]; p.w1 = (const u16*)d_in[8]; p.b1 = (const u16*)d_in[9];
    p.w2 = (const u16*)d_in[10]; p.b2 = (const u16*)d_in[11]; p.out = (u16*)d_out; p.ws = (char*)d_ws;
    (void)hipFuncSetAttribute((const void*)s4d_mixer_kernel, hipFuncAttributeMaxDynamicSharedMemorySize, LDS_BYTES);
    s4d_mixer_kernel<<<256, 256, LDS_BYTES, stream>>>(p);
}
```

```cpp
#define PROBE_KIND 0
#define PROBE_MUL 0
#define PROBE_WG 255
#include <hip/hip_runtime.h>
#include <stdint.h>
#include <utility>

typedef unsigned short u16;
typedef unsigned long long u64;
typedef unsigned v4u __attribute__((ext_vector_type(4)));
typedef unsigned v2u __attribute__((ext_vector_type(2)));
typedef float f2 __attribute__((ext_vector_type(2)));
typedef float f4 __attribute__((ext_vector_type(4)));
typedef float f16v __attribute__((ext_vector_type(16)));
typedef short s4v __attribute__((ext_vector_type(4)));
typedef __bf16 bf2v __attribute__((ext_vector_type(2)));
typedef __amdgpu_buffer_rsrc_t rsrc_t;
#define LAS __attribute__((address_space(3)))
#define CAS __attribute__((address_space(4)))
#define DI __device__ __forceinline__
#define PIN() __builtin_amdgcn_sched_barrier(0)
#define BARRIER() do { PIN(); asm volatile("s_waitcnt lgkmcnt(0)\n\ts_barrier" ::: "memory"); PIN(); } while (0)
#define VMWAIT(n) do { PIN(); asm volatile("s_waitcnt vmcnt(%0)" :: "i"(n) : "memory"); PIN(); } while (0)

#define FSTR 32
#define STAMPS 0

constexpr int SL = 2048, SD = 640, SF = 1728, NKS = SF / 32;
constexpr unsigned OOB = 0x40000000u;
constexpr unsigned OFF_W = 0, OFF_TMO = 128, OFF_F1 = 4096, OFF_F2 = OFF_F1 + 256 * FSTR, OFF_F3 = OFF_F2 + 256 * FSTR, OFF_DBG = 0x1000000;
constexpr unsigned OFF_STATS = 114688, OFF_XNB = 131072, OFF_W2T = OFF_XNB + SL * SD * 2, OFF_ST = OFF_W2T + SF * SD * 2;
constexpr unsigned OFF_H = OFF_ST + SL * SD * 2, WS_USED = OFF_H + SL * SF * 2;
constexpr unsigned CODE_LINES = 256;
static_assert(OFF_F3 + 256 * FSTR <= OFF_STATS, "workspace map");
constexpr unsigned SPIN_CAP = 1u << 16;

constexpr unsigned L_AUX = 131072, L_LNT = L_AUX, L_W2S = L_AUX + 10240, L_CS = L_AUX + 22528, L_B1 = L_CS + 512;
constexpr unsigned L_XY = 65536, XY_ROW = 528, XY_X = 32 * XY_ROW, XY_CH = 28160;
constexpr unsigned L_KP = 0, KP_CH = 4608, L_RC = L_KP + 2 * KP_CH, RC_COPY = 4160, RC_CH = 4 * RC_COPY;
constexpr unsigned L_U = L_RC + 2 * RC_CH, U_CH = 4608, L_RED2 = L_XY, L_R0 = L_XY + 2 * XY_CH;
constexpr unsigned L_U6 = L_XY + 16384, XY_BIAS = 38000;
constexpr unsigned L_SIMG = L_AUX, SIMG_ROW = 144, L_RED = 0;
constexpr unsigned LDS_BYTES = 163840;
static_assert(L_U + 2 * U_CH <= L_XY && L_R0 + 6 * RC_COPY <= LDS_BYTES && L_R0 <= L_AUX, "LDS map");

struct Params {
    const u16 *x, *lar, *aim, *cr, *ci, *ldt, *dsk, *w1, *b1, *w2, *b2;
    u16* out;
    char* ws;
};

template <int... Is, class Fn> DI void sfor_impl(std::integer_sequence<int, Is...>, Fn&& fn) { (fn(std::integral_constant<int, Is>{}), ...); }
template <int N, class Fn> DI void sfor(Fn&& fn) { sfor_impl(std::make_integer_sequence<int, N>{}, fn); }

DI unsigned rfl(unsigned v) { return __builtin_amdgcn_readfirstlane(v); }
DI unsigned sel(bool c, unsigned a, unsigned b) { return c ? a : b; }
DI unsigned opaque(unsigned v) { asm volatile("" : "+v"(v)); return v; }
DI float blo(unsigned d) { return __uint_as_float(d << 16); }
DI float bhi(unsigned d) { return __uint_as_float(d & 0xffff0000u); }
DI unsigned pk(float lo, float hi) { f2 v = {lo, hi}; return __builtin_bit_cast(unsigned, __builtin_convertvector(v, bf2v)); }
DI float rbf(float v) { return blo(pk(v, v)); }
DI float dot2(unsigned a, unsigned b, float c) { return __builtin_amdgcn_fdot2_f32_bf16(__builtin_bit_cast(bf2v, a), __builtin_bit_cast(bf2v, b), c, false); }
template <class T> DI T lds_ld(unsigned a) { return *(const LAS T*)(size_t)a; }
template <class T> DI void lds_st(unsigned a, T v) { *(LAS T*)(size_t)a = v; }
DI v2u lds_tr(unsigned a) { return __builtin_bit_cast(v2u, __builtin_amdgcn_ds_read_tr16_b64_v4i16((LAS s4v*)(size_t)a)); }

template <int CTRL> DI float dpp_add(float v) {
    return v + __builtin_bit_cast(float, __builtin_amdgcn_update_dpp(0, __builtin_bit_cast(int, v), CTRL, 0xf, 0xf, true));
}
DI float pair_sum(float a, float b) {
    const auto r = __builtin_amdgcn_permlane32_swap(__float_as_uint(a), __float_as_uint(b), false, false);
    float v = __uint_as_float(r[0]) + __uint_as_float(r[1]);
    v = dpp_add<0xB1>(v); v = dpp_add<0x4E>(v); v = dpp_add<0x141>(v); v = dpp_add<0x140>(v);
    return dpp_add<0x142>(v);
}
template <int LANE> DI float rdl(float v) { return __builtin_bit_cast(float, __builtin_amdgcn_readlane(__builtin_bit_cast(int, v), LANE)); }
DI float inv_sd(float var) { return __builtin_amdgcn_rcpf(rbf(__builtin_amdgcn_sqrtf(rbf(rbf(fmaxf(var, 0.f)) + 1e-5f)))); }

template <int IMM> DI void dma(unsigned vo, rsrc_t rs, unsigned so) {
    asm volatile("buffer_load_dwordx4 %0, %1, %2 offen offset:%3 lds" :: "v"(vo), "s"(rs), "s"(so), "i"(IMM));
}
DI void set_m0(unsigned v) { asm volatile("s_mov_b32 m0, %0\n\ts_nop 0" :: "s"(v)); }
template <int IMM> DI unsigned ld32(unsigned vo, rsrc_t rs, unsigned so) { unsigned r; asm volatile("buffer_load_dword %0, %1, %2, %3 offen offset:%4" : "=v"(r) : "v"(vo), "s"(rs), "s"(so), "i"(IMM)); return r; }
DI unsigned ld16(unsigned vo, rsrc_t rs, unsigned so) { unsigned r; asm volatile("buffer_load_ushort %0, %1, %2, %3 offen" : "=v"(r) : "v"(vo), "s"(rs), "s"(so)); return r; }
template <int IMM> DI v2u ld64(unsigned vo, rsrc_t rs, unsigned so) { v2u r; asm volatile("buffer_load_dwordx2 %0, %1, %2, %3 offen offset:%4" : "=v"(r) : "v"(vo), "s"(rs), "s"(so), "i"(IMM)); return r; }
template <int IMM> DI v2u ld64c(unsigned vo, rsrc_t rs, unsigned so) { v2u r; asm volatile("buffer_load_dwordx2 %0, %1, %2, %3 offen offset:%4 sc1" : "=v"(r) : "v"(vo), "s"(rs), "s"(so), "i"(IMM)); return r; }
template <int IMM> DI v4u ld128c(unsigned vo, rsrc_t rs, unsigned so) { v4u r; asm volatile("buffer_load_dwordx4 %0, %1, %2, %3 offen offset:%4 sc1" : "=v"(r) : "v"(vo), "s"(rs), "s"(so), "i"(IMM)); return r; }
template <int A, int IMM> DI void ld128a(unsigned vo, rsrc_t rs, unsigned so) {
    asm volatile("buffer_load_dwordx4 a[%c3:%c4], %0, %1, %2 offen offset:%5 sc1" :: "v"(vo), "s"(rs), "s"(so), "i"(A), "i"(A + 3), "i"(IMM));
}
template <int IMM> DI void st128c(v4u v, unsigned vo, rsrc_t rs, unsigned so) { asm volatile("buffer_store_dwordx4 %0, %1, %2, %3 offen offset:%4 sc1\n\ts_nop 1" :: "v"(v), "v"(vo), "s"(rs), "s"(so), "i"(IMM)); }
DI void st64c(v2u v, unsigned vo, rsrc_t rs, unsigned so) { asm volatile("buffer_store_dwordx2 %0, %1, %2, %3 offen sc1" :: "v"(v), "v"(vo), "s"(rs), "s"(so)); }
template <int IMM> DI void st64(v2u v, unsigned vo, rsrc_t rs, unsigned so) { asm volatile("buffer_store_dwordx2 %0, %1, %2, %3 offen offset:%4" :: "v"(v), "v"(vo), "s"(rs), "s"(so), "i"(IMM)); }
template <int IMM> DI void lds_st_tid(unsigned v) { asm volatile("ds_write_addtid_b32 %0 offset:%1" :: "v"(v), "i"(IMM)); }
DI void l2_touch(unsigned vo, rsrc_t rs, unsigned so) { asm volatile("buffer_load_dword a255, %0, %1, %2 offen" :: "v"(vo), "s"(rs), "s"(so)); }
template <class T> DI void tie(T& v) { asm volatile("" : "+v"(v)); }

DI void mfma32(f16v& c, v4u a, v4u b) { asm volatile("v_mfma_f32_32x32x16_bf16 %0, %1, %2, %0" : "+v"(c) : "v"(a), "v"(b)); }
DI void mfma16(f4& c, v4u a, v4u b) { asm volatile("v_mfma_f32_16x16x32_bf16 %0, %1, %2, %0" : "+v"(c) : "v"(a), "v"(b)); }
DI void mfma16z(f4& c, v4u a, v4u b) { asm volatile("v_mfma_f32_16x16x32_bf16 %0, %1, %2, 0" : "=&v"(c) : "v"(a), "v"(b)); }
DI void mfma32z(f16v& c, v4u a, v4u b) { asm volatile("v_mfma_f32_32x32x16_bf16 %0, %1, %2, 0" : "=&v"(c) : "v"(a), "v"(b)); }
template <int A> DI void mfma16a(f4& c, v4u b) { asm volatile("v_mfma_f32_16x16x32_bf16 %0, a[%c2:%c3], %1, %0" : "+v"(c) : "v"(b), "i"(A), "i"(A + 3)); }
template <int A> DI void mfma16az(f4& c, v4u b) { asm volatile("v_mfma_f32_16x16x32_bf16 %0, a[%c2:%c3], %1, 0" : "=&v"(c) : "v"(b), "i"(A), "i"(A + 3)); }
#define MFMA_DRAIN() do { PIN(); asm volatile("s_nop 15" ::: "memory"); PIN(); } while (0)

DI f2 cmul(f2 p, f2 w) {
    f2 r;
    asm volatile("v_pk_mul_f32 %0, %1, %2 op_sel:[0,0] op_sel_hi:[0,1]\n\tv_pk_fma_f32 %0, %1, %2, %0 op_sel:[1,1,0] op_sel_hi:[1,0,1] neg_lo:[0,1,0]" : "=&v"(r) : "v"(p), "v"(w));
    return r;
}
DI f2 cmul_s(f2 p, f2 w) {
    float a, b;
    asm volatile("v_mul_f32 %0, %2, %4\n\tv_mul_f32 %1, %2, %5\n\tv_fma_f32 %0, -%3, %5, %0\n\tv_fma_f32 %1, %3, %4, %1" : "=&v"(a), "=&v"(b) : "v"(p.x), "v"(p.y), "v"(w.x), "v"(w.y));
    return f2{a, b};
}
DI float fmul_s(float a, float b) { float r; asm("v_mul_f32 %0, %1, %2" : "=v"(r) : "v"(a), "v"(b)); return r; }
DI f2 cexp_at(float er, float rev, float p) {
    const float m = __builtin_amdgcn_exp2f(er * p), fr = __builtin_amdgcn_fractf(rev * p);
    const float c = __builtin_amdgcn_cosf(fr) * m;
    float s = __builtin_amdgcn_sinf(fr);
    asm volatile("" : "+v"(s));
    return f2{c, s * m};
}

#define ACLOB_16(n) "a" #n "0", "a" #n "1", "a" #n "2", "a" #n "3", "a" #n "4", "a" #n "5", "a" #n "6", "a" #n "7", "a" #n "8", "a" #n "9"
#define ACLOB_ALL "a0", "a1", "a2", "a3", "a4", "a5", "a6", "a7", "a8", "a9", ACLOB_16(1), ACLOB_16(2), ACLOB_16(3), ACLOB_16(4), ACLOB_16(5), ACLOB_16(6), \
    ACLOB_16(7), ACLOB_16(8), ACLOB_16(9), ACLOB_16(10), ACLOB_16(11), ACLOB_16(12), ACLOB_16(13), ACLOB_16(14), ACLOB_16(15), ACLOB_16(16), ACLOB_16(17), \
    ACLOB_16(18), ACLOB_16(19), ACLOB_16(20), ACLOB_16(21), ACLOB_16(22), ACLOB_16(23), ACLOB_16(24), "a250", "a251", "a252", "a253", "a254", "a255"

struct Ctx {
    rsrc_t rws;
    unsigned e_lo, e_hi;
    unsigned tid, lane, w, w16a, w16b, flag_vo;
    unsigned thr, w2a, w2b, w2l, w2so;
};
#define STAMP(i) do {} while (0)
#define CYC(i) do {} while (0)
template <int N> using ic = std::integral_constant<int, N>;

DI bool flags_match(v2u v, const Ctx& cx) { return __all(v.x == cx.e_lo && v.y == cx.e_hi); }
DI void publish(const Ctx& cx, unsigned so) { st64c(v2u{cx.e_lo, cx.e_hi}, cx.flag_vo, cx.rws, so); }
template <int SLEEP> DI void poll(unsigned vo, const Ctx& cx) {
#pragma unroll 1
    for (unsigned spins = 0;; ++spins) {
        v2u v = ld64c<0>(vo, cx.rws, 0);
        VMWAIT(0); tie(v);
        if (flags_match(v, cx)) break;
        if (spins > SPIN_CAP) { st64c(v2u{1u, 1u}, cx.lane == 0 ? OFF_TMO : OOB, cx.rws, 0); break; }
        __builtin_amdgcn_s_sleep(SLEEP);
    }
}
template <int S, int STRIDE> DI unsigned slot_off(unsigned base, unsigned wrapped, unsigned last, unsigned thr) {
    if constexpr (S == 13) return last;
    unsigned r;
    asm volatile("s_cmp_le_u32 %3, %4\n\ts_cselect_b32 %0, %2, %1\n\ts_add_u32 %0, %0, %5" : "=&s"(r) : "s"(base), "s"(wrapped), "s"(thr), "i"(S), "i"(S * STRIDE) : "scc");
    return r;
}
template <int LO, int HI> DI void w2_prefetch(Ctx& cx) {
    sfor<HI - LO>([&](auto ii) {
        constexpr int f = LO + ii, s = f / 5, c = f % 5;
        if constexpr (c == 0) cx.w2so = slot_off<s, 5120>(cx.w2a, cx.w2b, cx.w2l, cx.thr);
        if constexpr (c < 4) ld128a<4 * f, c * 1024>(cx.w16a, cx.rws, cx.w2so); else ld128a<4 * f, 0>(cx.w16b, cx.rws, cx.w2so);
    });
}

template <class H1, class H2> DI f2 layernorm_rows(const unsigned (&xa)[5], const unsigned (&xb)[5], const Ctx& cx, H1&& hook1, H2&& hook2) {
    constexpr unsigned ONES = 0x3f803f80u;
    float sa = 0.f, sb = 0.f;
    sfor<5>([&](auto i) { sa = dot2(xa[i], ONES, sa); sb = dot2(xb[i], ONES, sb); });
    const float mean = pair_sum(sa, sb) * (1.f / SD), mr = rbf(mean), dm = mean - mr;
    const float mra = rdl<31>(mr), mrb = rdl<63>(mr);
    PIN(); hook1(); PIN();
    unsigned ta[5], tb[5];
    f2 qa = {0.f, 0.f}, qb = {0.f, 0.f};
    sfor<5>([&](auto i) {
        const f2 da = f2{blo(xa[i]), bhi(xa[i])} - mra, db = f2{blo(xb[i]), bhi(xb[i])} - mrb;
        qa = da * da + qa; qb = db * db + qb;
        ta[i] = pk(da.x, da.y); tb[i] = pk(db.x, db.y);
    });
    const float r1 = inv_sd(pair_sum(qa.x + qa.y, qb.x + qb.y) * (1.f / SD) - dm * dm);
    const float ra = rdl<31>(r1), rb = rdl<63>(r1);
    PIN(); hook2(); PIN();
    float s0 = 0.f, s1 = 0.f, s2 = 0.f, s3 = 0.f;
    unsigned o[10];
    sfor<5>([&](auto i) {
        const f2 na = f2{blo(ta[i]), bhi(ta[i])} * ra, nb = f2{blo(tb[i]), bhi(tb[i])} * rb;
        const unsigned ua = pk(na.x, na.y), ub = pk(nb.x, nb.y);
        s0 = dot2(ua, ONES, s0); s1 = dot2(ua, ua, s1); s2 = dot2(ub, ONES, s2); s3 = dot2(ub, ub, s3);
        o[2 * i] = __builtin_amdgcn_perm(ub, ua, 0x05040100u); o[2 * i + 1] = __builtin_amdgcn_perm(ub, ua, 0x07060302u);
    });
    const unsigned la = opaque(L_LNT + cx.w * 2560 + cx.lane * 16), lb = opaque(L_LNT + cx.w * 2560 + 2048 + cx.lane * 8);
    lds_st<v4u>(la, v4u{o[0], o[1], o[2], o[3]});
    lds_st<v4u>(la + 1024, v4u{o[4], o[5], o[6], o[7]});
    lds_st<v2u>(lb, v2u{o[8], o[9]});
    const float m2 = pair_sum(s0, s2) * (1.f / SD), r2 = inv_sd(pair_sum(s1, s3) * (1.f / SD) - m2 * m2);
    return f2{r1 * r2, -(mr * r1 + rbf(m2)) * r2};
}
DI void store_xn(const Ctx& cx, f2 st, unsigned tb) {
    const unsigned a = opaque(L_LNT + cx.tid * 4);
    v4u v[3];
    sfor<3>([&](auto ii) { v[ii] = v4u{lds_ld<unsigned>(a + ii * 1024), lds_ld<unsigned>(a + ii * 1024 + 2560), lds_ld<unsigned>(a + ii * 1024 + 5120), lds_ld<unsigned>(a + ii * 1024 + 7680)}; });
    const unsigned so = OFF_XNB + tb * 10240, vo = cx.tid * 16;
    st128c<0>(v[0], vo, cx.rws, so);
    st128c<0>(v[1], vo, cx.rws, so + 4096);
    st128c<0>(v[2], cx.w < 2 ? vo : OOB, cx.rws, so + 8192);
    st64c(v2u{__float_as_uint(st.x), __float_as_uint(st.y)}, sel((cx.lane & 31) == 31, (cx.lane >> 5) * 8, OOB), cx.rws, OFF_STATS + (8 * tb + 2 * cx.w) * 8);
}

struct Kgen {
    unsigned la, ai, cr, ci, ldt;
    float dt, er, rev;
    f2 w8, w32, v1, v2, v256, y[4], x[8], last;
    unsigned row[53];
};
constexpr int KG_STEPS = 57;
constexpr int kg_row(int I) { return I == 1 ? 32 : I == 4 ? 1 : I == 5 ? 4 : I < 7 ? -1 : I < 27 ? I + 26 : I < 29 ? I - 25 : I < 56 ? I - 24 : -1; }
template <int I, bool BESIDE_MFMA> DI void kg_step(Kgen& k) {
    auto mul = [](f2 a, f2 b) { if constexpr (BESIDE_MFMA) return cmul_s(a, b); else return cmul(a, b); };
    constexpr float LOG2E = 1.44269504088896f, INV2PI = 0.159154943091895f;
    const f2 prev = k.last;
    if constexpr (I == 0) { k.dt = rbf(__builtin_amdgcn_exp2f(blo(k.ldt) * LOG2E)); k.er = rbf(__builtin_amdgcn_exp2f(blo(k.la) * LOG2E)); tie(k.dt); tie(k.er); k.row[0] = 0x3f80u; }
    else if constexpr (I == 1) { k.er = (-k.er * k.dt) * LOG2E; k.rev = (blo(k.ai) * k.dt) * INV2PI; k.x[0] = f2{fmul_s(blo(k.cr), k.dt) * 2.f, fmul_s(blo(k.ci), -2.f * k.dt)}; tie(k.er); tie(k.rev); tie(k.x[0]); k.last = k.x[0]; }
    else if constexpr (I == 2) { k.v1 = cexp_at(k.er, -k.rev, 1.f); tie(k.v1); }
    else if constexpr (I == 3) { k.v256 = cexp_at(k.er, -k.rev, 256.f); tie(k.v256); }
    else if constexpr (I == 4) { k.w8 = cexp_at(k.er, k.rev, 8.f); tie(k.w8); k.last = k.w8; }
    else if constexpr (I == 5) { k.w32 = cexp_at(k.er, k.rev, 32.f); tie(k.w32); k.last = k.w32; }
    else if constexpr (I == 6) { k.v2 = mul(k.v1, k.v1); }
    else if constexpr (I == 7) { k.last = k.x[1] = mul(k.x[0], k.v1); }
    else if constexpr (I < 14) { constexpr int i = I - 6; k.last = k.x[i] = mul(k.x[i - 2], k.v2); }
    else if constexpr (I < 18) { constexpr int i = 2 * (I - 14); k.last = k.x[i] = mul(k.x[i], k.v256); }
    else if constexpr (I < 22) { constexpr int i = 4 * (I & 1); k.last = k.x[i] = mul(k.x[i], k.v256); }
    else if constexpr (I < 27) { k.last = k.x[0] = mul(k.x[0], k.v256); }
    else if constexpr (I == 27) { k.y[0] = k.w32; k.y[1] = k.w8; k.last = k.y[2] = mul(k.w8, k.w8); }
    else if constexpr (I == 28) { k.last = k.y[3] = mul(k.y[2], k.w8); }
    else if constexpr (I < 56) { constexpr int j = (I - 24) & 3; k.last = k.y[j] = mul(k.y[j], k.w32); }
    if constexpr (I > 0 && kg_row(I - 1) >= 0) {
        k.row[kg_row(I - 1)] = pk(prev.x, prev.y); tie(k.row[kg_row(I - 1)]);
        if constexpr (!BESIDE_MFMA) lds_st_tid<XY_BIAS + kg_row(I - 1) * XY_ROW>(k.row[kg_row(I - 1)]);
    }
    if constexpr (!BESIDE_MFMA && I == 2) lds_st_tid<XY_BIAS>(k.row[0]);
}
template <int LO, int HI> DI void kg_write(const Kgen& k) {
    sfor<HI - LO>([&](auto ii) { constexpr int r = LO + ii; if constexpr (r < 53) lds_st_tid<XY_BIAS + r * XY_ROW>(k.row[r]); });
}
DI void kg_set_base(const Ctx& cx) { set_m0(L_XY - XY_BIAS + (cx.w >> 1) * XY_CH + (cx.w & 1) * 256); }

DI void kernel_table(const Ctx& cx) {
    const unsigned cidx = cx.w >> 1, half = cx.w & 1, r = cx.lane & 15, g4 = cx.lane >> 4;
    const unsigned xa = opaque(L_XY + cidx * XY_CH + XY_X + r * XY_ROW + g4 * 16), ya = opaque(xa - XY_X + half * (16 * XY_ROW));
    f4 k0, k1;
    v4u a[8], b0[8], b1[8];
    sfor<8>([&](auto ks) { a[ks] = lds_ld<v4u>(ya + ks * 64); b0[ks] = lds_ld<v4u>(xa + ks * 64); b1[ks] = lds_ld<v4u>(xa + 16 * XY_ROW + ks * 64); PIN(); });
    const unsigned i0 = sel(r < 8, r, sel(r < 12, 240 + 2 * r, 128 * (r & 14) - 1024 + 4 * (r & 1)));
    const unsigned i1 = sel(r <= 4, 1024 + 256 * r, 2049);
    const unsigned kp = L_KP + cidx * KP_CH + 256 * half + 64 * g4, a0 = opaque(kp + 2 * i0), a1 = opaque(kp + 2 * i1);
    PIN();
    sfor<8>([&](auto ks) { if constexpr (ks == 0) { mfma16z(k0, a[ks], b0[ks]); mfma16z(k1, a[ks], b1[ks]); } else { mfma16(k0, a[ks], b0[ks]); mfma16(k1, a[ks], b1[ks]); } });
    PIN(); asm volatile("s_nop 7" : "+v"(k0), "+v"(k1)); PIN();
    sfor<2>([&](auto e2) {
        const unsigned v0 = pk(k0[2 * e2], k0[2 * e2 + 1]), v1 = pk(k1[2 * e2], k1[2 * e2 + 1]);
        lds_st<u16>(a0 + 32 * e2, (u16)v0); lds_st<u16>(a0 + 32 * e2 + 16, (u16)(v0 >> 16));
        lds_st<u16>(a1 + 32 * e2, (u16)v1); lds_st<u16>(a1 + 32 * e2 + 16, (u16)(v1 >> 16));
    });
}
DI void expand_taps(const Ctx& cx, unsigned r0) {
    const unsigned m = 64 * (cx.w & 1) + cx.lane, kp = opaque(L_KP + (cx.w >> 1) * KP_CH + 16 * m);
    const v4u d = lds_ld<v4u>(kp);
    const unsigned d4 = lds_ld<unsigned>(kp + 16), e0 = lds_ld<unsigned>(kp + 2048), e4 = lds_ld<unsigned>(kp + 2048 + 16);
    const bool s1 = m < 32, s3 = cx.w & 1;
    const unsigned sel0 = s1 ? 0x01000302u : 0x01000100u, sel1 = s1 ? 0x01000302u : 0x01000504u, sel3 = s3 ? 0x05040504u : sel1;
    v4u o0, o1;
    o0.w = __builtin_amdgcn_perm(d.x, d.x, sel0);
    o0.z = __builtin_amdgcn_perm(d.z, s3 ? d.x : d.y, sel1);
    o0.y = __builtin_amdgcn_perm(d.z, d.z, sel0);
    o0.x = __builtin_amdgcn_perm(d4, d.w, sel3);
    o1.w = __builtin_amdgcn_perm(e0, e0, 0x01000100u);
    o1.z = o1.w;
    o1.y = __builtin_amdgcn_perm(e4, e0, (m & 1) ? 0x05040504u : 0x01000504u);
    o1.x = __builtin_amdgcn_perm(e4, e4, 0x01000100u);
    const unsigned a = opaque(r0 + 16 * (127 - m));
    lds_st<v4u>(a + 2048, o0);
    lds_st<v4u>(a, o1);
    lds_st<v4u>(r0 + 4096 + 16 * (m & 1), v4u{0, 0, 0, 0});
}
template <bool COPY0> DI void shift_taps(const Ctx& cx, unsigned r0) {
    const unsigned m = 64 * (cx.w & 1) + cx.lane, src = opaque(r0 + 16 * m), dst = opaque(L_RC + (cx.w >> 1) * RC_CH + 16 * m);
    sfor<2>([&](auto it) {
        const v4u d = lds_ld<v4u>(src + it * 2048);
        const v2u n = lds_ld<v2u>(src + it * 2048 + 16);
        const unsigned q0 = __builtin_amdgcn_alignbyte(d.y, d.x, 2), q1 = __builtin_amdgcn_alignbyte(d.z, d.y, 2), q2 = __builtin_amdgcn_alignbyte(d.w, d.z, 2);
        const unsigned q3 = __builtin_amdgcn_alignbyte(n.x, d.w, 2), q4 = __builtin_amdgcn_alignbyte(n.y, n.x, 2);
        if constexpr (COPY0) lds_st<v4u>(dst + it * 2048, d);
        lds_st<v4u>(dst + it * 2048 + RC_COPY, v4u{q0, q1, q2, q3});
        lds_st<v4u>(dst + it * 2048 + 2 * RC_COPY, v4u{d.y, d.z, d.w, n.x});
        lds_st<v4u>(dst + it * 2048 + 3 * RC_COPY, v4u{q1, q2, q3, q4});
    });
    lds_st<v2u>(L_RC + ((cx.lane & 31) >> 2) * RC_COPY + 4096 + (cx.lane & 3) * 8, v2u{0, 0});
}
template <class HOOK, class POST> DI void conv_pair(Ctx& cx, unsigned ch, unsigned ubase, float dskip, HOOK&& hook, POST&& post) {
    const unsigned cidx = cx.w >> 1, half = cx.w & 1, r = cx.lane & 15, g4 = cx.lane >> 4;
    f4 ca[8];
    {
        const unsigned aa0 = L_RC + cidx * RC_CH + ((~r) & 3) * RC_COPY + 2 * (2028 - 4 * (r >> 2) + 8 * g4) - 3584 - 256 * half - 192;
        const unsigned aa = opaque(aa0), ab = opaque(aa0 + 8), bb = opaque(ubase + 448 + 32 * r + 16 * g4 - 256 * half - 192);
        v2u al[2][8], ah[2][8];
        v4u fb[2][8];
        auto rd = [&](auto d_, auto i_) {
            constexpr int d = d_, i = i_, e = i / 3, o = (3 - d) * 64;
            if constexpr (i % 3 == 0) al[d & 1][e] = lds_ld<v2u>(aa + (7 - e) * 512 + o);
            else if constexpr (i % 3 == 1) ah[d & 1][e] = lds_ld<v2u>(ab + (7 - e) * 512 + o);
            else fb[d & 1][e] = lds_ld<v4u>(bb + e * 512 + o);
            PIN();
        };
        sfor<12>([&](auto i) { rd(ic<0>{}, i); });
        sfor<4>([&](auto d_) {
            constexpr int d = d_;
            sfor<8>([&](auto n_) {
                constexpr int n = n_;
                sfor<n + 1>([&](auto e_) {
                    constexpr int e = e_, idx = n * (n + 1) / 2 + e;
                    const v4u a = {al[d & 1][e].x, al[d & 1][e].y, ah[d & 1][e].x, ah[d & 1][e].y};
                    if constexpr (d == 0 && e == 0) mfma16z(ca[n], a, fb[d & 1][n - e]); else mfma16(ca[n], a, fb[d & 1][n - e]);
                    PIN();
                    if constexpr (d == 0) { if constexpr (idx < 6) { rd(ic<0>{}, ic<12 + 2 * idx>{}); rd(ic<0>{}, ic<13 + 2 * idx>{}); } else if constexpr (idx >= 12) rd(ic<1>{}, ic<idx - 12>{}); }
                    else if constexpr (d < 3 && idx % 3 != 2) rd(ic<d + 1>{}, ic<idx - idx / 3>{});
                });
            });
            hook(d_);
        });
    }
    STAMP(7);
    post();
    PIN(); asm volatile("s_nop 7"); PIN();
    sfor<8>([&](auto n) { tie(ca[n]); });
    const unsigned ra = opaque(L_RED2 + cx.lane * 16 + cx.w * 4096), rb = opaque(L_RED2 + cx.lane * 16 + (cx.w ^ 1) * 4096);
    if (half == 0) { sfor<4>([&](auto i) { lds_st<f4>(ra + i * 1024, ca[4 + i]); }); }
    else { sfor<4>([&](auto i) { lds_st<f4>(ra + i * 1024, ca[i]); ca[i] = ca[4 + i]; }); }
    const unsigned uaddr = opaque(ubase + 480 + 2 * (1024 * half + 16 * r + 4 * g4));
    f4 uf[4];
    sfor<4>([&](auto i) { const v2u u = lds_ld<v2u>(uaddr + i * 512); uf[i] = f4{blo(u.x), bhi(u.x), blo(u.y), bhi(u.y)} * dskip + ca[i]; });
    BARRIER();
    v2u pkd[4];
    sfor<4>([&](auto i) { const f4 y = uf[i] + lds_ld<f4>(rb + i * 1024); pkd[i] = v2u{pk(y[0], y[1]), pk(y[2], y[3])}; });
    const unsigned vo = 2 * (256 * (g4 & 1) + 16 * r + 8 * (g4 >> 1)), so = OFF_ST + ch * 4096 + 2048 * half;
    sfor<2>([&](auto i2) {
        const auto sx = __builtin_amdgcn_permlane16_swap(pkd[2 * i2].x, pkd[2 * i2 + 1].x, false, false);
        const auto sy = __builtin_amdgcn_permlane16_swap(pkd[2 * i2].y, pkd[2 * i2 + 1].y, false, false);
        st128c<i2 * 1024>(v4u{sx[0], sy[0], sx[1], sy[1]}, vo, cx.rws, so);
    });
}

__global__ void __launch_bounds__(256, 1) s4d_mixer_kernel(Params p) {
    u64 pr0;
    asm volatile("s_memrealtime %0\n\ts_waitcnt lgkmcnt(0)" : "=s"(pr0) :: "memory");
    PIN();
    extern __shared__ __attribute__((aligned(16))) char smem[];
    const unsigned tid = threadIdx.x, lane = tid & 63, w = rfl(tid >> 6);
    const unsigned b = blockIdx.x, g = b & 7, q = b >> 3;
    {
        u64 pc;
        asm volatile("s_getpc_b64 %0" : "=s"(pc) :: ACLOB_ALL);
        const unsigned line = (q & 7) * (CODE_LINES / 8) + w * (CODE_LINES / 32) + (lane & (CODE_LINES / 32 - 1));
        asm volatile("global_load_dword a255, %0, %1" :: "v"(line * 128u), "s"(pc & ~127ull));
    }
    asm volatile("" :: "v"((unsigned)(size_t)(LAS char*)smem));
    Ctx cx;
    STAMP(0);
    const unsigned tb = 32 * g + q;
    const rsrc_t rx = __builtin_amdgcn_make_buffer_rsrc((void*)p.x, 0, SL * SD * 2, 0x00020000);
    const rsrc_t rw1 = __builtin_amdgcn_make_buffer_rsrc((void*)p.w1, 0, SD * SF * 2, 0x00020000);
    unsigned xa[5], xb[5];
    v2u xa0, xa1, xb0, xb1;
    {
        const unsigned so = (8 * tb + 2 * w) * (SD * 2);
        asm volatile("s_nop 4");
        xa0 = ld64<0>(lane * 8, rx, so); xa1 = ld64<512>(lane * 8, rx, so); xa[4] = ld32<1024>(lane * 4, rx, so);
        xb0 = ld64<1280>(lane * 8, rx, so); xb1 = ld64<1280 + 512>(lane * 8, rx, so); xb[4] = ld32<1280 + 1024>(lane * 4, rx, so);
    }
    const unsigned tx = (512 * (g >> 1) + 16 * q + (lane & 15)) * 1280 + (lane >> 4) * 128;
    unsigned tw;
    {
        const unsigned st = lane >= 28, i = lane - 28 * st, hi = i >= 14;
        tw = sel(lane < 56, (64 * st + 2 * q + hi) * (SF * 2) + (g & 1) * 1792 + (i - 14 * hi) * 128, OOB);
    }
    if (w == 1) l2_touch(tx, rx, 0); else l2_touch(tw, rw1, sel(w == 0, 0, 2 * w - 2) * (64 * SF * 2));
    auto touch2 = [&]() {
        if (w == 0 || w == 3) l2_touch(sel(w == 0 || lane < 32, tx, OOB), rx, sel(w == 0, 4, 8) * 128);
        else l2_touch(tw, rw1, (4 + 2 * w) * (64 * SF * 2));
    };
    cx.rws = __builtin_amdgcn_make_buffer_rsrc((void*)p.ws, 0, WS_USED, 0x00020000);
    cx.tid = tid; cx.lane = lane; cx.w = w;
    v2u wv = ld64c<0>(opaque(OFF_W), cx.rws, 0);
    const bool spare = q >= 28;
    const unsigned ch0 = 80 * g + sel(spare, 6 * q - 112, 2 * q), cidx = w >> 1;
    const rsrc_t rla = __builtin_amdgcn_make_buffer_rsrc((void*)p.lar, 0, SD * 256, 0x00020000);
    const rsrc_t rai = __builtin_amdgcn_make_buffer_rsrc((void*)p.aim, 0, SD * 256, 0x00020000);
    const rsrc_t rcr = __builtin_amdgcn_make_buffer_rsrc((void*)p.cr, 0, SD * 256, 0x00020000);
    const rsrc_t rci = __builtin_amdgcn_make_buffer_rsrc((void*)p.ci, 0, SD * 256, 0x00020000);
    const unsigned pvo = w * 128 + lane * 2;
    Kgen kg;
    kg.last = f2{0.f, 0.f};
    kg.la = ld16(pvo, rla, ch0 * 256); kg.ai = ld16(pvo, rai, ch0 * 256); kg.cr = ld16(pvo, rcr, ch0 * 256); kg.ci = ld16(pvo, rci, ch0 * 256);
    const unsigned rt = 4 * g + (q >> 3), ct = q & 7;
    const unsigned nk = 14 - (w >> 1), rot = (ct * 13) >> 3, k0s = 14 * w - (w == 3 ? 1 : 0) + rot, klast = k0s + 13 - (rot ? 14 : 0);
    auto late_setup = [&]() {
        sfor<2>([&](auto i) { tie(xa0); tie(xa1); tie(xb0); tie(xb1); });
        tie(xa[4]); tie(xb[4]);
        xa[0] = xa0.x; xa[1] = xa0.y; xa[2] = xa1.x; xa[3] = xa1.y; xb[0] = xb0.x; xb[1] = xb0.y; xb[2] = xb1.x; xb[3] = xb1.y;
    };
    auto take_epoch = [&]() {
        tie(wv); tie(kg.la); tie(kg.ai); tie(kg.cr); tie(kg.ci);
        const u64 e = ((u64)rfl(wv.y) << 32 | rfl(wv.x)) + 0x9E3779B97F4A7C15ull;
        cx.e_lo = (unsigned)e; cx.e_hi = (unsigned)(e >> 32);
        cx.flag_vo = tid == 0 ? 0u : OOB;
        cx.w16a = lane * 16; cx.w16b = lane * 16 + 4096;
        cx.thr = nk - rot; cx.w2a = OFF_W2T + (ct * NKS + k0s) * 5120; cx.w2b = cx.w2a - nk * 5120;
        cx.w2l = sel(w < 2, OFF_W2T + (ct * NKS + klast) * 5120, WS_USED); cx.w2so = 0;
    };
    auto half16 = [&](const u16* base, unsigned ch) { const unsigned v = *(const CAS unsigned*)(base + ch); return (v >> (16 * cidx)) & 0xffffu; };

    const unsigned ti = 4 * (g >> 1) + q / 7, tj = 7 * (g & 1) + q % 7, wf = w >> 1, wt = w & 1;
    unsigned gx[4], gw[4];
    sfor<4>([&](auto ii) {
        gx[ii] = (32 * w + 8 * ii + (lane >> 3)) * 1280 + 16 * ((lane & 7) ^ (4 * (ii & 1) + (lane >> 4))) - ii * 1024;
        gw[ii] = (16 * w + 4 * ii + (lane >> 4)) * (SF * 2) + 16 * ((lane & 15) ^ (((lane >> 4) << 2) | ii)) - ii * 1024;
    });
    const unsigned sx0 = ti * (128 * 1280), sw0 = tj * 256;
    auto piece = [&](auto s_, auto pi_) {
        constexpr int s = s_, pi = pi_, ii = pi & 3;
        if constexpr (ii == 0) set_m0((s & 3) * 32768 + (pi >> 2) * 16384 + w * 4096);
        if constexpr (pi < 4) dma<ii * 1024>(gx[ii], rx, sx0 + s * 128); else dma<ii * 1024>(gw[ii], rw1, sw0 + s * (64 * SF * 2));
    };
    auto stage = [&](auto s_) { sfor<8>([&](auto pi) { piece(s_, pi); }); };
    const unsigned sid = 27 * g + sel(q < 26, q, 26), sks = sid >> 2, sqq = sid & 3;
    const rsrc_t rb1 = __builtin_amdgcn_make_buffer_rsrc((void*)p.b1, 0, SF * 2, 0x00020000);
    unsigned b1v = ld16((tid & 127) * 2, rb1, tj * 256);
    if (!spare) {
        const rsrc_t rw2 = __builtin_amdgcn_make_buffer_rsrc((void*)p.w2, 0, SD * SF * 2, 0x00020000);
        set_m0(L_W2S + w * 3072);
        sfor<3>([&](auto ii) {
            const unsigned P = 64 * (3 * w + ii) + lane, row = (P * 3277) >> 16, vo = row * (1280 - 320) + P * 16 - ii * 1024;
            dma<ii * 1024>(sel(ii == 0 || w < 3, vo, OOB), rw2, sks * (32 * 1280) + sqq * 320);
        });
        VMWAIT(10);
    } else VMWAIT(7);
    late_setup();
    STAMP(1);
    const f2 st = layernorm_rows(xa, xb, cx, [&]() { if (!spare) stage(ic<0>{}); }, [&]() { touch2(); if (!spare) stage(ic<1>{}); });
    if (!spare) VMWAIT(17); else VMWAIT(1);
    take_epoch(); tie(b1v);
    st64c(v2u{(unsigned)pr0, (unsigned)(pr0 >> 32)}, cx.flag_vo, cx.rws, 65536 + tb * 8);
    BARRIER();
    store_xn(cx, st, tb);
    unsigned ubase;

    if (!spare) {
        {
            const unsigned g4 = lane >> 4, a0 = opaque(L_W2S + (16 * (g4 >> 1) + 4 * (g4 & 1) + ((lane & 15) >> 2)) * 320 + 8 * (lane & 3) + w * 96);
            v4u fr[3];
            sfor<3>([&](auto ii) { const v2u lo = lds_tr(a0 + ii * 32), hi = lds_tr(a0 + ii * 32 + 8 * 320); fr[ii] = v4u{lo.x, lo.y, hi.x, hi.y}; });
            sfor<3>([&](auto ii) {
                const unsigned fi = 3 * w + ii;
                st128c<0>(fr[ii], cx.w16a, cx.rws, sel(fi < 10, OFF_W2T + (2 * sqq * NKS + sks) * 5120 + fi * 1024 + sel(fi >= 5, (NKS - 1) * 5120, 0), WS_USED));
            });
        }
        stage(ic<2>{});
        kg.ldt = half16(p.ldt, ch0);
        STAMP(2);
        unsigned ax[2][4], aw[2][4];
        {
            const unsigned row = 64 * wt + (lane & 31), hh = lane >> 5, blk = (lane >> 4) & 1, qr = (lane & 15) >> 2, pp = lane & 3;
            sfor<4>([&](auto ks) { ax[0][ks] = opaque(row * 128 + 16 * ((2 * ks + hh) ^ ((row >> 1) & 7))); ax[1][ks] = opaque(ax[0][ks] + 65536); });
            sfor<4>([&](auto i) {
                constexpr int mf = i >> 1, t = i & 1;
                const unsigned r = 8 * hh + 4 * t + qr, chn = 4 * (2 * wf + (mf ^ wt)) + 2 * blk + (pp >> 1);
                aw[0][i] = opaque(16384 + 256 * r + 16 * (chn ^ ((qr << 2) | ((2 * hh + t) & 3))) + 8 * (pp & 1)); aw[1][i] = opaque(aw[0][i] + 65536);
            });
        }
        const unsigned onesel = ((lane ^ (lane >> 4)) & 1) ? 0u : 0x3f803f80u;
        v4u ones = {onesel, onesel, onesel, onesel};
        tie(ones);
        constexpr int W7 = 4, W8 = 4, W9 = 4, WG0 = 12, WG1 = 28;
        f16v acc[2][2];
        f4 csum;
        v2u fa[2][4], plv, st0, st1;
        v4u fb[2][2], ub0, ub1;
        auto rd = [&](auto n_, auto i_) {
            constexpr int n = n_, i = i_, s = n >> 2, ks = n & 3, bf = n & 1, hi = (s & 3) >> 1;
            constexpr unsigned so = (s & 1) * 32768;
            if constexpr (n < 40) {
                if constexpr (i == 0) fb[bf][0] = lds_ld<v4u>(ax[hi][ks] + so);
                else if constexpr (i == 3) fb[bf][1] = lds_ld<v4u>(ax[hi][ks] + so + 4096);
                else fa[bf][i < 3 ? i - 1 : i - 2] = lds_tr(aw[hi][i < 3 ? i - 1 : i - 2] + so + ks * 4096);
            }
            PIN();
        };
        VMWAIT(24);
        BARRIER();
        sfor<6>([&](auto i) { rd(ic<0>{}, i); });
        piece(ic<3>{}, ic<0>{}); piece(ic<3>{}, ic<1>{});
        sfor<40>([&](auto n0_) {
            constexpr int n = n0_ + 1, s = n >> 2, ks = n & 3, bf = (n - 1) & 1;
            constexpr bool first = n == 1;
            if constexpr (ks == 0 && n < 40) {
                constexpr int WAITS[10] = {0, 23, 16, 17, 17, 16, 17, 16, 8 + 2 + W7, W7 + W8};
                VMWAIT(WAITS[s]);
                if constexpr (s == 7) { tie(plv); if (!flags_match(plv, cx)) poll<2>(OFF_F1 + tid * FSTR, cx); }
                BARRIER();
                if constexpr (s == 2) publish(cx, OFF_F1 + tb * FSTR);
                if constexpr (s == 5) plv = ld64c<0>(tid * FSTR, cx.rws, OFF_F1);
                if constexpr (s == 7) {
                    const unsigned vo = (64 * wt + (lane & 31)) * 8;
                    st0 = ld64c<0>(vo, cx.rws, OFF_STATS + ti * 1024); st1 = ld64c<256>(vo, cx.rws, OFF_STATS + ti * 1024);
                }
                if constexpr (s == 8) kg_set_base(cx);
                PIN();
            }
            auto vm = [&](auto m_) {
                constexpr int pi = 2 * ks + m_;
                if constexpr (s <= 6) piece(ic<s + 3>{}, ic<pi>{});
                else if constexpr (s <= 9 && m_ == 0) w2_prefetch<(s - 7) * 4 + ks, (s - 7) * 4 + ks + 1>(cx);
                PIN();
            };
            auto side = [&](auto m_) {
                constexpr int m = m_;
                if constexpr (n <= 6) { if constexpr (m == 0) kg_step<n - 1, true>(kg); }
                else if constexpr (n < 32) { kg_step<6 + 2 * (n - 7) + m, true>(kg); if constexpr (n == 31 && m == 1) kg_step<56, true>(kg); }
                else if constexpr (n < 40) kg_write<(n - 32) * 7 + 4 * m, (n - 32) * 7 + 4 + 3 * m>(kg);
                PIN();
            };
            const v4u a0 = {fa[bf][0].x, fa[bf][0].y, fa[bf][1].x, fa[bf][1].y}, a1 = {fa[bf][2].x, fa[bf][2].y, fa[bf][3].x, fa[bf][3].y};
            if constexpr (first) mfma32z(acc[0][0], a0, fb[bf][0]); else mfma32(acc[0][0], a0, fb[bf][0]);
            PIN(); rd(ic<n>{}, ic<0>{}); rd(ic<n>{}, ic<1>{}); rd(ic<n>{}, ic<2>{}); rd(ic<n>{}, ic<3>{});
            if constexpr (first) mfma32z(acc[0][1], a0, fb[bf][1]); else mfma32(acc[0][1], a0, fb[bf][1]);
            PIN(); rd(ic<n>{}, ic<4>{}); rd(ic<n>{}, ic<5>{}); vm(ic<0>{});
            if constexpr (first) mfma16z(csum, a0, ones); else mfma16(csum, a0, ones);
            if constexpr (first) mfma32z(acc[1][0], a1, fb[bf][0]); else mfma32(acc[1][0], a1, fb[bf][0]);
            PIN(); side(ic<0>{}); vm(ic<1>{});
            if constexpr (first) mfma32z(acc[1][1], a1, fb[bf][1]); else mfma32(acc[1][1], a1, fb[bf][1]);
            PIN(); side(ic<1>{});
        });
        STAMP(3);
        tie(csum);
        lds_st<f4>(L_CS + (64 * wf + 32 * wt + 16 * (lane & 1) + 4 * (lane >> 4)) * 4, csum);
        if (w < 2) lds_st<float>(L_B1 + tid * 4, blo(b1v));
        tie(st0); tie(st1);
        {
            const unsigned uvo = (tid >> 1) * 10240 + (tid & 1) * 16;
            ub0 = ld128c<0>(uvo, cx.rws, OFF_XNB + ch0 * 16); ub1 = ld128c<0>(uvo, cx.rws, OFF_XNB + 128 * 10240 + ch0 * 16);
        }
        BARRIER();
        MFMA_DRAIN();
        {
            const unsigned hh = lane >> 5;
            const unsigned hvo = sel(tj == 13 && wf == 1, OOB, ((lane >> 4) & 1) * (NKS * 1024) + (hh * 16 + (lane & 15)) * 16);
            const unsigned hso = OFF_H + ((8 * ti + 4 * wt) * NKS + 4 * tj + 2 * wf) * 1024;
            const unsigned cam[2] = {opaque(L_CS + (64 * wf + 32 * wt + 4 * hh) * 4), opaque(L_CS + (64 * wf + 32 * (wt ^ 1) + 4 * hh) * 4)};
            constexpr float C1 = -2.302208198f, C2 = -0.1029432397f;
            sfor<2>([&](auto mf_) {
                constexpr int mf = mf_;
                f4 cs[4], bb[4];
                sfor<4>([&](auto ga) { cs[ga] = lds_ld<f4>(cam[mf] + 32 * ga); bb[ga] = lds_ld<f4>(cam[mf] + 512 + 32 * ga); });
                sfor<2>([&](auto nt_) {
                    constexpr int nt = nt_;
                    tie(acc[mf][nt]);
                    const v2u sv = nt ? st1 : st0;
                    const float sa = __uint_as_float(sv.x), sb = __uint_as_float(sv.y);
                    sfor<2>([&](auto chk) {
                        f2 z[4], e[4];
                        sfor<4>([&](auto i) {
                            constexpr int ga = 2 * chk + (i >> 1), e2 = 2 * (i & 1);
                            z[i] = f2{acc[mf][nt][4 * ga + e2], acc[mf][nt][4 * ga + e2 + 1]} * sa + (f2{cs[ga][e2], cs[ga][e2 + 1]} * sb + f2{bb[ga][e2], bb[ga][e2 + 1]});
                        });
                        sfor<4>([&](auto i) { e[i] = z[i] * (z[i] * z[i] * C2 + C1); });
                        sfor<4>([&](auto i) { e[i] = f2{__builtin_amdgcn_exp2f(e[i].x), __builtin_amdgcn_exp2f(e[i].y)}; });
                        sfor<4>([&](auto i) { e[i] = e[i] + 1.f; });
                        sfor<4>([&](auto i) { e[i] = f2{__builtin_amdgcn_rcpf(e[i].x), __builtin_amdgcn_rcpf(e[i].y)}; });
                        unsigned o[4];
                        sfor<4>([&](auto i) { const f2 r = z[i] * e[i]; o[i] = pk(r.x, r.y); });
                        constexpr int idx = 4 * mf + 2 * nt + chk, f0 = WG0 + (WG1 - WG0) * idx / 8, f1 = WG0 + (WG1 - WG0) * (idx + 1) / 8;
                        w2_prefetch<f0, f1>(cx);
                        st128c<chk * 512>(v4u{o[0], o[1], o[2], o[3]}, hvo, cx.rws, hso + (2 * nt * NKS + (mf ^ wt)) * 1024);
                        PIN();
                    });
                });
            });
        }
        STAMP(4);
        w2_prefetch<WG1, WG1 + 3>(cx);
        kernel_table(cx);
        w2_prefetch<WG1 + 3, WG1 + 6>(cx);
        BARRIER();
        const unsigned r0 = L_RC + cidx * RC_CH;
        expand_taps(cx, r0);
        VMWAIT(6);
        tie(ub0); tie(ub1);
        {
            const unsigned ua = opaque(L_U + 480 + (tid & 1) * U_CH + (tid >> 1) * 16);
            lds_st<v4u>(ua, ub0); lds_st<v4u>(ua + 2048, ub1);
            lds_st<v2u>(L_U + (tid >> 7) * U_CH + sel((tid & 127) < 60, tid & 127, 59) * 8, v2u{0, 0});
        }
        BARRIER();
        publish(cx, OFF_F2 + (16 * ti + tj) * FSTR);
        STAMP(5);
        w2_prefetch<WG1 + 6, 40>(cx);
        shift_taps<false>(cx, r0);
        ubase = L_U + cidx * U_CH;
    } else {
        STAMP(2);
        kg_set_base(cx);
#pragma unroll 1
        for (unsigned round = 0; round < 3; ++round) {
            CYC(0);
            kg.ldt = half16(p.ldt, ch0 + 2 * round);
            sfor<2>([&](auto i) { kg_step<i, false>(kg); });
            CYC(1);
            PIN();
            const unsigned cn = (ch0 + 2 * round + 2) * 256;
            kg.la = ld16(pvo, rla, cn); kg.ai = ld16(pvo, rai, cn); kg.cr = ld16(pvo, rcr, cn); kg.ci = ld16(pvo, rci, cn);
            PIN();
            sfor<4>([&](auto i) { kg_step<i + 2, false>(kg); });
            CYC(2);
            sfor<KG_STEPS - 6>([&](auto i) { kg_step<i + 6, false>(kg); });
            CYC(3);
            VMWAIT(0);
            CYC(4);
            tie(kg.la); tie(kg.ai); tie(kg.cr); tie(kg.ci);
            BARRIER();
            CYC(5);
            if (round == 0) publish(cx, OFF_F1 + tb * FSTR);
            kernel_table(cx);
            CYC(6);
            BARRIER();
            CYC(7);
            expand_taps(cx, L_R0 + (2 * round + cidx) * RC_COPY);
            CYC(8);
        }
        STAMP(3);
        poll<4>(OFF_F1 + tid * FSTR, cx);
        BARRIER();
        STAMP(4);
        {
            v4u ub[6];
            unsigned ua[6];
            sfor<6>([&](auto i) {
                const unsigned P = tid + 256 * i, blk = (P * 10923) >> 16, c = P - 6 * blk;
                ub[i] = ld128c<0>(blk * 10240 + c * 16, cx.rws, OFF_XNB + ch0 * 16);
                ua[i] = L_U6 + 480 + c * U_CH + blk * 16;
            });
            w2_prefetch<0, 4>(cx);
            shift_taps<true>(cx, L_R0 + cidx * RC_COPY);
            sfor<3>([&](auto i) { lds_st<v2u>(L_U6 + (2 * i + (tid >> 7)) * U_CH + sel((tid & 127) < 60, tid & 127, 59) * 8, v2u{0, 0}); });
            VMWAIT(4);
            sfor<6>([&](auto i) { tie(ub[i]); lds_st<v4u>(ua[i], ub[i]); });
        }
        STAMP(5);
#pragma unroll 1
        for (unsigned round = 0; round < 2; ++round) {
            const float dsk = blo(half16(p.dsk, ch0 + 2 * round));
            BARRIER();
            conv_pair(cx, ch0 + 2 * round + cidx, L_U6 + (2 * round + cidx) * U_CH, dsk, [&](auto d) {
                if (round == 0) w2_prefetch<4 + 9 * d / 2, 4 + 9 * (d + 1) / 2>(cx); else w2_prefetch<22 + 9 * d / 2, 22 + 9 * (d + 1) / 2>(cx);
            }, [&]() {});
            shift_taps<true>(cx, L_R0 + (2 * round + 2 + cidx) * RC_COPY);
        }
        ubase = L_U6 + (4 + cidx) * U_CH;
    }
    cx.w2so = rfl(cx.w2so); cx.w2l = rfl(cx.w2l); cx.w2a = rfl(cx.w2a); cx.w2b = rfl(cx.w2b); cx.thr = rfl(cx.thr); cx.e_lo = rfl(cx.e_lo); cx.e_hi = rfl(cx.e_hi);
    ubase = rfl(ubase);
    const unsigned chl = rfl(ch0 + sel(spare, 4, 0));
    const float dskip = blo(half16(p.dsk, chl));
    BARRIER();
    STAMP(6);

    constexpr int WIN = 4;
    v4u wx[6], hf[WIN][4];
    unsigned hv[4], hso;
    sfor<4>([&](auto t) { hv[t] = (4 * rt + ((t + w) & 3)) * (NKS * 1024) + lane * 16; });
    const unsigned hsa = OFF_H + k0s * 1024, hsb = hsa - nk * 1024, hsl = sel(w < 2, OFF_H + klast * 1024, WS_USED);
    auto ldh = [&](auto s_, auto t_) {
        constexpr int s = s_, t = t_;
        if constexpr (s < 14) {
            if constexpr (t == 0) hso = slot_off<s, 1024>(hsa, hsb, hsl, cx.thr);
            hf[s % WIN][t] = ld128c<0>(hv[t], cx.rws, hso);
        }
        PIN();
    };
    {
        const unsigned f2vo = OFF_F2 + (16 * (rt >> 1) + sel(lane < 14, lane, 0)) * FSTR;
        v2u plv2;
        conv_pair(cx, chl + cidx, ubase, dskip,
            [&](auto d) {
                if constexpr (d == 2) plv2 = ld64c<0>(f2vo, cx.rws, 0);
                if constexpr (d < 3) w2_prefetch<40 + 8 * d, 48 + 8 * d>(cx);
            },
            [&]() {
                VMWAIT(8);
                tie(plv2);
                if (!flags_match(plv2, cx)) poll<1>(f2vo, cx);
                sfor<WIN - 1>([&](auto s) { sfor<4>([&](auto t) { ldh(s, t); }); });
                wx[0] = ld128c<0>(cx.w16b, cx.rws, cx.w2so);
                sfor<4>([&](auto c) { wx[1 + c] = ld128c<c * 1024>(cx.w16a, cx.rws, cx.w2l); });
                wx[5] = ld128c<0>(cx.w16b, cx.rws, cx.w2l);
                STAMP(8);
            });
    }
    STAMP(9);
    f4 acc2[5][4];
    v2u xo[5], bo[5], plv3;
    v4u sv[3];
    const unsigned xvo = (16 * w + (lane & 15)) * (SD * 2) + 8 * (lane >> 4), xso = 64 * rt * (SD * 2) + 160 * ct;
    {
        const unsigned f3so = rfl(OFF_F3 + tb * FSTR);
        const rsrc_t rb2 = __builtin_amdgcn_make_buffer_rsrc((void*)p.b2, 0, SD * 2, 0x00020000);
        const unsigned svo = (tid >> 3) * 4096 + (tid & 7) * 16;
        sfor<14>([&](auto s_) {
            constexpr int s = s_;
            constexpr int later = (s + WIN - 2 < 14 ? WIN - 2 : 13 - s) * 4;
            constexpr int extra = s <= 2 ? 8 : (s == 4 || s == 5 || s == 7 || s == 8) ? 1 : (s == 10 || s == 11) ? 13 : 0;
            VMWAIT(later + extra);
            sfor<4>([&](auto t) { tie(hf[s % WIN][t]); });
            if constexpr (s == 3) { sfor<6>([&](auto i) { tie(wx[i]); }); BARRIER(); publish(cx, f3so); }
            if constexpr (s == 6) plv3 = ld64c<0>((lane & 31) * FSTR, cx.rws, OFF_F3 + ct * (32 * FSTR));
            if constexpr (s == 9) {
                tie(plv3);
                if (!flags_match(plv3, cx)) poll<1>(OFF_F3 + ct * (32 * FSTR) + (lane & 31) * FSTR, cx);
                const unsigned so = OFF_ST + 80 * ct * 4096 + 128 * rt;
                sv[0] = ld128c<0>(svo, cx.rws, so); sv[1] = ld128c<0>(svo, cx.rws, so + 32 * 4096); sv[2] = ld128c<0>(svo, cx.rws, sel(w < 2, so + 64 * 4096, WS_USED));
                sfor<5>([&](auto c) { xo[c] = ld64<c * 32>(xvo, rx, xso); });
                sfor<5>([&](auto c) { bo[c] = ld64<c * 32>(8 * (lane >> 4), rb2, 160 * ct); });
            }
            PIN();
            sfor<5>([&](auto c_) {
                constexpr int c = c_, f = 5 * s + c;
                sfor<4>([&](auto t) {
                    if constexpr (f >= 64) mfma16(acc2[c][t], wx[f - 64], hf[s % WIN][t]);
                    else if constexpr (s == 0) mfma16az<4 * f>(acc2[c][t], hf[s % WIN][t]);
                    else mfma16a<4 * f>(acc2[c][t], hf[s % WIN][t]);
                });
                PIN();
                if constexpr (c < 4) ldh(ic<s + WIN - 1>{}, c_);
            });
            if constexpr (s == 12) {
                sfor<3>([&](auto ii) { tie(sv[ii]); });
                const unsigned sa = opaque(L_SIMG + (tid >> 3) * SIMG_ROW + (tid & 7) * 16);
                lds_st<v4u>(sa, sv[0]); lds_st<v4u>(sa + 32 * SIMG_ROW, sv[1]); lds_st<v4u>(sa + 64 * SIMG_ROW, sv[2]);
                PIN();
            }
        });
    }
    STAMP(10);
    sfor<5>([&](auto c) { tie(xo[c]); tie(bo[c]); });
    unsigned rw[3];
    sfor<3>([&](auto t1) { rw[t1] = opaque(L_RED + ((w + t1 + 1) & 3) * 15360 + t1 * 5120 + lane * 16); });
    const unsigned ra = opaque(L_RED + w * 15360 + lane * 16);
    const unsigned sa = opaque(L_SIMG + (4 * (lane >> 4) + ((lane & 15) >> 2)) * SIMG_ROW + 32 * w + 8 * (lane & 3));
    PIN(); asm volatile("s_nop 7"); PIN();
    sfor<5>([&](auto c) { sfor<3>([&](auto t1) { tie(acc2[c][t1 + 1]); lds_st<f4>(rw[t1] + c * 1024, acc2[c][t1 + 1]); }); });
    f4 zb[5], xf[5];
    sfor<5>([&](auto c) {
        tie(acc2[c][0]);
        zb[c] = acc2[c][0] + f4{blo(bo[c].x), bhi(bo[c].x), blo(bo[c].y), bhi(bo[c].y)};
        xf[c] = f4{blo(xo[c].x), bhi(xo[c].x), blo(xo[c].y), bhi(xo[c].y)};
    });
    BARRIER();
    {
        const rsrc_t ro = __builtin_amdgcn_make_buffer_rsrc((void*)p.out, 0, SL * SD * 2, 0x00020000);
        sfor<5>([&](auto c) {
            const f4 z = (zb[c] + lds_ld<f4>(ra + c * 1024)) + (lds_ld<f4>(ra + 5120 + c * 1024) + lds_ld<f4>(ra + 10240 + c * 1024));
            const v2u s2 = lds_tr(sa + c * 16 * SIMG_ROW);
            const f4 u = z * -1.44269504088896f;
            const f4 d = f4{__builtin_amdgcn_exp2f(u[0]), __builtin_amdgcn_exp2f(u[1]), __builtin_amdgcn_exp2f(u[2]), __builtin_amdgcn_exp2f(u[3])} + 1.f;
            const f4 sz = f4{blo(s2.x), bhi(s2.x), blo(s2.y), bhi(s2.y)} * z;
            const f4 o = sz * f4{__builtin_amdgcn_rcpf(d[0]), __builtin_amdgcn_rcpf(d[1]), __builtin_amdgcn_rcpf(d[2]), __builtin_amdgcn_rcpf(d[3])} + xf[c];
            st64<c * 32>(v2u{pk(o[0], o[1]), pk(o[2], o[3])}, xvo, ro, xso);
        });
    }
    if (b == PROBE_WG && w == 0) {
        unsigned mn = 0xffffffffu, mx = 0;
        const unsigned base = (unsigned)pr0 - 100000u;
        sfor<4>([&](auto i) { v2u v = ld64c<i * 512>(lane * 8, cx.rws, 65536); VMWAIT(0); tie(v); const unsigned d = v.x - base; mn = min(mn, d); mx = max(mx, d); });
        for (int o = 32; o; o >>= 1) { mn = min(mn, (unsigned)__shfl_xor((int)mn, o)); mx = max(mx, (unsigned)__shfl_xor((int)mx, o)); }
        mn = rfl(mn); mx = rfl(mx);
        u64 t0, t1;
        asm volatile("s_memrealtime %0\n\ts_waitcnt lgkmcnt(0)" : "=s"(t0));
        const u64 d = (u64)(PROBE_KIND == 0 ? ((unsigned)t0 - base) - mn : mx - mn) * PROBE_MUL;
        do { asm volatile("s_memrealtime %0\n\ts_waitcnt lgkmcnt(0)" : "=s"(t1)); } while (t1 - t0 < d);
    }
    if (b == 0) publish(cx, OFF_W);
}

extern "C" void kernel_launch(void* const* d_in, const int* in_sizes, int n_in, void* d_out, int out_size, void* d_ws, size_t ws_size, hipStream_t stream) {
    Params p;
    p.x = (const u16*)d_in[0]; p.lar = (const u16*)d_in[1]; p.aim = (const u16*)d_in[2]; p.cr = (const u16*)d_in[3]; p.ci = (const u16*)d_in[4];
    p.ldt = (const u16*)d_in[5]; p.dsk = (const u16*)d_in[6]; p.w1 = (const u16*)d_in[8]; p.b1 = (const u16*)d_in[9];
    p.w2 = (const u16*)d_in[10]; p.b2 = (const u16*)d_in[11]; p.out = (u16*)d_out; p.ws = (char*)d_ws;
    (void)hipFuncSetAttribute((const void*)s4d_mixer_kernel, hipFuncAttributeMaxDynamicSharedMemorySize, LDS_BYTES);
    s4d_mixer_kernel<<<256, 256, LDS_BYTES, stream>>>(p);
}
```

```cpp
#include <hip/hip_runtime.h>
#include <stdint.h>
#include <utility>

typedef unsigned short u16;
typedef unsigned long long u64;
typedef unsigned v4u __attribute__((ext_vector_type(4)));
typedef unsigned v2u __attribute__((ext_vector_type(2)));
typedef float f2 __attribute__((ext_vector_type(2)));
typedef float f4 __attribute__((ext_vector_type(4)));
typedef float f16v __attribute__((ext_vector_type(16)));
typedef short s4v __attribute__((ext_vector_type(4)));
typedef __bf16 bf2v __attribute__((ext_vector_type(2)));
typedef __amdgpu_buffer_rsrc_t rsrc_t;
#define LAS __attribute__((address_space(3)))
#define CAS __attribute__((address_space(4)))
#define DI __device__ __forceinline__
#define PIN() __builtin_amdgcn_sched_barrier(0)
#define BARRIER() do { PIN(); asm volatile("s_waitcnt lgkmcnt(0)\n\ts_barrier" ::: "memory"); PIN(); } while (0)
#define VMWAIT(n) do { PIN(); asm volatile("s_waitcnt vmcnt(%0)" :: "i"(n) : "memory"); PIN(); } while (0)

#define FSTR 32
#define STAMPS 0

constexpr int SL = 2048, SD = 640, SF = 1728, NKS = SF / 32;
constexpr unsigned OOB = 0x40000000u;
constexpr unsigned OFF_W = 0, OFF_TMO = 128, OFF_F1 = 4096, OFF_F2 = OFF_F1 + 256 * FSTR, OFF_F3 = OFF_F2 + 256 * FSTR, OFF_DBG = 0x1000000;
constexpr unsigned OFF_STATS = 114688, OFF_XNB = 131072, OFF_W2T = OFF_XNB + SL * SD * 2, OFF_ST = OFF_W2T + SF * SD * 2;
constexpr unsigned OFF_H = OFF_ST + SL * SD * 2, WS_USED = OFF_H + SL * SF * 2;
constexpr unsigned CODE_LINES = 256;
static_assert(OFF_F3 + 256 * FSTR <= OFF_STATS, "workspace map");
constexpr unsigned SPIN_CAP = 1u << 16;

constexpr unsigned L_AUX = 131072, L_LNT = L_AUX, L_W2S = L_AUX + 10240, L_CS = L_AUX + 22528, L_B1 = L_CS + 512;
constexpr unsigned L_XY = 65536, XY_ROW = 528, XY_X = 32 * XY_ROW, XY_CH = 28160;
constexpr unsigned L_KP = 0, KP_CH = 4608, L_RC = L_KP + 2 * KP_CH, RC_COPY = 4160, RC_CH = 4 * RC_COPY;
constexpr unsigned L_U = L_RC + 2 * RC_CH, U_CH = 4608, L_RED2 = L_XY, L_R0 = L_XY + 2 * XY_CH;
constexpr unsigned L_U6 = L_XY + 16384, XY_BIAS = 38000;
constexpr unsigned L_SIMG = L_AUX, SIMG_ROW = 144, L_RED = 0;
constexpr unsigned LDS_BYTES = 163840;
static_assert(L_U + 2 * U_CH <= L_XY && L_R0 + 6 * RC_COPY <= LDS_BYTES && L_R0 <= L_AUX, "LDS map");

struct Params {
    const u16 *x, *lar, *aim, *cr, *ci, *ldt, *dsk, *w1, *b1, *w2, *b2;
    u16* out;
    char* ws;
};

template <int... Is, class Fn> DI void sfor_impl(std::integer_sequence<int, Is...>, Fn&& fn) { (fn(std::integral_constant<int, Is>{}), ...); }
template <int N, class Fn> DI void sfor(Fn&& fn) { sfor_impl(std::make_integer_sequence<int, N>{}, fn); }

DI unsigned rfl(unsigned v) { return __builtin_amdgcn_readfirstlane(v); }
DI unsigned sel(bool c, unsigned a, unsigned b) { return c ? a : b; }
DI unsigned opaque(unsigned v) { asm volatile("" : "+v"(v)); return v; }
DI float blo(unsigned d) { return __uint_as_float(d << 16); }
DI float bhi(unsigned d) { return __uint_as_float(d & 0xffff0000u); }
DI unsigned pk(float lo, float hi) { f2 v = {lo, hi}; return __builtin_bit_cast(unsigned, __builtin_convertvector(v, bf2v)); }
DI float rbf(float v) { return blo(pk(v, v)); }
DI float dot2(unsigned a, unsigned b, float c) { return __builtin_amdgcn_fdot2_f32_bf16(__builtin_bit_cast(bf2v, a), __builtin_bit_cast(bf2v, b), c, false); }
template <class T> DI T lds_ld(unsigned a) { return *(const LAS T*)(size_t)a; }
template <class T> DI void lds_st(unsigned a, T v) { *(LAS T*)(size_t)a = v; }
DI v2u lds_tr(unsigned a) { return __builtin_bit_cast(v2u, __builtin_amdgcn_ds_read_tr16_b64_v4i16((LAS s4v*)(size_t)a)); }

template <int CTRL> DI float dpp_add(float v) {
    return v + __builtin_bit_cast(float, __builtin_amdgcn_update_dpp(0, __builtin_bit_cast(int, v), CTRL, 0xf, 0xf, true));
}
DI float pair_sum(float a, float b) {
    const auto r = __builtin_amdgcn_permlane32_swap(__float_as_uint(a), __float_as_uint(b), false, false);
    float v = __uint_as_float(r[0]) + __uint_as_float(r[1]);
    v = dpp_add<0xB1>(v); v = dpp_add<0x4E>(v); v = dpp_add<0x141>(v); v = dpp_add<0x140>(v);
    return dpp_add<0x142>(v);
}
template <int LANE> DI float rdl(float v) { return __builtin_bit_cast(float, __builtin_amdgcn_readlane(__builtin_bit_cast(int, v), LANE)); }
DI float inv_sd(float var) { return __builtin_amdgcn_rcpf(rbf(__builtin_amdgcn_sqrtf(rbf(rbf(fmaxf(var, 0.f)) + 1e-5f)))); }

template <int IMM> DI void dma(unsigned vo, rsrc_t rs, unsigned so) {
    asm volatile("buffer_load_dwordx4 %0, %1, %2 offen offset:%3 lds" :: "v"(vo), "s"(rs), "s"(so), "i"(IMM));
}
DI void set_m0(unsigned v) { asm volatile("s_mov_b32 m0, %0\n\ts_nop 0" :: "s"(v)); }
template <int IMM> DI unsigned ld32(unsigned vo, rsrc_t rs, unsigned so) { unsigned r; asm volatile("buffer_load_dword %0, %1, %2, %3 offen offset:%4" : "=v"(r) : "v"(vo), "s"(rs), "s"(so), "i"(IMM)); return r; }
DI unsigned ld16(unsigned vo, rsrc_t rs, unsigned so) { unsigned r; asm volatile("buffer_load_ushort %0, %1, %2, %3 offen" : "=v"(r) : "v"(vo), "s"(rs), "s"(so)); return r; }
template <int IMM> DI v2u ld64(unsigned vo, rsrc_t rs, unsigned so) { v2u r; asm volatile("buffer_load_dwordx2 %0, %1, %2, %3 offen offset:%4" : "=v"(r) : "v"(vo), "s"(rs), "s"(so), "i"(IMM)); return r; }
template <int IMM> DI v2u ld64c(unsigned vo, rsrc_t rs, unsigned so) { v2u r; asm volatile("buffer_load_dwordx2 %0, %1, %2, %3 offen offset:%4 sc1" : "=v"(r) : "v"(vo), "s"(rs), "s"(so), "i"(IMM)); return r; }
template <int IMM> DI v4u ld128c(unsigned vo, rsrc_t rs, unsigned so) { v4u r; asm volatile("buffer_load_dwordx4 %0, %1, %2, %3 offen offset:%4 sc1" : "=v"(r) : "v"(vo), "s"(rs), "s"(so), "i"(IMM)); return r; }
template <int A, int IMM> DI void ld128a(unsigned vo, rsrc_t rs, unsigned so) {
    asm volatile("buffer_load_dwordx4 a[%c3:%c4], %0, %1, %2 offen offset:%5 sc1" :: "v"(vo), "s"(rs), "s"(so), "i"(A), "i"(A + 3), "i"(IMM));
}
template <int IMM> DI void st128c(v4u v, unsigned vo, rsrc_t rs, unsigned so) { asm volatile("buffer_store_dwordx4 %0, %1, %2, %3 offen offset:%4 sc1\n\ts_nop 1" :: "v"(v), "v"(vo), "s"(rs), "s"(so), "i"(IMM)); }
DI void st64c(v2u v, unsigned vo, rsrc_t rs, unsigned so) { asm volatile("buffer_store_dwordx2 %0, %1, %2, %3 offen sc1" :: "v"(v), "v"(vo), "s"(rs), "s"(so)); }
template <int IMM> DI void st64(v2u v, unsigned vo, rsrc_t rs, unsigned so) { asm volatile("buffer_store_dwordx2 %0, %1, %2, %3 offen offset:%4" :: "v"(v), "v"(vo), "s"(rs), "s"(so), "i"(IMM)); }
template <int IMM> DI void lds_st_tid(unsigned v) { asm volatile("ds_write_addtid_b32 %0 offset:%1" :: "v"(v), "i"(IMM)); }
DI void l2_touch(unsigned vo, rsrc_t rs, unsigned so) { asm volatile("buffer_load_dword a255, %0, %1, %2 offen" :: "v"(vo), "s"(rs), "s"(so)); }
template <class T> DI void tie(T& v) { asm volatile("" : "+v"(v)); }

DI void mfma32(f16v& c, v4u a, v4u b) { asm volatile("v_mfma_f32_32x32x16_bf16 %0, %1, %2, %0" : "+v"(c) : "v"(a), "v"(b)); }
DI void mfma16(f4& c, v4u a, v4u b) { asm volatile("v_mfma_f32_16x16x32_bf16 %0, %1, %2, %0" : "+v"(c) : "v"(a), "v"(b)); }
DI void mfma16z(f4& c, v4u a, v4u b) { asm volatile("v_mfma_f32_16x16x32_bf16 %0, %1, %2, 0" : "=&v"(c) : "v"(a), "v"(b)); }
DI void mfma32z(f16v& c, v4u a, v4u b) { asm volatile("v_mfma_f32_32x32x16_bf16 %0, %1, %2, 0" : "=&v"(c) : "v"(a), "v"(b)); }
template <int A> DI void mfma16a(f4& c, v4u b) { asm volatile("v_mfma_f32_16x16x32_bf16 %0, a[%c2:%c3], %1, %0" : "+v"(c) : "v"(b), "i"(A), "i"(A + 3)); }
template <int A> DI void mfma16az(f4& c, v4u b) { asm volatile("v_mfma_f32_16x16x32_bf16 %0, a[%c2:%c3], %1, 0" : "=&v"(c) : "v"(b), "i"(A), "i"(A + 3)); }
#define MFMA_DRAIN() do { PIN(); asm volatile("s_nop 15" ::: "memory"); PIN(); } while (0)

DI f2 cmul(f2 p, f2 w) {
    f2 r;
    asm volatile("v_pk_mul_f32 %0, %1, %2 op_sel:[0,0] op_sel_hi:[0,1]\n\tv_pk_fma_f32 %0, %1, %2, %0 op_sel:[1,1,0] op_sel_hi:[1,0,1] neg_lo:[0,1,0]" : "=&v"(r) : "v"(p), "v"(w));
    return r;
}
DI f2 cmul_s(f2 p, f2 w) {
    float a, b;
    asm volatile("v_mul_f32 %0, %2, %4\n\tv_mul_f32 %1, %2, %5\n\tv_fma_f32 %0, -%3, %5, %0\n\tv_fma_f32 %1, %3, %4, %1" : "=&v"(a), "=&v"(b) : "v"(p.x), "v"(p.y), "v"(w.x), "v"(w.y));
    return f2{a, b};
}
DI float fmul_s(float a, float b) { float r; asm("v_mul_f32 %0, %1, %2" : "=v"(r) : "v"(a), "v"(b)); return r; }
DI f2 cexp_at(float er, float rev, float p) {
    const float m = __builtin_amdgcn_exp2f(er * p), fr = __builtin_amdgcn_fractf(rev * p);
    const float c = __builtin_amdgcn_cosf(fr) * m;
    float s = __builtin_amdgcn_sinf(fr);
    asm volatile("" : "+v"(s));
    return f2{c, s * m};
}

#define ACLOB_16(n) "a" #n "0", "a" #n "1", "a" #n "2", "a" #n "3", "a" #n "4", "a" #n "5", "a" #n "6", "a" #n "7", "a" #n "8", "a" #n "9"
#define ACLOB_ALL "a0", "a1", "a2", "a3", "a4", "a5", "a6", "a7", "a8", "a9", ACLOB_16(1), ACLOB_16(2), ACLOB_16(3), ACLOB_16(4), ACLOB_16(5), ACLOB_16(6), \
    ACLOB_16(7), ACLOB_16(8), ACLOB_16(9), ACLOB_16(10), ACLOB_16(11), ACLOB_16(12), ACLOB_16(13), ACLOB_16(14), ACLOB_16(15), ACLOB_16(16), ACLOB_16(17), \
    ACLOB_16(18), ACLOB_16(19), ACLOB_16(20), ACLOB_16(21), ACLOB_16(22), ACLOB_16(23), ACLOB_16(24), "a250", "a251", "a252", "a253", "a254", "a255"

struct Ctx {
    rsrc_t rws;
    unsigned e_lo, e_hi;
    unsigned tid, lane, w, w16a, w16b, flag_vo;
    unsigned thr, w2a, w2b, w2l, w2so;
};
#define STAMP(i) do {} while (0)
#define CYC(i) do {} while (0)
template <int N> using ic = std::integral_constant<int, N>;

DI bool flags_match(v2u v, const Ctx& cx) { return __all(v.x == cx.e_lo && v.y == cx.e_hi); }
DI void publish(const Ctx& cx, unsigned so) { st64c(v2u{cx.e_lo, cx.e_hi}, cx.flag_vo, cx.rws, so); }
template <int SLEEP> DI void poll(unsigned vo, const Ctx& cx) {
#pragma unroll 1
    for (unsigned spins = 0;; ++spins) {
        v2u v = ld64c<0>(vo, cx.rws, 0);
        VMWAIT(0); tie(v);
        if (flags_match(v, cx)) break;
        if (spins > SPIN_CAP) { st64c(v2u{1u, 1u}, cx.lane == 0 ? OFF_TMO : OOB, cx.rws, 0); break; }
        __builtin_amdgcn_s_sleep(SLEEP);
    }
}
template <int S, int STRIDE> DI unsigned slot_off(unsigned base, unsigned wrapped, unsigned last, unsigned thr) {
    if constexpr (S == 13) return last;
    unsigned r;
    asm volatile("s_cmp_le_u32 %3, %4\n\ts_cselect_b32 %0, %2, %1\n\ts_add_u32 %0, %0, %5" : "=&s"(r) : "s"(base), "s"(wrapped), "s"(thr), "i"(S), "i"(S * STRIDE) : "scc");
    return r;
}
template <int LO, int HI> DI void w2_prefetch(Ctx& cx) {
    sfor<HI - LO>([&](auto ii) {
        constexpr int f = LO + ii, s = f / 5, c = f % 5;
        if constexpr (c == 0) cx.w2so = slot_off<s, 5120>(cx.w2a, cx.w2b, cx.w2l, cx.thr);
        if constexpr (c < 4) ld128a<4 * f, c * 1024>(cx.w16a, cx.rws, cx.w2so); else ld128a<4 * f, 0>(cx.w16b, cx.rws, cx.w2so);
    });
}

template <class H1, class H2> DI f2 layernorm_rows(const unsigned (&xa)[5], const unsigned (&xb)[5], const Ctx& cx, H1&& hook1, H2&& hook2) {
    constexpr unsigned ONES = 0x3f803f80u;
    float sa = 0.f, sb = 0.f;
    sfor<5>([&](auto i) { sa = dot2(xa[i], ONES, sa); sb = dot2(xb[i], ONES, sb); });
    const float mean = pair_sum(sa, sb) * (1.f / SD), mr = rbf(mean), dm = mean - mr;
    const float mra = rdl<31>(mr), mrb = rdl<63>(mr);
    PIN(); hook1(); PIN();
    unsigned ta[5], tb[5];
    f2 qa = {0.f, 0.f}, qb = {0.f, 0.f};
    sfor<5>([&](auto i) {
        const f2 da = f2{blo(xa[i]), bhi(xa[i])} - mra, db = f2{blo(xb[i]), bhi(xb[i])} - mrb;
        qa = da * da + qa; qb = db * db + qb;
        ta[i] = pk(da.x, da.y); tb[i] = pk(db.x, db.y);
    });
    const float r1 = inv_sd(pair_sum(qa.x + qa.y, qb.x + qb.y) * (1.f / SD) - dm * dm);
    const float ra = rdl<31>(r1), rb = rdl<63>(r1);
    PIN(); hook2(); PIN();
    float s0 = 0.f, s1 = 0.f, s2 = 0.f, s3 = 0.f;
    unsigned o[10];
    sfor<5>([&](auto i) {
        const f2 na = f2{blo(ta[i]), bhi(ta[i])} * ra, nb = f2{blo(tb[i]), bhi(tb[i])} * rb;
        const unsigned ua = pk(na.x, na.y), ub = pk(nb.x, nb.y);
        s0 = dot2(ua, ONES, s0); s1 = dot2(ua, ua, s1); s2 = dot2(ub, ONES, s2); s3 = dot2(ub, ub, s3);
        o[2 * i] = __builtin_amdgcn_perm(ub, ua, 0x05040100u); o[2 * i + 1] = __builtin_amdgcn_perm(ub, ua, 0x07060302u);
    });
    const unsigned la = opaque(L_LNT + cx.w * 2560 + cx.lane * 16), lb = opaque(L_LNT + cx.w * 2560 + 2048 + cx.lane * 8);
    lds_st<v4u>(la, v4u{o[0], o[1], o[2], o[3]});
    lds_st<v4u>(la + 1024, v4u{o[4], o[5], o[6], o[7]});
    lds_st<v2u>(lb, v2u{o[8], o[9]});
    const float m2 = pair_sum(s0, s2) * (1.f / SD), r2 = inv_sd(pair_sum(s1, s3) * (1.f / SD) - m2 * m2);
    return f2{r1 * r2, -(mr * r1 + rbf(m2)) * r2};
}
DI void store_xn(const Ctx& cx, f2 st, unsigned tb) {
    const unsigned a = opaque(L_LNT + cx.tid * 4);
    v4u v[3];
    sfor<3>([&](auto ii) { v[ii] = v4u{lds_ld<unsigned>(a + ii * 1024), lds_ld<unsigned>(a + ii * 1024 + 2560), lds_ld<unsigned>(a + ii * 1024 + 5120), lds_ld<unsigned>(a + ii * 1024 + 7680)}; });
    const unsigned so = OFF_XNB + tb * 10240, vo = cx.tid * 16;
    st128c<0>(v[0], vo, cx.rws, so);
    st128c<0>(v[1], vo, cx.rws, so + 4096);
    st128c<0>(v[2], cx.w < 2 ? vo : OOB, cx.rws, so + 8192);
    st64c(v2u{__float_as_uint(st.x), __float_as_uint(st.y)}, sel((cx.lane & 31) == 31, (cx.lane >> 5) * 8, OOB), cx.rws, OFF_STATS + (8 * tb + 2 * cx.w) * 8);
}

struct Kgen {
    unsigned la, ai, cr, ci, ldt;
    float dt, er, rev;
    f2 w8, w32, v1, v2, v256, y[4], x[8], last;
    unsigned row[53];
};
constexpr int KG_STEPS = 57;
constexpr int kg_row(int I) { return I == 1 ? 32 : I == 4 ? 1 : I == 5 ? 4 : I < 7 ? -1 : I < 27 ? I + 26 : I < 29 ? I - 25 : I < 56 ? I - 24 : -1; }
template <int I, bool BESIDE_MFMA> DI void kg_step(Kgen& k) {
    auto mul = [](f2 a, f2 b) { if constexpr (BESIDE_MFMA) return cmul_s(a, b); else return cmul(a, b); };
    constexpr float LOG2E = 1.44269504088896f, INV2PI = 0.159154943091895f;
    const f2 prev = k.last;
    if constexpr (I == 0) { k.dt = rbf(__builtin_amdgcn_exp2f(blo(k.ldt) * LOG2E)); k.er = rbf(__builtin_amdgcn_exp2f(blo(k.la) * LOG2E)); tie(k.dt); tie(k.er); k.row[0] = 0x3f80u; }
    else if constexpr (I == 1) { k.er = (-k.er * k.dt) * LOG2E; k.rev = (blo(k.ai) * k.dt) * INV2PI; k.x[0] = f2{fmul_s(blo(k.cr), k.dt) * 2.f, fmul_s(blo(k.ci), -2.f * k.dt)}; tie(k.er); tie(k.rev); tie(k.x[0]); k.last = k.x[0]; }
    else if constexpr (I == 2) { k.v1 = cexp_at(k.er, -k.rev, 1.f); tie(k.v1); }
    else if constexpr (I == 3) { k.v256 = cexp_at(k.er, -k.rev, 256.f); tie(k.v256); }
    else if constexpr (I == 4) { k.w8 = cexp_at(k.er, k.rev, 8.f); tie(k.w8); k.last = k.w8; }
    else if constexpr (I == 5) { k.w32 = cexp_at(k.er, k.rev, 32.f); tie(k.w32); k.last = k.w32; }
    else if constexpr (I == 6) { k.v2 = mul(k.v1, k.v1); }
    else if constexpr (I == 7) { k.last = k.x[1] = mul(k.x[0], k.v1); }
    else if constexpr (I < 14) { constexpr int i = I - 6; k.last = k.x[i] = mul(k.x[i - 2], k.v2); }
    else if constexpr (I < 18) { constexpr int i = 2 * (I - 14); k.last = k.x[i] = mul(k.x[i], k.v256); }
    else if constexpr (I < 22) { constexpr int i = 4 * (I & 1); k.last = k.x[i] = mul(k.x[i], k.v256); }
    else if constexpr (I < 27) { k.last = k.x[0] = mul(k.x[0], k.v256); }
    else if constexpr (I == 27) { k.y[0] = k.w32; k.y[1] = k.w8; k.last = k.y[2] = mul(k.w8, k.w8); }
    else if constexpr (I == 28) { k.last = k.y[3] = mul(k.y[2], k.w8); }
    else if constexpr (I < 56) { constexpr int j = (I - 24) & 3; k.last = k.y[j] = mul(k.y[j], k.w32); }
    if constexpr (I > 0 && kg_row(I - 1) >= 0) {
        k.row[kg_row(I - 1)] = pk(prev.x, prev.y); tie(k.row[kg_row(I - 1)]);
        if constexpr (!BESIDE_MFMA) lds_st_tid<XY_BIAS + kg_row(I - 1) * XY_ROW>(k.row[kg_row(I - 1)]);
    }
    if constexpr (!BESIDE_MFMA && I == 2) lds_st_tid<XY_BIAS>(k.row[0]);
}
template <int LO, int HI> DI void kg_write(const Kgen& k) {
    sfor<HI - LO>([&](auto ii) { constexpr int r = LO + ii; if constexpr (r < 53) lds_st_tid<XY_BIAS + r * XY_ROW>(k.row[r]); });
}
DI void kg_set_base(const Ctx& cx) { set_m0(L_XY - XY_BIAS + (cx.w >> 1) * XY_CH + (cx.w & 1) * 256); }

DI void kernel_table(const Ctx& cx) {
    const unsigned cidx = cx.w >> 1, half = cx.w & 1, r = cx.lane & 15, g4 = cx.lane >> 4;
    const unsigned xa = opaque(L_XY + cidx * XY_CH + XY_X + r * XY_ROW + g4 * 16), ya = opaque(xa - XY_X + half * (16 * XY_ROW));
    f4 k0, k1;
    v4u a[8], b0[8], b1[8];
    sfor<8>([&](auto ks) { a[ks] = lds_ld<v4u>(ya + ks * 64); b0[ks] = lds_ld<v4u>(xa + ks * 64); b1[ks] = lds_ld<v4u>(xa + 16 * XY_ROW + ks * 64); PIN(); });
    const unsigned i0 = sel(r < 8, r, sel(r < 12, 240 + 2 * r, 128 * (r & 14) - 1024 + 4 * (r & 1)));
    const unsigned i1 = sel(r <= 4, 1024 + 256 * r, 2049);
    const unsigned kp = L_KP + cidx * KP_CH + 256 * half + 64 * g4, a0 = opaque(kp + 2 * i0), a1 = opaque(kp + 2 * i1);
    PIN();
    sfor<8>([&](auto ks) { if constexpr (ks == 0) { mfma16z(k0, a[ks], b0[ks]); mfma16z(k1, a[ks], b1[ks]); } else { mfma16(k0, a[ks], b0[ks]); mfma16(k1, a[ks], b1[ks]); } });
    PIN(); asm volatile("s_nop 7" : "+v"(k0), "+v"(k1)); PIN();
    sfor<2>([&](auto e2) {
        const unsigned v0 = pk(k0[2 * e2], k0[2 * e2 + 1]), v1 = pk(k1[2 * e2], k1[2 * e2 + 1]);
        lds_st<u16>(a0 + 32 * e2, (u16)v0); lds_st<u16>(a0 + 32 * e2 + 16, (u16)(v0 >> 16));
        lds_st<u16>(a1 + 32 * e2, (u16)v1); lds_st<u16>(a1 + 32 * e2 + 16, (u16)(v1 >> 16));
    });
}
DI void expand_taps(const Ctx& cx, unsigned r0) {
    const unsigned m = 64 * (cx.w & 1) + cx.lane, kp = opaque(L_KP + (cx.w >> 1) * KP_CH + 16 * m);
    const v4u d = lds_ld<v4u>(kp);
    const unsigned d4 = lds_ld<unsigned>(kp + 16), e0 = lds_ld<unsigned>(kp + 2048), e4 = lds_ld<unsigned>(kp + 2048 + 16);
    const bool s1 = m < 32, s3 = cx.w & 1;
    const unsigned sel0 = s1 ? 0x01000302u : 0x01000100u, sel1 = s1 ? 0x01000302u : 0x01000504u, sel3 = s3 ? 0x05040504u : sel1;
    v4u o0, o1;
    o0.w = __builtin_amdgcn_perm(d.x, d.x, sel0);
    o0.z = __builtin_amdgcn_perm(d.z, s3 ? d.x : d.y, sel1);
    o0.y = __builtin_amdgcn_perm(d.z, d.z, sel0);
    o0.x = __builtin_amdgcn_perm(d4, d.w, sel3);
    o1.w = __builtin_amdgcn_perm(e0, e0, 0x01000100u);
    o1.z = o1.w;
    o1.y = __builtin_amdgcn_perm(e4, e0, (m & 1) ? 0x05040504u : 0x01000504u);
    o1.x = __builtin_amdgcn_perm(e4, e4, 0x01000100u);
    const unsigned a = opaque(r0 + 16 * (127 - m));
    lds_st<v4u>(a + 2048, o0);
    lds_st<v4u>(a, o1);
    lds_st<v4u>(r0 + 4096 + 16 * (m & 1), v4u{0, 0, 0, 0});
}
template <bool COPY0> DI void shift_taps(const Ctx& cx, unsigned r0) {
    const unsigned m = 64 * (cx.w & 1) + cx.lane, src = opaque(r0 + 16 * m), dst = opaque(L_RC + (cx.w >> 1) * RC_CH + 16 * m);
    sfor<2>([&](auto it) {
        const v4u d = lds_ld<v4u>(src + it * 2048);
        const v2u n = lds_ld<v2u>(src + it * 2048 + 16);
        const unsigned q0 = __builtin_amdgcn_alignbyte(d.y, d.x, 2), q1 = __builtin_amdgcn_alignbyte(d.z, d.y, 2), q2 = __builtin_amdgcn_alignbyte(d.w, d.z, 2);
        const unsigned q3 = __builtin_amdgcn_alignbyte(n.x, d.w, 2), q4 = __builtin_amdgcn_alignbyte(n.y, n.x, 2);
        if constexpr (COPY0) lds_st<v4u>(dst + it * 2048, d);
        lds_st<v4u>(dst + it * 2048 + RC_COPY, v4u{q0, q1, q2, q3});
        lds_st<v4u>(dst + it * 2048 + 2 * RC_COPY, v4u{d.y, d.z, d.w, n.x});
        lds_st<v4u>(dst + it * 2048 + 3 * RC_COPY, v4u{q1, q2, q3, q4});
    });
    lds_st<v2u>(L_RC + ((cx.lane & 31) >> 2) * RC_COPY + 4096 + (cx.lane & 3) * 8, v2u{0, 0});
}
template <class HOOK, class POST> DI void conv_pair(Ctx& cx, unsigned ch, unsigned ubase, float dskip, HOOK&& hook, POST&& post) {
    const unsigned cidx = cx.w >> 1, half = cx.w & 1, r = cx.lane & 15, g4 = cx.lane >> 4;
    f4 ca[8];
    {
        const unsigned aa0 = L_RC + cidx * RC_CH + ((~r) & 3) * RC_COPY + 2 * (2028 - 4 * (r >> 2) + 8 * g4) - 3584 - 256 * half - 192;
        const unsigned aa = opaque(aa0), ab = opaque(aa0 + 8), bb = opaque(ubase + 448 + 32 * r + 16 * g4 - 256 * half - 192);
        v2u al[2][8], ah[2][8];
        v4u fb[2][8];
        auto rd = [&](auto d_, auto i_) {
            constexpr int d = d_, i = i_, e = i / 3, o = (3 - d) * 64;
            if constexpr (i % 3 == 0) al[d & 1][e] = lds_ld<v2u>(aa + (7 - e) * 512 + o);
            else if constexpr (i % 3 == 1) ah[d & 1][e] = lds_ld<v2u>(ab + (7 - e) * 512 + o);
            else fb[d & 1][e] = lds_ld<v4u>(bb + e * 512 + o);
            PIN();
        };
        sfor<12>([&](auto i) { rd(ic<0>{}, i); });
        sfor<4>([&](auto d_) {
            constexpr int d = d_;
            sfor<8>([&](auto n_) {
                constexpr int n = n_;
                sfor<n + 1>([&](auto e_) {
                    constexpr int e = e_, idx = n * (n + 1) / 2 + e;
                    const v4u a = {al[d & 1][e].x, al[d & 1][e].y, ah[d & 1][e].x, ah[d & 1][e].y};
                    if constexpr (d == 0 && e == 0) mfma16z(ca[n], a, fb[d & 1][n - e]); else mfma16(ca[n], a, fb[d & 1][n - e]);
                    PIN();
                    if constexpr (d == 0) { if constexpr (idx < 6) { rd(ic<0>{}, ic<12 + 2 * idx>{}); rd(ic<0>{}, ic<13 + 2 * idx>{}); } else if constexpr (idx >= 12) rd(ic<1>{}, ic<idx - 12>{}); }
                    else if constexpr (d < 3 && idx % 3 != 2) rd(ic<d + 1>{}, ic<idx - idx / 3>{});
                });
            });
            hook(d_);
        });
    }
    STAMP(7);
    post();
    PIN(); asm volatile("s_nop 7"); PIN();
    sfor<8>([&](auto n) { tie(ca[n]); });
    const unsigned ra = opaque(L_RED2 + cx.lane * 16 + cx.w * 4096), rb = opaque(L_RED2 + cx.lane * 16 + (cx.w ^ 1) * 4096);
    if (half == 0) { sfor<4>([&](auto i) { lds_st<f4>(ra + i * 1024, ca[4 + i]); }); }
    else { sfor<4>([&](auto i) { lds_st<f4>(ra + i * 1024, ca[i]); ca[i] = ca[4 + i]; }); }
    const unsigned uaddr = opaque(ubase + 480 + 2 * (1024 * half + 16 * r + 4 * g4));
    f4 uf[4];
    sfor<4>([&](auto i) { const v2u u = lds_ld<v2u>(uaddr + i * 512); uf[i] = f4{blo(u.x), bhi(u.x), blo(u.y), bhi(u.y)} * dskip + ca[i]; });
    BARRIER();
    v2u pkd[4];
    sfor<4>([&](auto i) { const f4 y = uf[i] + lds_ld<f4>(rb + i * 1024); pkd[i] = v2u{pk(y[0], y[1]), pk(y[2], y[3])}; });
    const unsigned vo = 2 * (256 * (g4 & 1) + 16 * r + 8 * (g4 >> 1)), so = OFF_ST + ch * 4096 + 2048 * half;
    sfor<2>([&](auto i2) {
        const auto sx = __builtin_amdgcn_permlane16_swap(pkd[2 * i2].x, pkd[2 * i2 + 1].x, false, false);
        const auto sy = __builtin_amdgcn_permlane16_swap(pkd[2 * i2].y, pkd[2 * i2 + 1].y, false, false);
        st128c<i2 * 1024>(v4u{sx[0], sy[0], sx[1], sy[1]}, vo, cx.rws, so);
    });
}

__global__ void __launch_bounds__(256, 1) s4d_mixer_kernel(Params p) {
    extern __shared__ __attribute__((aligned(16))) char smem[];
    const unsigned tid = threadIdx.x, lane = tid & 63, w = rfl(tid >> 6);
    const unsigned b = blockIdx.x, g = b & 7, q = b >> 3;
    {
        u64 pc;
        asm volatile("s_getpc_b64 %0" : "=s"(pc) :: ACLOB_ALL);
        const unsigned line = (q & 7) * (CODE_LINES / 8) + w * (CODE_LINES / 32) + (lane & (CODE_LINES / 32 - 1));
        asm volatile("global_load_dword a255, %0, %1" :: "v"(line * 128u), "s"(pc & ~127ull));
    }
    asm volatile("" :: "v"((unsigned)(size_t)(LAS char*)smem));
    Ctx cx;
    STAMP(0);
    const unsigned tb = 32 * g + q;
    const rsrc_t rx = __builtin_amdgcn_make_buffer_rsrc((void*)p.x, 0, SL * SD * 2, 0x00020000);
    const rsrc_t rw1 = __builtin_amdgcn_make_buffer_rsrc((void*)p.w1, 0, SD * SF * 2, 0x00020000);
    unsigned xa[5], xb[5];
    v2u xa0, xa1, xb0, xb1;
    {
        const unsigned so = (8 * tb + 2 * w) * (SD * 2);
        asm volatile("s_nop 4");
        xa0 = ld64<0>(lane * 8, rx, so); xa1 = ld64<512>(lane * 8, rx, so); xa[4] = ld32<1024>(lane * 4, rx, so);
        xb0 = ld64<1280>(lane * 8, rx, so); xb1 = ld64<1280 + 512>(lane * 8, rx, so); xb[4] = ld32<1280 + 1024>(lane * 4, rx, so);
    }
    const unsigned tx = (512 * (g >> 1) + 16 * q + (lane & 15)) * 1280 + (lane >> 4) * 128;
    unsigned tw;
    {
        const unsigned st = lane >= 28, i = lane - 28 * st, hi = i >= 14;
        tw = sel(lane < 56, (64 * st + 2 * q + hi) * (SF * 2) + (g & 1) * 1792 + (i - 14 * hi) * 128, OOB);
    }
    if (w == 1) l2_touch(tx, rx, 0); else l2_touch(tw, rw1, sel(w == 0, 0, 2 * w - 2) * (64 * SF * 2));
    auto touch2 = [&]() {
        if (w == 0 || w == 3) l2_touch(sel(w == 0 || lane < 32, tx, OOB), rx, sel(w == 0, 4, 8) * 128);
        else l2_touch(tw, rw1, (4 + 2 * w) * (64 * SF * 2));
    };
    cx.rws = __builtin_amdgcn_make_buffer_rsrc((void*)p.ws, 0, WS_USED, 0x00020000);
    cx.tid = tid; cx.lane = lane; cx.w = w;
    v2u wv = ld64c<0>(opaque(OFF_W), cx.rws, 0);
    const bool spare = q >= 28;
    const unsigned ch0 = 80 * g + sel(spare, 6 * q - 112, 2 * q), cidx = w >> 1;
    const rsrc_t rla = __builtin_amdgcn_make_buffer_rsrc((void*)p.lar, 0, SD * 256, 0x00020000);
    const rsrc_t rai = __builtin_amdgcn_make_buffer_rsrc((void*)p.aim, 0, SD * 256, 0x00020000);
    const rsrc_t rcr = __builtin_amdgcn_make_buffer_rsrc((void*)p.cr, 0, SD * 256, 0x00020000);
    const rsrc_t rci = __builtin_amdgcn_make_buffer_rsrc((void*)p.ci, 0, SD * 256, 0x00020000);
    const unsigned pvo = w * 128 + lane * 2;
    Kgen kg;
    kg.last = f2{0.f, 0.f};
    kg.la = ld16(pvo, rla, ch0 * 256); kg.ai = ld16(pvo, rai, ch0 * 256); kg.cr = ld16(pvo, rcr, ch0 * 256); kg.ci = ld16(pvo, rci, ch0 * 256);
    const unsigned rt = 4 * g + (q >> 3), ct = q & 7;
    const unsigned nk = 14 - (w >> 1), rot = (ct * 13) >> 3, k0s = 14 * w - (w == 3 ? 1 : 0) + rot, klast = k0s + 13 - (rot ? 14 : 0);
    auto late_setup = [&]() {
        sfor<2>([&](auto i) { tie(xa0); tie(xa1); tie(xb0); tie(xb1); });
        tie(xa[4]); tie(xb[4]);
        xa[0] = xa0.x; xa[1] = xa0.y; xa[2] = xa1.x; xa[3] = xa1.y; xb[0] = xb0.x; xb[1] = xb0.y; xb[2] = xb1.x; xb[3] = xb1.y;
    };
    auto take_epoch = [&]() {
        tie(wv); tie(kg.la); tie(kg.ai); tie(kg.cr); tie(kg.ci);
        const u64 e = ((u64)rfl(wv.y) << 32 | rfl(wv.x)) + 0x9E3779B97F4A7C15ull;
        cx.e_lo = (unsigned)e; cx.e_hi = (unsigned)(e >> 32);
        cx.flag_vo = tid == 0 ? 0u : OOB;
        cx.w16a = lane * 16; cx.w16b = lane * 16 + 4096;
        cx.thr = nk - rot; cx.w2a = OFF_W2T + (ct * NKS + k0s) * 5120; cx.w2b = cx.w2a - nk * 5120;
        cx.w2l = sel(w < 2, OFF_W2T + (ct * NKS + klast) * 5120, WS_USED); cx.w2so = 0;
    };
    auto half16 = [&](const u16* base, unsigned ch) { const unsigned v = *(const CAS unsigned*)(base + ch); return (v >> (16 * cidx)) & 0xffffu; };

    const unsigned ti = 4 * (g >> 1) + q / 7, tj = 7 * (g & 1) + q % 7, wf = w >> 1, wt = w & 1;
    unsigned gx[4], gw[4];
    sfor<4>([&](auto ii) {
        gx[ii] = (32 * w + 8 * ii + (lane >> 3)) * 1280 + 16 * ((lane & 7) ^ (4 * (ii & 1) + (lane >> 4))) - ii * 1024;
        gw[ii] = (16 * w + 4 * ii + (lane >> 4)) * (SF * 2) + 16 * ((lane & 15) ^ (((lane >> 4) << 2) | ii)) - ii * 1024;
    });
    const unsigned sx0 = ti * (128 * 1280), sw0 = tj * 256;
    auto piece = [&](auto s_, auto pi_) {
        constexpr int s = s_, pi = pi_, ii = pi & 3;
        if constexpr (ii == 0) set_m0((s & 3) * 32768 + (pi >> 2) * 16384 + w * 4096);
        if constexpr (pi < 4) dma<ii * 1024>(gx[ii], rx, sx0 + s * 128); else dma<ii * 1024>(gw[ii], rw1, sw0 + s * (64 * SF * 2));
    };
    auto stage = [&](auto s_) { sfor<8>([&](auto pi) { piece(s_, pi); }); };
    const unsigned sid = 27 * g + sel(q < 26, q, 26), sks = sid >> 2, sqq = sid & 3;
    const rsrc_t rb1 = __builtin_amdgcn_make_buffer_rsrc((void*)p.b1, 0, SF * 2, 0x00020000);
    unsigned b1v = ld16((tid & 127) * 2, rb1, tj * 256);
    if (!spare) {
        const rsrc_t rw2 = __builtin_amdgcn_make_buffer_rsrc((void*)p.w2, 0, SD * SF * 2, 0x00020000);
        set_m0(L_W2S + w * 3072);
        sfor<3>([&](auto ii) {
            const unsigned P = 64 * (3 * w + ii) + lane, row = (P * 3277) >> 16, vo = row * (1280 - 320) + P * 16 - ii * 1024;
            dma<ii * 1024>(sel(ii == 0 || w < 3, vo, OOB), rw2, sks * (32 * 1280) + sqq * 320);
        });
        VMWAIT(10);
    } else VMWAIT(7);
    late_setup();
    STAMP(1);
    const f2 st = layernorm_rows(xa, xb, cx, [&]() { if (!spare) stage(ic<0>{}); }, [&]() { touch2(); if (!spare) stage(ic<1>{}); });
    if (!spare) VMWAIT(17); else VMWAIT(1);
    take_epoch(); tie(b1v);
    BARRIER();
    store_xn(cx, st, tb);
    unsigned ubase;

    if (!spare) {
        {
            const unsigned g4 = lane >> 4, a0 = opaque(L_W2S + (16 * (g4 >> 1) + 4 * (g4 & 1) + ((lane & 15) >> 2)) * 320 + 8 * (lane & 3) + w * 96);
            v4u fr[3];
            sfor<3>([&](auto ii) { const v2u lo = lds_tr(a0 + ii * 32), hi = lds_tr(a0 + ii * 32 + 8 * 320); fr[ii] = v4u{lo.x, lo.y, hi.x, hi.y}; });
            sfor<3>([&](auto ii) {
                const unsigned fi = 3 * w + ii;
                st128c<0>(fr[ii], cx.w16a, cx.rws, sel(fi < 10, OFF_W2T + (2 * sqq * NKS + sks) * 5120 + fi * 1024 + sel(fi >= 5, (NKS - 1) * 5120, 0), WS_USED));
            });
        }
        stage(ic<2>{});
        kg.ldt = half16(p.ldt, ch0);
        STAMP(2);
        unsigned ax[2][4], aw[2][4];
        {
            const unsigned row = 64 * wt + (lane & 31), hh = lane >> 5, blk = (lane >> 4) & 1, qr = (lane & 15) >> 2, pp = lane & 3;
            sfor<4>([&](auto ks) { ax[0][ks] = opaque(row * 128 + 16 * ((2 * ks + hh) ^ ((row >> 1) & 7))); ax[1][ks] = opaque(ax[0][ks] + 65536); });
            sfor<4>([&](auto i) {
                constexpr int mf = i >> 1, t = i & 1;
                const unsigned r = 8 * hh + 4 * t + qr, chn = 4 * (2 * wf + (mf ^ wt)) + 2 * blk + (pp >> 1);
                aw[0][i] = opaque(16384 + 256 * r + 16 * (chn ^ ((qr << 2) | ((2 * hh + t) & 3))) + 8 * (pp & 1)); aw[1][i] = opaque(aw[0][i] + 65536);
            });
        }
        const unsigned onesel = ((lane ^ (lane >> 4)) & 1) ? 0u : 0x3f803f80u;
        v4u ones = {onesel, onesel, onesel, onesel};
        tie(ones);
        constexpr int W7 = 4, W8 = 4, W9 = 4, WG0 = 12, WG1 = 28;
        f16v acc[2][2];
        f4 csum;
        v2u fa[2][4], plv, st0, st1;
        v4u fb[2][2], ub0, ub1;
        auto rd = [&](auto n_, auto i_) {
            constexpr int n = n_, i = i_, s = n >> 2, ks = n & 3, bf = n & 1, hi = (s & 3) >> 1;
            constexpr unsigned so = (s & 1) * 32768;
            if constexpr (n < 40) {
                if constexpr (i == 0) fb[bf][0] = lds_ld<v4u>(ax[hi][ks] + so);
                else if constexpr (i == 3) fb[bf][1] = lds_ld<v4u>(ax[hi][ks] + so + 4096);
                else fa[bf][i < 3 ? i - 1 : i - 2] = lds_tr(aw[hi][i < 3 ? i - 1 : i - 2] + so + ks * 4096);
            }
            PIN();
        };
        VMWAIT(24);
        BARRIER();
        sfor<6>([&](auto i) { rd(ic<0>{}, i); });
        piece(ic<3>{}, ic<0>{}); piece(ic<3>{}, ic<1>{});
        sfor<40>([&](auto n0_) {
            constexpr int n = n0_ + 1, s = n >> 2, ks = n & 3, bf = (n - 1) & 1;
            constexpr bool first = n == 1;
            if constexpr (ks == 0 && n < 40) {
                constexpr int WAITS[10] = {0, 23, 16, 17, 17, 16, 17, 16, 8 + 2 + W7, W7 + W8};
                VMWAIT(WAITS[s]);
                if constexpr (s == 7) { tie(plv); if (!flags_match(plv, cx)) poll<2>(OFF_F1 + tid * FSTR, cx); }
                BARRIER();
                if constexpr (s == 2) publish(cx, OFF_F1 + tb * FSTR);
                if constexpr (s == 5) plv = ld64c<0>(tid * FSTR, cx.rws, OFF_F1);
                if constexpr (s == 7) {
                    const unsigned vo = (64 * wt + (lane & 31)) * 8;
                    st0 = ld64c<0>(vo, cx.rws, OFF_STATS + ti * 1024); st1 = ld64c<256>(vo, cx.rws, OFF_STATS + ti * 1024);
                }
                if constexpr (s == 8) kg_set_base(cx);
                PIN();
            }
            auto vm = [&](auto m_) {
                constexpr int pi = 2 * ks + m_;
                if constexpr (s <= 6) piece(ic<s + 3>{}, ic<pi>{});
                else if constexpr (s <= 9 && m_ == 0) w2_prefetch<(s - 7) * 4 + ks, (s - 7) * 4 + ks + 1>(cx);
                PIN();
            };
            auto side = [&](auto m_) {
                constexpr int m = m_;
                if constexpr (n <= 6) { if constexpr (m == 0) kg_step<n - 1, true>(kg); }
                else if constexpr (n < 32) { kg_step<6 + 2 * (n - 7) + m, true>(kg); if constexpr (n == 31 && m == 1) kg_step<56, true>(kg); }
                else if constexpr (n < 40) kg_write<(n - 32) * 7 + 4 * m, (n - 32) * 7 + 4 + 3 * m>(kg);
                PIN();
            };
            const v4u a0 = {fa[bf][0].x, fa[bf][0].y, fa[bf][1].x, fa[bf][1].y}, a1 = {fa[bf][2].x, fa[bf][2].y, fa[bf][3].x, fa[bf][3].y};
            if constexpr (first) mfma32z(acc[0][0], a0, fb[bf][0]); else mfma32(acc[0][0], a0, fb[bf][0]);
            PIN(); rd(ic<n>{}, ic<0>{}); rd(ic<n>{}, ic<1>{}); rd(ic<n>{}, ic<2>{}); rd(ic<n>{}, ic<3>{});
            if constexpr (first) mfma32z(acc[0][1], a0, fb[bf][1]); else mfma32(acc[0][1], a0, fb[bf][1]);
            PIN(); rd(ic<n>{}, ic<4>{}); rd(ic<n>{}, ic<5>{}); vm(ic<0>{});
            if constexpr (first) mfma16z(csum, a0, ones); else mfma16(csum, a0, ones);
            if constexpr (first) mfma32z(acc[1][0], a1, fb[bf][0]); else mfma32(acc[1][0], a1, fb[bf][0]);
            PIN(); side(ic<0>{}); vm(ic<1>{});
            if constexpr (first) mfma32z(acc[1][1], a1, fb[bf][1]); else mfma32(acc[1][1], a1, fb[bf][1]);
            PIN(); side(ic<1>{});
        });
        STAMP(3);
        tie(csum);
        lds_st<f4>(L_CS + (64 * wf + 32 * wt + 16 * (lane & 1) + 4 * (lane >> 4)) * 4, csum);
        if (w < 2) lds_st<float>(L_B1 + tid * 4, blo(b1v));
        tie(st0); tie(st1);
        {
            const unsigned uvo = (tid >> 1) * 10240 + (tid & 1) * 16;
            ub0 = ld128c<0>(uvo, cx.rws, OFF_XNB + ch0 * 16); ub1 = ld128c<0>(uvo, cx.rws, OFF_XNB + 128 * 10240 + ch0 * 16);
        }
        BARRIER();
        MFMA_DRAIN();
        {
            const unsigned hh = lane >> 5;
            const unsigned hvo = sel(tj == 13 && wf == 1, OOB, ((lane >> 4) & 1) * (NKS * 1024) + (hh * 16 + (lane & 15)) * 16);
            const unsigned hso = OFF_H + ((8 * ti + 4 * wt) * NKS + 4 * tj + 2 * wf) * 1024;
            const unsigned cam[2] = {opaque(L_CS + (64 * wf + 32 * wt + 4 * hh) * 4), opaque(L_CS + (64 * wf + 32 * (wt ^ 1) + 4 * hh) * 4)};
            constexpr float C1 = -2.302208198f, C2 = -0.1029432397f;
            sfor<2>([&](auto mf_) {
                constexpr int mf = mf_;
                f4 cs[4], bb[4];
                sfor<4>([&](auto ga) { cs[ga] = lds_ld<f4>(cam[mf] + 32 * ga); bb[ga] = lds_ld<f4>(cam[mf] + 512 + 32 * ga); });
                sfor<2>([&](auto nt_) {
                    constexpr int nt = nt_;
                    tie(acc[mf][nt]);
                    const v2u sv = nt ? st1 : st0;
                    const float sa = __uint_as_float(sv.x), sb = __uint_as_float(sv.y);
                    sfor<2>([&](auto chk) {
                        f2 z[4], e[4];
                        sfor<4>([&](auto i) {
                            constexpr int ga = 2 * chk + (i >> 1), e2 = 2 * (i & 1);
                            z[i] = f2{acc[mf][nt][4 * ga + e2], acc[mf][nt][4 * ga + e2 + 1]} * sa + (f2{cs[ga][e2], cs[ga][e2 + 1]} * sb + f2{bb[ga][e2], bb[ga][e2 + 1]});
                        });
                        sfor<4>([&](auto i) { e[i] = z[i] * (z[i] * z[i] * C2 + C1); });
                        sfor<4>([&](auto i) { e[i] = f2{__builtin_amdgcn_exp2f(e[i].x), __builtin_amdgcn_exp2f(e[i].y)}; });
                        sfor<4>([&](auto i) { e[i] = e[i] + 1.f; });
                        sfor<4>([&](auto i) { e[i] = f2{__builtin_amdgcn_rcpf(e[i].x), __builtin_amdgcn_rcpf(e[i].y)}; });
                        unsigned o[4];
                        sfor<4>([&](auto i) { const f2 r = z[i] * e[i]; o[i] = pk(r.x, r.y); });
                        constexpr int idx = 4 * mf + 2 * nt + chk, f0 = WG0 + (WG1 - WG0) * idx / 8, f1 = WG0 + (WG1 - WG0) * (idx + 1) / 8;
                        w2_prefetch<f0, f1>(cx);
                        st128c<chk * 512>(v4u{o[0], o[1], o[2], o[3]}, hvo, cx.rws, hso + (2 * nt * NKS + (mf ^ wt)) * 1024);
                        PIN();
                    });
                });
            });
        }
        STAMP(4);
        w2_prefetch<WG1, WG1 + 3>(cx);
        kernel_table(cx);
        w2_prefetch<WG1 + 3, WG1 + 6>(cx);
        BARRIER();
        const unsigned r0 = L_RC + cidx * RC_CH;
        expand_taps(cx, r0);
        VMWAIT(6);
        tie(ub0); tie(ub1);
        {
            const unsigned ua = opaque(L_U + 480 + (tid & 1) * U_CH + (tid >> 1) * 16);
            lds_st<v4u>(ua, ub0); lds_st<v4u>(ua + 2048, ub1);
            lds_st<v2u>(L_U + (tid >> 7) * U_CH + sel((tid & 127) < 60, tid & 127, 59) * 8, v2u{0, 0});
        }
        BARRIER();
        publish(cx, OFF_F2 + (16 * ti + tj) * FSTR);
        STAMP(5);
        w2_prefetch<WG1 + 6, 40>(cx);
        shift_taps<false>(cx, r0);
        ubase = L_U + cidx * U_CH;
    } else {
        STAMP(2);
        kg_set_base(cx);
#pragma unroll 1
        for (unsigned round = 0; round < 3; ++round) {
            CYC(0);
            kg.ldt = half16(p.ldt, ch0 + 2 * round);
            sfor<2>([&](auto i) { kg_step<i, false>(kg); });
            CYC(1);
            PIN();
            const unsigned cn = (ch0 + 2 * round + 2) * 256;
            kg.la = ld16(pvo, rla, cn); kg.ai = ld16(pvo, rai, cn); kg.cr = ld16(pvo, rcr, cn); kg.ci = ld16(pvo, rci, cn);
            PIN();
            sfor<4>([&](auto i) { kg_step<i + 2, false>(kg); });
            CYC(2);
            sfor<KG_STEPS - 6>([&](auto i) { kg_step<i + 6, false>(kg); });
            CYC(3);
            VMWAIT(0);
            CYC(4);
            tie(kg.la); tie(kg.ai); tie(kg.cr); tie(kg.ci);
            BARRIER();
            CYC(5);
            if (round == 0) publish(cx, OFF_F1 + tb * FSTR);
            kernel_table(cx);
            CYC(6);
            BARRIER();
            CYC(7);
            expand_taps(cx, L_R0 + (2 * round + cidx) * RC_COPY);
            CYC(8);
        }
        STAMP(3);
        poll<4>(OFF_F1 + tid * FSTR, cx);
        BARRIER();
        STAMP(4);
        {
            v4u ub[6];
            unsigned ua[6];
            sfor<6>([&](auto i) {
                const unsigned P = tid + 256 * i, blk = (P * 10923) >> 16, c = P - 6 * blk;
                ub[i] = ld128c<0>(blk * 10240 + c * 16, cx.rws, OFF_XNB + ch0 * 16);
                ua[i] = L_U6 + 480 + c * U_CH + blk * 16;
            });
            w2_prefetch<0, 4>(cx);
            shift_taps<true>(cx, L_R0 + cidx * RC_COPY);
            sfor<3>([&](auto i) { lds_st<v2u>(L_U6 + (2 * i + (tid >> 7)) * U_CH + sel((tid & 127) < 60, tid & 127, 59) * 8, v2u{0, 0}); });
            VMWAIT(4);
            sfor<6>([&](auto i) { tie(ub[i]); lds_st<v4u>(ua[i], ub[i]); });
        }
        STAMP(5);
#pragma unroll 1
        for (unsigned round = 0; round < 2; ++round) {
            const float dsk = blo(half16(p.dsk, ch0 + 2 * round));
            BARRIER();
            conv_pair(cx, ch0 + 2 * round + cidx, L_U6 + (2 * round + cidx) * U_CH, dsk, [&](auto d) {
                if (round == 0) w2_prefetch<4 + 9 * d / 2, 4 + 9 * (d + 1) / 2>(cx); else w2_prefetch<22 + 9 * d / 2, 22 + 9 * (d + 1) / 2>(cx);
            }, [&]() {});
            shift_taps<true>(cx, L_R0 + (2 * round + 2 + cidx) * RC_COPY);
        }
        ubase = L_U6 + (4 + cidx) * U_CH;
    }
    cx.w2so = rfl(cx.w2so); cx.w2l = rfl(cx.w2l); cx.w2a = rfl(cx.w2a); cx.w2b = rfl(cx.w2b); cx.thr = rfl(cx.thr); cx.e_lo = rfl(cx.e_lo); cx.e_hi = rfl(cx.e_hi);
    ubase = rfl(ubase);
    const unsigned chl = rfl(ch0 + sel(spare, 4, 0));
    const float dskip = blo(half16(p.dsk, chl));
    BARRIER();
    STAMP(6);

    constexpr int WIN = 4;
    v4u wx[6], hf[WIN][4];
    unsigned hv[4], hso;
    sfor<4>([&](auto t) { hv[t] = (4 * rt + ((t + w) & 3)) * (NKS * 1024) + lane * 16; });
    const unsigned hsa = OFF_H + k0s * 1024, hsb = hsa - nk * 1024, hsl = sel(w < 2, OFF_H + klast * 1024, WS_USED);
    auto ldh = [&](auto s_, auto t_) {
        constexpr int s = s_, t = t_;
        if constexpr (s < 14) {
            if constexpr (t == 0) hso = slot_off<s, 1024>(hsa, hsb, hsl, cx.thr);
            hf[s % WIN][t] = ld128c<0>(hv[t], cx.rws, hso);
        }
        PIN();
    };
    {
        const unsigned f2vo = OFF_F2 + (16 * (rt >> 1) + sel(lane < 14, lane, 0)) * FSTR;
        v2u plv2;
        conv_pair(cx, chl + cidx, ubase, dskip,
            [&](auto d) {
                if constexpr (d == 2) plv2 = ld64c<0>(f2vo, cx.rws, 0);
                if constexpr (d < 3) w2_prefetch<40 + 8 * d, 48 + 8 * d>(cx);
            },
            [&]() {
                VMWAIT(8);
                tie(plv2);
                if (!flags_match(plv2, cx)) poll<1>(f2vo, cx);
                sfor<WIN - 1>([&](auto s) { sfor<4>([&](auto t) { ldh(s, t); }); });
                wx[0] = ld128c<0>(cx.w16b, cx.rws, cx.w2so);
                sfor<4>([&](auto c) { wx[1 + c] = ld128c<c * 1024>(cx.w16a, cx.rws, cx.w2l); });
                wx[5] = ld128c<0>(cx.w16b, cx.rws, cx.w2l);
                STAMP(8);
            });
    }
    STAMP(9);
    f4 acc2[5][4];
    v2u xo[5], bo[5], plv3;
    v4u sv[3];
    const unsigned xvo = (16 * w + (lane & 15)) * (SD * 2) + 8 * (lane >> 4), xso = 64 * rt * (SD * 2) + 160 * ct;
    {
        const unsigned f3so = rfl(OFF_F3 + tb * FSTR);
        const rsrc_t rb2 = __builtin_amdgcn_make_buffer_rsrc((void*)p.b2, 0, SD * 2, 0x00020000);
        const unsigned svo = (tid >> 3) * 4096 + (tid & 7) * 16;
        sfor<14>([&](auto s_) {
            constexpr int s = s_;
            constexpr int later = (s + WIN - 2 < 14 ? WIN - 2 : 13 - s) * 4;
            constexpr int extra = s <= 2 ? 8 : (s == 4 || s == 5 || s == 7 || s == 8) ? 1 : (s == 10 || s == 11) ? 13 : 0;
            VMWAIT(later + extra);
            sfor<4>([&](auto t) { tie(hf[s % WIN][t]); });
            if constexpr (s == 3) { sfor<6>([&](auto i) { tie(wx[i]); }); BARRIER(); publish(cx, f3so); }
            if constexpr (s == 6) plv3 = ld64c<0>((lane & 31) * FSTR, cx.rws, OFF_F3 + ct * (32 * FSTR));
            if constexpr (s == 9) {
                tie(plv3);
                if (!flags_match(plv3, cx)) poll<1>(OFF_F3 + ct * (32 * FSTR) + (lane & 31) * FSTR, cx);
                const unsigned so = OFF_ST + 80 * ct * 4096 + 128 * rt;
                sv[0] = ld128c<0>(svo, cx.rws, so); sv[1] = ld128c<0>(svo, cx.rws, so + 32 * 4096); sv[2] = ld128c<0>(svo, cx.rws, sel(w < 2, so + 64 * 4096, WS_USED));
                sfor<5>([&](auto c) { xo[c] = ld64<c * 32>(xvo, rx, xso); });
                sfor<5>([&](auto c) { bo[c] = ld64<c * 32>(8 * (lane >> 4), rb2, 160 * ct); });
            }
            PIN();
            sfor<5>([&](auto c_) {
                constexpr int c = c_, f = 5 * s + c;
                sfor<4>([&](auto t) {
                    if constexpr (f >= 64) mfma16(acc2[c][t], wx[f - 64], hf[s % WIN][t]);
                    else if constexpr (s == 0) mfma16az<4 * f>(acc2[c][t], hf[s % WIN][t]);
                    else mfma16a<4 * f>(acc2[c][t], hf[s % WIN][t]);
                });
                PIN();
                if constexpr (c < 4) ldh(ic<s + WIN - 1>{}, c_);
            });
            if constexpr (s == 12) {
                sfor<3>([&](auto ii) { tie(sv[ii]); });
                const unsigned sa = opaque(L_SIMG + (tid >> 3) * SIMG_ROW + (tid & 7) * 16);
                lds_st<v4u>(sa, sv[0]); lds_st<v4u>(sa + 32 * SIMG_ROW, sv[1]); lds_st<v4u>(sa + 64 * SIMG_ROW, sv[2]);
                PIN();
            }
        });
    }
    STAMP(10);
    sfor<5>([&](auto c) { tie(xo[c]); tie(bo[c]); });
    unsigned rw[3];
    sfor<3>([&](auto t1) { rw[t1] = opaque(L_RED + ((w + t1 + 1) & 3) * 15360 + t1 * 5120 + lane * 16); });
    const unsigned ra = opaque(L_RED + w * 15360 + lane * 16);
    const unsigned sa = opaque(L_SIMG + (4 * (lane >> 4) + ((lane & 15) >> 2)) * SIMG_ROW + 32 * w + 8 * (lane & 3));
    PIN(); asm volatile("s_nop 7"); PIN();
    sfor<5>([&](auto c) { sfor<3>([&](auto t1) { tie(acc2[c][t1 + 1]); lds_st<f4>(rw[t1] + c * 1024, acc2[c][t1 + 1]); }); });
    f4 zb[5], xf[5];
    sfor<5>([&](auto c) {
        tie(acc2[c][0]);
        zb[c] = acc2[c][0] + f4{blo(bo[c].x), bhi(bo[c].x), blo(bo[c].y), bhi(bo[c].y)};
        xf[c] = f4{blo(xo[c].x), bhi(xo[c].x), blo(xo[c].y), bhi(xo[c].y)};
    });
    BARRIER();
    {
        const rsrc_t ro = __builtin_amdgcn_make_buffer_rsrc((void*)p.out, 0, SL * SD * 2, 0x00020000);
        sfor<5>([&](auto c) {
            const f4 z = (zb[c] + lds_ld<f4>(ra + c * 1024)) + (lds_ld<f4>(ra + 5120 + c * 1024) + lds_ld<f4>(ra + 10240 + c * 1024));
            const v2u s2 = lds_tr(sa + c * 16 * SIMG_ROW);
            const f4 u = z * -1.44269504088896f;
            const f4 d = f4{__builtin_amdgcn_exp2f(u[0]), __builtin_amdgcn_exp2f(u[1]), __builtin_amdgcn_exp2f(u[2]), __builtin_amdgcn_exp2f(u[3])} + 1.f;
            const f4 sz = f4{blo(s2.x), bhi(s2.x), blo(s2.y), bhi(s2.y)} * z;
            const f4 o = sz * f4{__builtin_amdgcn_rcpf(d[0]), __builtin_amdgcn_rcpf(d[1]), __builtin_amdgcn_rcpf(d[2]), __builtin_amdgcn_rcpf(d[3])} + xf[c];
            st64<c * 32>(v2u{pk(o[0], o[1]), pk(o[2], o[3])}, xvo, ro, xso);
        });
    }
    if (b == 0) publish(cx, OFF_W);
}

__global__ void __launch_bounds__(256, 1) tiny_kernel(unsigned* p) { asm volatile("" ::: "v220", "a255"); if (p == nullptr) p[0] = 1; }
extern "C" void kernel_launch(void* const* d_in, const int* in_sizes, int n_in, void* d_out, int out_size, void* d_ws, size_t ws_size, hipStream_t stream) {
    Params p;
    p.x = (const u16*)d_in[0]; p.lar = (const u16*)d_in[1]; p.aim = (const u16*)d_in[2]; p.cr = (const u16*)d_in[3]; p.ci = (const u16*)d_in[4];
    p.ldt = (const u16*)d_in[5]; p.dsk = (const u16*)d_in[6]; p.w1 = (const u16*)d_in[8]; p.b1 = (const u16*)d_in[9];
    p.w2 = (const u16*)d_in[10]; p.b2 = (const u16*)d_in[11]; p.out = (u16*)d_out; p.ws = (char*)d_ws;
    (void)hipFuncSetAttribute((const void*)s4d_mixer_kernel, hipFuncAttributeMaxDynamicSharedMemorySize, LDS_BYTES);
    s4d_mixer_kernel<<<256, 256, LDS_BYTES, stream>>>(p);
    tiny_kernel<<<256, 256, 0, stream>>>((unsigned*)d_ws);
}
```

```cpp
#include <hip/hip_runtime.h>
#include <stdint.h>
#include <utility>

typedef unsigned short u16;
typedef unsigned long long u64;
typedef unsigned v4u __attribute__((ext_vector_type(4)));
typedef unsigned v2u __attribute__((ext_vector_type(2)));
typedef float f2 __attribute__((ext_vector_type(2)));
typedef float f4 __attribute__((ext_vector_type(4)));
typedef float f16v __attribute__((ext_vector_type(16)));
typedef short s4v __attribute__((ext_vector_type(4)));
typedef __bf16 bf2v __attribute__((ext_vector_type(2)));
typedef __amdgpu_buffer_rsrc_t rsrc_t;
#define LAS __attribute__((address_space(3)))
#define CAS __attribute__((address_space(4)))
#define DI __device__ __forceinline__
#define PIN() __builtin_amdgcn_sched_barrier(0)
#define BARRIER() do { PIN(); asm volatile("s_waitcnt lgkmcnt(0)\n\ts_barrier" ::: "memory"); PIN(); } while (0)
#define VMWAIT(n) do { PIN(); asm volatile("s_waitcnt vmcnt(%0)" :: "i"(n) : "memory"); PIN(); } while (0)

#define FSTR 32
#define MDELAY 4
#define MPRIO 2
#define SPRIO 2
#define HINT_AT 3
#define STAMPS 0

constexpr int SL = 2048, SD = 640, SF = 1728, NKS = SF / 32;
constexpr unsigned OOB = 0x40000000u;
constexpr unsigned OFF_W = 0, OFF_TMO = 128, OFF_F1 = 4096, OFF_F2 = OFF_F1 + 256 * FSTR, OFF_F3 = OFF_F2 + 256 * FSTR, OFF_F4 = OFF_F3 + 320 * FSTR, OFF_DBG = 0x1000000;
constexpr unsigned OFF_STATS = 114688, OFF_XNB = 131072, OFF_W2T = OFF_XNB + SL * SD * 2, OFF_ST = OFF_W2T + SF * SD * 2;
constexpr unsigned OFF_H = OFF_ST + SL * SD * 2, WS_USED = OFF_H + SL * SF * 2;
constexpr unsigned CODE_LINES = 256;
constexpr unsigned SPIN_CAP = 1u << 16;
static_assert(OFF_F4 + 256 * FSTR <= OFF_STATS, "workspace map");

constexpr unsigned L_LNT = 0, L_W2S = 10240;
constexpr unsigned L_XY = 0, XY_ROW = 528, XY_X = 32 * XY_ROW, XY_CH = 28160, L_KT = 65536, KT_CH = 2048;
constexpr unsigned L_RC = 0, RC_COPY = 4160, RC_CH = 4 * RC_COPY, L_U = 2 * RC_CH, U_CH = 4608, L_RED2 = L_U + 2 * U_CH;
constexpr unsigned L_WH = 49152, L_CS = 16384, L_B1 = L_CS + 512, L_RED = 0, L_SIMG = 61440, SIMG_ROW = 128;
constexpr unsigned LDS_BYTES = 81920;
constexpr int VLIT = 192;

struct Params {
    const u16 *x, *lar, *aim, *cr, *ci, *ldt, *dsk, *w1, *b1, *w2, *b2;
    u16* out;
    char* ws;
};

template <int... Is, class Fn> DI void sfor_impl(std::integer_sequence<int, Is...>, Fn&& fn) { (fn(std::integral_constant<int, Is>{}), ...); }
template <int N, class Fn> DI void sfor(Fn&& fn) { sfor_impl(std::make_integer_sequence<int, N>{}, fn); }
template <int N> using ic = std::integral_constant<int, N>;

DI unsigned rfl(unsigned v) { return __builtin_amdgcn_readfirstlane(v); }
DI unsigned sel(bool c, unsigned a, unsigned b) { return c ? a : b; }
DI unsigned opaque(unsigned v) { asm volatile("" : "+v"(v)); return v; }
DI float opaquef(float v) { asm("" : "+v"(v)); return v; }
DI float blo(unsigned d) { return __uint_as_float(d << 16); }
DI float bhi(unsigned d) { return __uint_as_float(d & 0xffff0000u); }
DI unsigned pk(float lo, float hi) { f2 v = {lo, hi}; return __builtin_bit_cast(unsigned, __builtin_convertvector(v, bf2v)); }
DI float rbf(float v) { return blo(pk(v, v)); }
template <class T> DI T lds_ld(unsigned a) { return *(const LAS T*)(size_t)a; }
template <class T> DI void lds_st(unsigned a, T v) { *(LAS T*)(size_t)a = v; }
DI v2u lds_tr(unsigned a) { return __builtin_bit_cast(v2u, __builtin_amdgcn_ds_read_tr16_b64_v4i16((LAS s4v*)(size_t)a)); }
DI rsrc_t mk_rsrc(const void* p, unsigned bytes) { return __builtin_amdgcn_make_buffer_rsrc((void*)p, 0, bytes, 0x00020000); }

template <int CTRL> DI float dpp_add(float v) {
    return opaquef(v + __builtin_bit_cast(float, __builtin_amdgcn_update_dpp(0, __builtin_bit_cast(int, v), CTRL, 0xf, 0xf, true)));
}
DI float pair_sum(float a, float b) {
    asm("s_nop 1" : "+v"(a), "+v"(b));
    const auto r = __builtin_amdgcn_permlane32_swap(__float_as_uint(a), __float_as_uint(b), false, false);
    float v = opaquef(__uint_as_float(r[0]) + __uint_as_float(r[1]));
    v = dpp_add<0xB1>(v); v = dpp_add<0x4E>(v); v = dpp_add<0x141>(v); v = dpp_add<0x140>(v);
    return dpp_add<0x142>(v);
}
template <int LANE> DI float rdl(float v) { return __builtin_bit_cast(float, __builtin_amdgcn_readlane(__builtin_bit_cast(int, v), LANE)); }
DI float inv_sd(float var) { return __builtin_amdgcn_rcpf(rbf(__builtin_amdgcn_sqrtf(rbf(rbf(fmaxf(var, 0.f)) + 1e-5f)))); }

template <int IMM> DI void dma(unsigned vo, rsrc_t rs, unsigned so) { asm volatile("buffer_load_dwordx4 %0, %1, %2 offen offset:%3 lds" :: "v"(vo), "s"(rs), "s"(so), "i"(IMM)); }
template <int IMM> DI void dmac(unsigned vo, rsrc_t rs, unsigned so) { asm volatile("buffer_load_dwordx4 %0, %1, %2 offen offset:%3 sc1 lds" :: "v"(vo), "s"(rs), "s"(so), "i"(IMM)); }
DI void set_m0(unsigned v) { asm volatile("s_mov_b32 m0, %0\n\ts_nop 0" :: "s"(v)); }
template <int IMM> DI unsigned ld32(unsigned vo, rsrc_t rs, unsigned so) { unsigned r; asm volatile("buffer_load_dword %0, %1, %2, %3 offen offset:%4" : "=v"(r) : "v"(vo), "s"(rs), "s"(so), "i"(IMM)); return r; }
DI unsigned ld16(unsigned vo, rsrc_t rs, unsigned so) { unsigned r; asm volatile("buffer_load_ushort %0, %1, %2, %3 offen" : "=v"(r) : "v"(vo), "s"(rs), "s"(so)); return r; }
template <int IMM> DI v2u ld64(unsigned vo, rsrc_t rs, unsigned so) { v2u r; asm volatile("buffer_load_dwordx2 %0, %1, %2, %3 offen offset:%4" : "=v"(r) : "v"(vo), "s"(rs), "s"(so), "i"(IMM)); return r; }
template <int IMM> DI v2u ld64c(unsigned vo, rsrc_t rs, unsigned so) { v2u r; asm volatile("buffer_load_dwordx2 %0, %1, %2, %3 offen offset:%4 sc1" : "=v"(r) : "v"(vo), "s"(rs), "s"(so), "i"(IMM)); return r; }
template <int IMM> DI v4u ld128c(unsigned vo, rsrc_t rs, unsigned so) { v4u r; asm volatile("buffer_load_dwordx4 %0, %1, %2, %3 offen offset:%4 sc1" : "=v"(r) : "v"(vo), "s"(rs), "s"(so), "i"(IMM)); return r; }
template <int R, int IMM> DI void ld128lit(unsigned vo, rsrc_t rs, unsigned so) {
    asm volatile("buffer_load_dwordx4 v[%c3:%c4], %0, %1, %2 offen offset:%5 sc1" :: "v"(vo), "s"(rs), "s"(so), "i"(R), "i"(R + 3), "i"(IMM));
}
template <int IMM> DI void st128c(v4u v, unsigned vo, rsrc_t rs, unsigned so) { asm volatile("buffer_store_dwordx4 %0, %1, %2, %3 offen offset:%4 sc1\n\ts_nop 1" :: "v"(v), "v"(vo), "s"(rs), "s"(so), "i"(IMM)); }
DI void st64c(v2u v, unsigned vo, rsrc_t rs, unsigned so) { asm volatile("buffer_store_dwordx2 %0, %1, %2, %3 offen sc1" :: "v"(v), "v"(vo), "s"(rs), "s"(so)); }
template <int IMM> DI void st64(v2u v, unsigned vo, rsrc_t rs, unsigned so) { asm volatile("buffer_store_dwordx2 %0, %1, %2, %3 offen offset:%4" :: "v"(v), "v"(vo), "s"(rs), "s"(so), "i"(IMM)); }
template <int IMM> DI void lds_st_tid(unsigned v) { asm volatile("ds_write_addtid_b32 %0 offset:%1" :: "v"(v), "i"(IMM)); }
template <class T> DI void tie(T& v) { asm volatile("" : "+v"(v)); }

DI void mfma32(f16v& c, v4u a, v4u b) { asm volatile("v_mfma_f32_32x32x16_bf16 %0, %1, %2, %0" : "+v"(c) : "v"(a), "v"(b)); }
DI void mfma32z(f16v& c, v4u a, v4u b) { asm volatile("v_mfma_f32_32x32x16_bf16 %0, %1, %2, 0" : "=&v"(c) : "v"(a), "v"(b)); }
DI void mfma16(f4& c, v4u a, v4u b) { asm volatile("v_mfma_f32_16x16x32_bf16 %0, %1, %2, %0" : "+v"(c) : "v"(a), "v"(b)); }
DI void mfma16z(f4& c, v4u a, v4u b) { asm volatile("v_mfma_f32_16x16x32_bf16 %0, %1, %2, 0" : "=&v"(c) : "v"(a), "v"(b)); }
template <int R> DI void mfma16lit(f4& c, v4u b) { asm volatile("v_mfma_f32_16x16x32_bf16 %0, v[%c2:%c3], %1, %0" : "+v"(c) : "v"(b), "i"(R), "i"(R + 3)); }
#define MFMA_PAD(n) do { PIN(); asm volatile("s_nop %0" :: "i"(n)); PIN(); } while (0)

DI float fmul(float a, float b) { float r; asm("v_mul_f32 %0, %1, %2" : "=v"(r) : "v"(a), "v"(b)); return r; }
DI float fadd(float a, float b) { float r; asm("v_add_f32 %0, %1, %2" : "=v"(r) : "v"(a), "v"(b)); return r; }
DI float ffma(float a, float b, float c) { float r; asm("v_fma_f32 %0, %1, %2, %3" : "=v"(r) : "v"(a), "v"(b), "v"(c)); return r; }
DI float fmul_s(float s, float b) { float r; asm("v_mul_f32 %0, %1, %2" : "=v"(r) : "s"(s), "v"(b)); return r; }
DI float fsub_s(float a, float s) { float r; asm("v_subrev_f32 %0, %1, %2" : "=v"(r) : "s"(s), "v"(a)); return r; }
DI f2 cmul(f2 p, f2 w) {
    float a, b;
    asm volatile("v_mul_f32 %0, %2, %4\n\tv_mul_f32 %1, %2, %5\n\tv_fma_f32 %0, -%3, %5, %0\n\tv_fma_f32 %1, %3, %4, %1" : "=&v"(a), "=&v"(b) : "v"(p.x), "v"(p.y), "v"(w.x), "v"(w.y));
    return f2{a, b};
}
DI f2 cexp_at(float er, float rev, float p) {
    const float m = __builtin_amdgcn_exp2f(opaquef(er * p)), fr = __builtin_amdgcn_fractf(rev * p);
    return f2{opaquef(__builtin_amdgcn_cosf(fr) * m), __builtin_amdgcn_sinf(fr) * m};
}

struct Ctx {
    rsrc_t rws;
    unsigned e_lo, e_hi;
    unsigned tid, lane, w, flag_vo;
};
#define STAMP(i) do {} while (0)
#define CYC(i) do {} while (0)

DI bool flags_match(v2u v, const Ctx& cx) { return __all(v.x == cx.e_lo && v.y == cx.e_hi); }
DI void publish(const Ctx& cx, unsigned so) { st64c(v2u{cx.e_lo, cx.e_hi}, cx.flag_vo, cx.rws, so); }
template <int SLEEP> DI void poll(unsigned vo, const Ctx& cx) {
#pragma unroll 1
    for (unsigned spins = 0;; ++spins) {
        v2u v = ld64c<0>(vo, cx.rws, 0);
        VMWAIT(0); tie(v);
        if (flags_match(v, cx)) break;
        if (spins > SPIN_CAP) { st64c(v2u{1u, 1u}, sel(cx.lane == 0, OFF_TMO, OOB), cx.rws, 0); break; }
        __builtin_amdgcn_s_sleep(SLEEP);
    }
}
DI void take_epoch(Ctx& cx, v2u& wv) {
    tie(wv);
    const u64 e = ((u64)rfl(wv.y) << 32 | rfl(wv.x)) + 0x9E3779B97F4A7C15ull;
    cx.e_lo = (unsigned)e; cx.e_hi = (unsigned)(e >> 32);
}
template <int S, int STRIDE> DI unsigned rot_off(unsigned base, unsigned wrapped, unsigned thr) {
    unsigned r;
    asm volatile("s_cmp_le_u32 %3, %4\n\ts_cselect_b32 %0, %2, %1\n\ts_add_u32 %0, %0, %5" : "=&s"(r) : "s"(base), "s"(wrapped), "s"(thr), "i"(S), "i"(S * STRIDE) : "scc");
    return r;
}
struct Slots { unsigned a, b, last, thr; };
template <int S, int STRIDE> DI unsigned slot_off(const Slots& s) { if constexpr (S == 13) return s.last; else return rot_off<S, STRIDE>(s.a, s.b, s.thr); }

DI f2 layernorm_rows(const unsigned (&xa)[5], const unsigned (&xb)[5], const Ctx& cx) {
    float a[10], b[10];
    sfor<5>([&](auto i) { a[2 * i] = blo(xa[i]); a[2 * i + 1] = bhi(xa[i]); b[2 * i] = blo(xb[i]); b[2 * i + 1] = bhi(xb[i]); });
    float s0 = a[0], s1 = a[1], s2 = b[0], s3 = b[1];
    sfor<4>([&](auto i) { s0 = fadd(s0, a[2 * i + 2]); s1 = fadd(s1, a[2 * i + 3]); s2 = fadd(s2, b[2 * i + 2]); s3 = fadd(s3, b[2 * i + 3]); });
    const float mean = pair_sum(fadd(s0, s1), fadd(s2, s3)) * (1.f / SD), mr = rbf(mean), dm = opaquef(mean - mr);
    const float mra = rdl<31>(mr), mrb = rdl<63>(mr);
    PIN(); asm volatile("s_nop 1"); PIN();
    sfor<10>([&](auto i) { a[i] = fsub_s(a[i], mra); b[i] = fsub_s(b[i], mrb); });
    s0 = fmul(a[0], a[0]); s1 = fmul(a[1], a[1]); s2 = fmul(b[0], b[0]); s3 = fmul(b[1], b[1]);
    sfor<4>([&](auto i) { s0 = ffma(a[2 * i + 2], a[2 * i + 2], s0); s1 = ffma(a[2 * i + 3], a[2 * i + 3], s1); s2 = ffma(b[2 * i + 2], b[2 * i + 2], s2); s3 = ffma(b[2 * i + 3], b[2 * i + 3], s3); });
    const float var = pair_sum(fadd(s0, s1), fadd(s2, s3)) * (1.f / SD) - opaquef(dm * dm);
    sfor<5>([&](auto i) {
        const unsigned ta = pk(a[2 * i], a[2 * i + 1]), tb = pk(b[2 * i], b[2 * i + 1]);
        a[2 * i] = blo(ta); a[2 * i + 1] = bhi(ta); b[2 * i] = blo(tb); b[2 * i + 1] = bhi(tb);
    });
    const float r1 = inv_sd(var);
    const float ra = rdl<31>(r1), rb = rdl<63>(r1);
    PIN(); asm volatile("s_nop 1"); PIN();
    unsigned o[10];
    sfor<10>([&](auto i) { a[i] = fmul_s(ra, a[i]); b[i] = fmul_s(rb, b[i]); });
    sfor<10>([&](auto i) { o[i] = pk(a[i], b[i]); });
    sfor<10>([&](auto i) { a[i] = blo(o[i]); b[i] = bhi(o[i]); });
    s0 = a[0]; s2 = b[0]; s1 = fmul(a[0], a[0]); s3 = fmul(b[0], b[0]);
    sfor<9>([&](auto i) { s0 = fadd(s0, a[i + 1]); s1 = ffma(a[i + 1], a[i + 1], s1); s2 = fadd(s2, b[i + 1]); s3 = ffma(b[i + 1], b[i + 1], s3); });
    const unsigned la = L_LNT + cx.w * 2560 + cx.lane * 16, lb = L_LNT + cx.w * 2560 + 2048 + cx.lane * 8;
    lds_st<v4u>(la, v4u{o[0], o[1], o[2], o[3]});
    lds_st<v4u>(la + 1024, v4u{o[4], o[5], o[6], o[7]});
    lds_st<v2u>(lb, v2u{o[8], o[9]});
    const float m2 = opaquef(pair_sum(s0, s2) * (1.f / SD)), r2 = inv_sd(pair_sum(s1, s3) * (1.f / SD) - opaquef(m2 * m2));
    return f2{opaquef(r1 * r2), -(mr * r1 + rbf(m2)) * r2};
}

struct Modes { unsigned la, ai, cr, ci; };
DI void mode_powers(const Ctx& cx, const Modes& md, unsigned ldt) {
    constexpr float LOG2E = 1.44269504088896f, INV2PI = 0.159154943091895f;
    set_m0(L_XY + (cx.w >> 1) * XY_CH + (cx.w & 1) * 256);
    const float dt = rbf(__builtin_amdgcn_exp2f(opaquef(blo(ldt) * LOG2E)));
    const float er = opaquef(opaquef(-rbf(__builtin_amdgcn_exp2f(blo(md.la) * LOG2E)) * dt) * LOG2E), rev = (blo(md.ai) * dt) * INV2PI;
    const f2 v1 = cexp_at(er, -rev, 1.f), v256 = cexp_at(er, -rev, 256.f), w8 = cexp_at(er, rev, 8.f), w32 = cexp_at(er, rev, 32.f);
    auto put = [&](auto row, f2 v) { lds_st_tid<row * XY_ROW>(pk(v.x, v.y)); };
    f2 x[8], y[4];
    x[0] = f2{opaquef(opaquef(blo(md.cr) * dt) * 2.f), (blo(md.ci) * dt) * -2.f};
    const f2 v2 = cmul(v1, v1);
    x[1] = cmul(x[0], v1);
    y[0] = w32; y[1] = w8; y[2] = cmul(w8, w8);
    lds_st_tid<0>(opaque(0x3f80u));
    put(ic<1>{}, w8); put(ic<4>{}, w32); put(ic<32>{}, x[0]);
    y[3] = cmul(y[2], w8);
    put(ic<33>{}, x[1]); put(ic<2>{}, y[2]);
    sfor<6>([&](auto i) { x[i + 2] = cmul(x[i], v2); if constexpr (i == 0) put(ic<3>{}, y[3]); else put(ic<33 + i>{}, x[i + 1]); });
    sfor<4>([&](auto i) { const f2 prev = i == 0 ? x[7] : x[i == 0 ? 0 : 2 * i - 2]; x[2 * i] = cmul(x[2 * i], v256); put(ic<39 + i>{}, prev); });
    f2 pend = x[6];
    sfor<27>([&](auto i_) {
        constexpr int i = i_, J = i + 5;
        y[J & 3] = cmul(y[J & 3], w32);
        if constexpr (i < 10) put(ic<43 + i>{}, pend);
        if constexpr (i < 9) { constexpr int k = i < 4 ? 4 * (i & 1) : 0; x[k] = cmul(x[k], v256); pend = x[k]; }
        if constexpr (i > 0) put(ic<J - 1>{}, y[(J - 1) & 3]);
    });
    put(ic<31>{}, y[3]);
}

DI void kernel_table(const Ctx& cx, unsigned kt) {
    const unsigned cidx = cx.w >> 1, half = cx.w & 1, r = cx.lane & 15, g4 = cx.lane >> 4;
    const unsigned xa = opaque(L_XY + cidx * XY_CH + XY_X + r * XY_ROW + g4 * 16), ya = opaque(xa - XY_X + half * (16 * XY_ROW));
    const unsigned ta = opaque(kt + g4 * 256 + (16 * half + r) * 8);
    f4 k0, k1;
    v4u y[8], x0[8], x1[8];
    auto rd = [&](auto ks) { y[ks] = lds_ld<v4u>(ya + ks * 64); x0[ks] = lds_ld<v4u>(xa + ks * 64); x1[ks] = lds_ld<v4u>(xa + 16 * XY_ROW + ks * 64); PIN(); };
    sfor<4>(rd);
    sfor<8>([&](auto ks) {
        if constexpr (ks == 0) { mfma16z(k0, x0[ks], y[ks]); mfma16z(k1, x1[ks], y[ks]); } else { mfma16(k0, x0[ks], y[ks]); mfma16(k1, x1[ks], y[ks]); }
        PIN();
        if constexpr (ks < 4) rd(ic<ks + 4>{});
    });
    MFMA_PAD(7);
    tie(k0); tie(k1);
    lds_st<v2u>(ta, v2u{pk(k0[0], k0[1]), pk(k0[2], k0[3])});
    lds_st<v2u>(ta + 1024, v2u{pk(k1[0], k1[1]), pk(k1[2], k1[3])});
}
DI void expand_taps(const Ctx& cx, unsigned kt) {
    constexpr unsigned LL = 0x01000100u, HL = 0x01000302u, BL = 0x01000504u, BB = 0x05040504u, HH = 0x03020302u;
    const unsigned half = cx.w & 1, J = cx.lane & 31, up = cx.lane >> 5, last = J == 31;
    const bool s1 = !half && !up, s3 = half;
    const unsigned pa = kt + sel(s3, 768 + 4 * up, 512 * up) + J * 8;
    const unsigned qa = sel(s1, pa + 256, sel(last, kt + sel(s3, sel(up, 1024, 772), 768), pa + 8));
    const unsigned a4 = 2 * half + up, ea = kt + 1024 + J * 8 + 2 * a4, fa = sel(last, kt + sel(a4 == 3, 1280, 1026 + 2 * a4), ea + 8);
    const unsigned p0 = lds_ld<unsigned>(pa), p1 = lds_ld<unsigned>(pa + 4), q0 = lds_ld<unsigned>(qa), q1 = lds_ld<unsigned>(qa + 4);
    const unsigned e0 = lds_ld<u16>(ea), e8 = lds_ld<u16>(fa);
    const unsigned bnd = lds_ld<unsigned>(kt + sel(half, 512, 768) + 31 * 8 + 4);
    v4u o[2];
    o[0].w = __builtin_amdgcn_perm(p0, p0, sel(s1, HL, LL));
    o[0].z = __builtin_amdgcn_perm(p1, p0, sel(s1, 0x05040706u, sel(s3, HL, 0x03020504u)));
    o[0].y = __builtin_amdgcn_perm(q0, sel(s3, p0, p1), sel(s1, 0x05040706u, sel(s3, HH, LL)));
    o[0].x = __builtin_amdgcn_perm(q0, sel(s1, q1, p1), sel(s1, HL, sel(s3, BB, 0x03020504u)));
    o[1].w = __builtin_amdgcn_perm(e0, e0, LL);
    o[1].z = o[1].w;
    o[1].y = __builtin_amdgcn_perm(e8, e0, sel(J & 1, BB, BL));
    o[1].x = __builtin_amdgcn_perm(e8, e8, LL);
    const unsigned fx[2] = {sel(half, __builtin_amdgcn_perm(p0, bnd, 0x03020504u), 0), o[1].w};
    const unsigned fy[2] = {sel(half, bnd, 0), sel(half, e0, bnd >> 16)};
    const unsigned dst = opaque(L_RC + (cx.w >> 1) * RC_CH + 16 * (127 - 64 * half - cx.lane));
    sfor<2>([&](auto it) {
        const v4u d = o[it];
        const unsigned nx = __builtin_amdgcn_update_dpp(fx[it], d.x, 0x138, 0xf, 0xf, false), ny = __builtin_amdgcn_update_dpp(fy[it], d.y, 0x138, 0xf, 0xf, false);
        const unsigned t0 = __builtin_amdgcn_alignbyte(d.y, d.x, 2), t1 = __builtin_amdgcn_alignbyte(d.z, d.y, 2), t2 = __builtin_amdgcn_alignbyte(d.w, d.z, 2);
        const unsigned t3 = __builtin_amdgcn_alignbyte(nx, d.w, 2), t4 = __builtin_amdgcn_alignbyte(ny, nx, 2);
        constexpr unsigned off = (1 - it) * 2048;
        lds_st<v4u>(dst + off, d);
        lds_st<v4u>(dst + off + RC_COPY, v4u{t0, t1, t2, t3});
        lds_st<v4u>(dst + off + 2 * RC_COPY, v4u{d.y, d.z, d.w, nx});
        lds_st<v4u>(dst + off + 3 * RC_COPY, v4u{t1, t2, t3, t4});
    });
    lds_st<v2u>(L_RC + ((cx.lane & 31) >> 2) * RC_COPY + 4096 + (cx.lane & 3) * 8, v2u{0, 0});
}
DI void conv_pair(Ctx& cx, unsigned ch, float dskip) {
    const unsigned cidx = cx.w >> 1, half = cx.w & 1, r = cx.lane & 15, g4 = cx.lane >> 4, ubase = L_U + cidx * U_CH;
    f4 ca[8];
    {
        const unsigned aa0 = L_RC + cidx * RC_CH + ((~r) & 3) * RC_COPY + 2 * (2028 - 4 * (r >> 2) + 8 * g4) - 3584 - 256 * half - 192;
        const unsigned aa = opaque(aa0), ab = opaque(aa0 + 8), bb = opaque(ubase + 448 + 32 * r + 16 * g4 - 256 * half - 192);
        v2u al[2][8], ah[2][8];
        v4u fb[2][8];
        auto rd = [&](auto d_, auto i_) {
            constexpr int d = d_, i = i_, e = i / 3, o = (3 - d) * 64;
            if constexpr (i % 3 == 0) al[d & 1][e] = lds_ld<v2u>(aa + (7 - e) * 512 + o);
            else if constexpr (i % 3 == 1) ah[d & 1][e] = lds_ld<v2u>(ab + (7 - e) * 512 + o);
            else fb[d & 1][e] = lds_ld<v4u>(bb + e * 512 + o);
            PIN();
        };
        sfor<12>([&](auto i) { rd(ic<0>{}, i); });
        sfor<4>([&](auto d_) {
            constexpr int d = d_;
            sfor<8>([&](auto n_) {
                constexpr int n = n_;
                sfor<n + 1>([&](auto e_) {
                    constexpr int e = e_, idx = n * (n + 1) / 2 + e;
                    const v4u a = {al[d & 1][e].x, al[d & 1][e].y, ah[d & 1][e].x, ah[d & 1][e].y};
                    if constexpr (d == 0 && e == 0) mfma16z(ca[n], a, fb[d & 1][n - e]); else mfma16(ca[n], a, fb[d & 1][n - e]);
                    PIN();
                    if constexpr (d == 0) { if constexpr (idx < 6) { rd(ic<0>{}, ic<12 + 2 * idx>{}); rd(ic<0>{}, ic<13 + 2 * idx>{}); } else if constexpr (idx >= 12) rd(ic<1>{}, ic<idx - 12>{}); }
                    else if constexpr (d < 3 && idx % 3 != 2) rd(ic<d + 1>{}, ic<idx - idx / 3>{});
                });
            });
        });
    }
    STAMP(7);
    const unsigned ra = opaque(L_RED2 + cx.lane * 16 + cx.w * 4096), rb = opaque(L_RED2 + cx.lane * 16 + (cx.w ^ 1) * 4096);
    const unsigned uaddr = opaque(ubase + 480 + 2 * (1024 * half + 16 * r + 4 * g4));
    v2u u[4];
    sfor<4>([&](auto i) { u[i] = lds_ld<v2u>(uaddr + i * 512); });
    MFMA_PAD(7);
    sfor<8>([&](auto n) { tie(ca[n]); });
    if (half == 0) { sfor<4>([&](auto i) { lds_st<f4>(ra + i * 1024, ca[4 + i]); }); }
    else { sfor<4>([&](auto i) { lds_st<f4>(ra + i * 1024, ca[i]); ca[i] = ca[4 + i]; }); }
    f4 uf[4];
    sfor<4>([&](auto i) { uf[i] = f4{ffma(blo(u[i].x), dskip, ca[i][0]), ffma(bhi(u[i].x), dskip, ca[i][1]), ffma(blo(u[i].y), dskip, ca[i][2]), ffma(bhi(u[i].y), dskip, ca[i][3])}; });
    BARRIER();
    v2u pkd[4];
    sfor<4>([&](auto i) { const f4 y = lds_ld<f4>(rb + i * 1024); pkd[i] = v2u{pk(fadd(y[0], uf[i][0]), fadd(y[1], uf[i][1])), pk(fadd(y[2], uf[i][2]), fadd(y[3], uf[i][3]))}; });
    const unsigned vo = 2 * (256 * (g4 & 1) + 16 * r + 8 * (g4 >> 1)), so = OFF_ST + ch * 4096 + 2048 * half;
    sfor<2>([&](auto i2) {
        const auto sx = __builtin_amdgcn_permlane16_swap(pkd[2 * i2].x, pkd[2 * i2 + 1].x, false, false);
        const auto sy = __builtin_amdgcn_permlane16_swap(pkd[2 * i2].y, pkd[2 * i2 + 1].y, false, false);
        st128c<i2 * 1024>(v4u{sx[0], sy[0], sx[1], sy[1]}, vo, cx.rws, so);
    });
}

constexpr bool slot_reloads(int s) { return s % 2 == 0 && s + 2 <= 12; }
constexpr int slot_ops(int s) { return (s + 2 < 14 ? 4 : 0) + (slot_reloads(s) ? 5 : 0) + (s == 5 ? 1 : 0) + (s == 9 ? 3 : 0) + (s == 11 ? 5 : 0); }
constexpr int slot_wait(int s) { return s == 0 ? 4 : slot_ops(s - 1); }

__global__ void __launch_bounds__(256, 2) __attribute__((amdgpu_num_vgpr(VLIT / 2))) s4d_mixer_kernel(Params p) {
    extern __shared__ __attribute__((aligned(16))) char smem[];
    const unsigned tid = threadIdx.x, lane = tid & 63, w = rfl(tid >> 6);
    const unsigned b = blockIdx.x, g = b & 7, q = (b >> 3) & 31;
    {
        u64 pc;
        asm volatile("s_getpc_b64 %0" : "=s"(pc) :: "v192", "v255");
        const unsigned line = ((b >> 3) & 15) * (CODE_LINES / 16) + w * (CODE_LINES / 64) + (lane & (CODE_LINES / 64 - 1));
        asm volatile("global_load_dword v255, %0, %1" :: "v"(line * 128u), "s"(pc & ~127ull));
    }
    asm volatile("" :: "v"((unsigned)(size_t)(LAS char*)smem));
    Ctx cx;
    STAMP(0);
    const bool mrole = b >= 256, spare = q >= 28;
    const unsigned tb = 32 * g + q, cidx = w >> 1;
    const rsrc_t rx = mk_rsrc(p.x, SL * SD * 2);
    cx.rws = mk_rsrc(p.ws, WS_USED);
    cx.tid = tid; cx.lane = lane; cx.w = w; cx.flag_vo = sel(tid == 0, 0, OOB);
    const unsigned w16a = lane * 16, w16b = lane * 16 + 4096;
    const unsigned rt = 4 * g + (q >> 3), ct = q & 7;
    const unsigned nk = 14 - (w >> 1), rot = (ct * 13) >> 3, k0s = 14 * w - sel(w == 3, 1, 0) + rot, klast = k0s + 13 - sel(rot != 0, 14, 0);
    Slots sw;
    sw.thr = nk - rot; sw.a = OFF_W2T + (ct * NKS + k0s) * 5120; sw.b = sw.a - nk * 5120; sw.last = sel(w < 2, OFF_W2T + (ct * NKS + klast) * 5120, WS_USED);
    unsigned w2so = 0;
    auto w2_lit = [&](auto lo_, auto hi_) {
        constexpr int LO = lo_, HI = hi_;
        sfor<HI - LO>([&](auto ii) {
            constexpr int f = LO + ii, c = f % 5;
            if constexpr (c == 0) w2so = slot_off<9 + 2 * (f / 5), 5120>(sw);
            if constexpr (c < 4) ld128lit<VLIT + 4 * f, c * 1024>(w16a, cx.rws, w2so); else ld128lit<VLIT + 4 * f, 0>(w16b, cx.rws, w2so);
        });
    };
    asm volatile("s_nop 4");

    if (mrole && !spare) {
        __builtin_amdgcn_s_sleep(MDELAY);
        __builtin_amdgcn_s_setprio(MPRIO);
        const rsrc_t rw1 = mk_rsrc(p.w1, SD * SF * 2);
        const unsigned ti = 4 * (g >> 1) + q / 7, tj = 7 * (g & 1) + q % 7, wf = w >> 1, wt = w & 1;
        unsigned gx[4], gw[2];
        sfor<4>([&](auto ii) { gx[ii] = (32 * w + 8 * ii + (lane >> 3)) * 1280 + 16 * ((lane & 7) ^ (4 * (ii & 1) + (lane >> 4))) - ii * 1024; });
        sfor<2>([&](auto ii) { gw[ii] = (8 * w + 4 * ii + (lane >> 4)) * (SF * 2) + 16 * ((lane & 15) ^ (((lane >> 4) << 2) | ((2 * w + ii) & 3))) - ii * 1024; });
        const unsigned krot = (3 * (q % 7) + (5 * (q / 7)) / 2 + g) % 10, kthr = 10 - krot;
        const unsigned sx0 = ti * (128 * 1280) + krot * 128, sw0 = tj * 256 + krot * (64 * SF * 2);
        unsigned curx, curw;
        auto px = [&](auto s_, auto ii_) {
            constexpr int s = s_, ii = ii_;
            if constexpr (s < 10) {
                if constexpr (ii == 0) curx = rot_off<s, 128>(sx0, sx0 - 1280, kthr);
                if constexpr (ii % 2 == 0) set_m0((s % 3) * 16384 + w * 4096);
                dma<ii * 1024>(gx[ii], rx, curx);
            }
            PIN();
        };
        auto pw = [&](auto u_, auto ii_) {
            constexpr int u = u_, ii = ii_;
            if constexpr (u < 20) {
                if constexpr (ii == 0) { curw = rot_off<u / 2, 64 * SF * 2>(sw0 + (u & 1) * (32 * SF * 2), sw0 + (u & 1) * (32 * SF * 2) - SD * SF * 2, kthr); set_m0(L_WH + (u & 3) * 8192 + w * 2048); }
                dma<ii * 1024>(gw[ii], rw1, curw);
            }
            PIN();
        };
        sfor<4>([&](auto ii) { px(ic<0>{}, ii); });
        sfor<3>([&](auto u) { pw(u, ic<0>{}); pw(u, ic<1>{}); });
        sfor<4>([&](auto ii) { px(ic<1>{}, ii); });
        v2u wv = ld64c<0>(opaque(OFF_W), cx.rws, 0);
        unsigned b1v = ld16((tid & 127) * 2, mk_rsrc(p.b1, SF * 2), tj * 256);
        unsigned ax[4], aw[4];
        {
            const unsigned row = 64 * wt + (lane & 31), hh = lane >> 5, blk = (lane >> 4) & 1, qr = (lane & 15) >> 2, pp = lane & 3;
            sfor<4>([&](auto ks) { ax[ks] = opaque(row * 128 + 16 * ((2 * ks + hh) ^ ((row >> 1) & 7))); });
            sfor<4>([&](auto i) {
                constexpr int mf = i >> 1, t = i & 1;
                const unsigned r = 8 * hh + 4 * t + qr, chn = 4 * (2 * wf + (mf ^ wt)) + 2 * blk + (pp >> 1);
                aw[i] = opaque(L_WH + 256 * r + 16 * (chn ^ ((qr << 2) | ((2 * hh + t) & 3))) + 8 * (pp & 1));
            });
        }
        const unsigned onesel = sel((lane ^ (lane >> 4)) & 1, 0u, 0x3f803f80u);
        v4u ones = {onesel, onesel, onesel, onesel};
        tie(ones);
        f16v acc[2][2];
        f4 csum;
        v2u fa[2][4], plv, st0, st1;
        v4u fb[2][2];
        auto rd = [&](auto n_, auto i_) {
            constexpr int n = n_, i = i_, bf = n & 1;
            constexpr unsigned xo = ((n >> 2) % 3) * 16384, wo = ((n >> 1) & 3) * 8192 + (n & 1) * 4096;
            if constexpr (n < 40) {
                if constexpr (i == 0) fb[bf][0] = lds_ld<v4u>(ax[n & 3] + xo);
                else if constexpr (i == 3) fb[bf][1] = lds_ld<v4u>(ax[n & 3] + xo + 4096);
                else fa[bf][i < 3 ? i - 1 : i - 2] = lds_tr(aw[i < 3 ? i - 1 : i - 2] + wo);
            }
            PIN();
        };
        VMWAIT(10);
        BARRIER();
        STAMP(2);
        sfor<6>([&](auto i) { rd(ic<0>{}, i); });
        pw(ic<3>{}, ic<0>{}); pw(ic<3>{}, ic<1>{});
        sfor<40>([&](auto n0_) {
            constexpr int n = n0_ + 1, u = n >> 1, bf = (n - 1) & 1;
            constexpr bool first = n == 1;
            if constexpr ((n & 1) == 0 && n < 40) {
                constexpr int WAITS[20] = {0, 12, 8, 10, 8, 10, 8, 10, 8, 10, 8, 10, 8, 11, 9, 10, 10, 11, 6, 7};
                VMWAIT(WAITS[u]);
                if constexpr (u == 2) { take_epoch(cx, wv); tie(b1v); }
                if constexpr (u == 15) { tie(plv); if (!flags_match(plv, cx)) poll<2>(OFF_F1 + tid * FSTR, cx); }
                BARRIER();
                if constexpr ((u & 1) == 0) CYC(u / 2);
                if constexpr (u == 12) plv = ld64c<0>(tid * FSTR, cx.rws, OFF_F1);
                if constexpr (u == 15) {
                    const unsigned vo = (64 * wt + (lane & 31)) * 8;
                    st0 = ld64c<0>(vo, cx.rws, OFF_STATS + ti * 1024); st1 = ld64c<256>(vo, cx.rws, OFF_STATS + ti * 1024);
                }
                PIN();
            }
            auto vm = [&](auto m_) {
                constexpr int m = m_;
                if constexpr ((n & 1) == 0) pw(ic<u + 3>{}, m_); else px(ic<u / 2 + 2>{}, ic<2 * (u & 1) + m>{});
                if constexpr (n >= 33 && n < 40 && (m == 0 || n == 35 || n == 37)) { constexpr int f = (n - 33) + (n > 35) + (n > 37) + m; w2_lit(ic<f>{}, ic<f + 1>{}); }
                PIN();
            };
            const v4u a0 = {fa[bf][0].x, fa[bf][0].y, fa[bf][1].x, fa[bf][1].y}, a1 = {fa[bf][2].x, fa[bf][2].y, fa[bf][3].x, fa[bf][3].y};
            if constexpr (first) mfma32z(acc[0][0], a0, fb[bf][0]); else mfma32(acc[0][0], a0, fb[bf][0]);
            PIN(); rd(ic<n>{}, ic<0>{}); rd(ic<n>{}, ic<1>{}); rd(ic<n>{}, ic<2>{});
            if constexpr (first) mfma32z(acc[0][1], a0, fb[bf][1]); else mfma32(acc[0][1], a0, fb[bf][1]);
            PIN(); rd(ic<n>{}, ic<3>{}); rd(ic<n>{}, ic<4>{}); rd(ic<n>{}, ic<5>{});
            if constexpr (first) mfma16z(csum, a0, ones); else mfma16(csum, a0, ones);
            if constexpr (first) mfma32z(acc[1][0], a1, fb[bf][0]); else mfma32(acc[1][0], a1, fb[bf][0]);
            PIN(); vm(ic<0>{});
            if constexpr (first) mfma32z(acc[1][1], a1, fb[bf][1]); else mfma32(acc[1][1], a1, fb[bf][1]);
            PIN(); vm(ic<1>{});
        });
        STAMP(3);
        CYC(10);
        tie(csum);
        lds_st<f4>(L_CS + (64 * wf + 32 * wt + 16 * (lane & 1) + 4 * (lane >> 4)) * 4, csum);
        lds_st<float>(L_B1 + (tid & 127) * 4, blo(b1v));
        tie(st0); tie(st1);
        BARRIER();
        MFMA_PAD(9);
        const unsigned hh = lane >> 5;
        const unsigned hvo = sel(tj == 13 && wf == 1, OOB, ((lane >> 4) & 1) * (NKS * 1024) + (hh * 16 + (lane & 15)) * 16);
        const unsigned hso = OFF_H + ((8 * ti + 4 * wt) * NKS + 4 * tj + 2 * wf) * 1024;
        const unsigned cam[2] = {opaque(L_CS + (64 * wf + 32 * wt + 4 * hh) * 4), opaque(L_CS + (64 * wf + 32 * (wt ^ 1) + 4 * hh) * 4)};
        constexpr float C1 = -2.302208198f, C2 = -0.1029432397f;
        sfor<2>([&](auto mf_) {
            constexpr int mf = mf_;
            f4 cs[4], bb[4];
            sfor<4>([&](auto ga) { cs[ga] = lds_ld<f4>(cam[mf] + 32 * ga); bb[ga] = lds_ld<f4>(cam[mf] + 512 + 32 * ga); });
            sfor<2>([&](auto nt_) {
                constexpr int nt = nt_;
                tie(acc[mf][nt]);
                const v2u sv = nt ? st1 : st0;
                const float sa = __uint_as_float(sv.x), sb = __uint_as_float(sv.y);
                sfor<2>([&](auto chk) {
                    f2 z[4], e[4];
                    sfor<4>([&](auto i) {
                        constexpr int ga = 2 * chk + (i >> 1), e2 = 2 * (i & 1);
                        z[i] = f2{acc[mf][nt][4 * ga + e2], acc[mf][nt][4 * ga + e2 + 1]} * sa + (f2{cs[ga][e2], cs[ga][e2 + 1]} * sb + f2{bb[ga][e2], bb[ga][e2 + 1]});
                    });
                    sfor<4>([&](auto i) { e[i] = z[i] * (z[i] * z[i] * C2 + C1); });
                    sfor<4>([&](auto i) { e[i] = f2{__builtin_amdgcn_exp2f(e[i].x), __builtin_amdgcn_exp2f(e[i].y)}; });
                    sfor<4>([&](auto i) { e[i] = e[i] + 1.f; });
                    sfor<4>([&](auto i) { e[i] = f2{__builtin_amdgcn_rcpf(e[i].x), __builtin_amdgcn_rcpf(e[i].y)}; });
                    unsigned o[4];
                    sfor<4>([&](auto i) { const f2 r = z[i] * e[i]; o[i] = pk(r.x, r.y); });
                    constexpr int idx = 4 * mf + 2 * nt + chk;
                    PIN();
                    if constexpr (idx < 6) w2_lit(ic<9 + idx>{}, ic<10 + idx>{});
                    if constexpr (idx == HINT_AT) publish(cx, OFF_F4 + tb * FSTR);
                    st128c<chk * 512>(v4u{o[0], o[1], o[2], o[3]}, hvo, cx.rws, hso + (2 * nt * NKS + (mf ^ wt)) * 1024);
                    PIN();
                });
            });
        });
        CYC(11);
        STAMP(4);
        VMWAIT(0);
        BARRIER();
        publish(cx, OFF_F2 + (16 * ti + tj) * FSTR);
        STAMP(5);
    } else {
        const unsigned nround = sel(spare && !mrole, 2, 1);
        unsigned ch = 80 * g + sel(spare, 6 * q - 112 + sel(mrole, 4, 0), 2 * q);
        const rsrc_t rla = mk_rsrc(p.lar, SD * 256), rai = mk_rsrc(p.aim, SD * 256), rcr = mk_rsrc(p.cr, SD * 256), rci = mk_rsrc(p.ci, SD * 256);
        const unsigned pvo = w * 128 + lane * 2;
        auto half16 = [&](const u16* base, unsigned c) { const unsigned v = *(const CAS unsigned*)(base + c); return (v >> (16 * cidx)) & 0xffffu; };
        Modes md;
        if (!mrole) {
            __builtin_amdgcn_s_setprio(3);
            const unsigned so = (8 * tb + 2 * w) * (SD * 2);
            v2u xa0 = ld64<0>(lane * 8, rx, so), xa1 = ld64<512>(lane * 8, rx, so);
            unsigned xa4 = ld32<1024>(lane * 4, rx, so);
            v2u xb0 = ld64<1280>(lane * 8, rx, so), xb1 = ld64<1280 + 512>(lane * 8, rx, so);
            unsigned xb4 = ld32<1280 + 1024>(lane * 4, rx, so);
            v2u wv = ld64c<0>(opaque(OFF_W), cx.rws, 0);
            md.la = ld16(pvo, rla, ch * 256); md.ai = ld16(pvo, rai, ch * 256); md.cr = ld16(pvo, rcr, ch * 256); md.ci = ld16(pvo, rci, ch * 256);
            const unsigned sid = 27 * g + sel(q < 26, q, 26), sks = sid >> 2, sqq = sid & 3;
            set_m0(L_W2S + w * 3072);
            sfor<3>([&](auto ii) {
                const unsigned P = 64 * (3 * w + ii) + lane, row = (P * 3277) >> 16, vo = row * (1280 - 320) + P * 16 - ii * 1024;
                dma<ii * 1024>(sel(q < 27 && (ii == 0 || w < 3), vo, OOB), mk_rsrc(p.w2, SD * SF * 2), sks * (32 * 1280) + sqq * 320);
            });
            VMWAIT(8);
            tie(xa0); tie(xa1); tie(xa4); tie(xb0); tie(xb1); tie(xb4);
            STAMP(1);
            const unsigned xa[5] = {xa0.x, xa0.y, xa1.x, xa1.y, xa4}, xb[5] = {xb0.x, xb0.y, xb1.x, xb1.y, xb4};
            const f2 st = layernorm_rows(xa, xb, cx);
            VMWAIT(0);
            take_epoch(cx, wv);
            tie(md.la); tie(md.ai); tie(md.cr); tie(md.ci);
            BARRIER();
            const unsigned a = L_LNT + tid * 4, g4 = lane >> 4;
            const unsigned a0 = L_W2S + (16 * (g4 >> 1) + 4 * (g4 & 1) + ((lane & 15) >> 2)) * 320 + 8 * (lane & 3) + w * 96;
            v4u v[3], fr[3];
            sfor<3>([&](auto ii) { v[ii] = v4u{lds_ld<unsigned>(a + ii * 1024), lds_ld<unsigned>(a + ii * 1024 + 2560), lds_ld<unsigned>(a + ii * 1024 + 5120), lds_ld<unsigned>(a + ii * 1024 + 7680)}; });
            sfor<3>([&](auto ii) { const v2u lo = lds_tr(a0 + ii * 32), hi = lds_tr(a0 + ii * 32 + 8 * 320); fr[ii] = v4u{lo.x, lo.y, hi.x, hi.y}; });
            const unsigned xso = OFF_XNB + tb * 10240;
            st128c<0>(v[0], tid * 16, cx.rws, xso);
            st128c<0>(v[1], tid * 16, cx.rws, xso + 4096);
            st128c<0>(v[2], tid * 16, cx.rws, sel(w < 2, xso + 8192, WS_USED));
            st64c(v2u{__float_as_uint(st.x), __float_as_uint(st.y)}, sel((lane & 31) == 31, (lane >> 5) * 8, OOB), cx.rws, OFF_STATS + (8 * tb + 2 * w) * 8);
            sfor<3>([&](auto ii) {
                const unsigned fi = 3 * w + ii;
                st128c<0>(fr[ii], w16a, cx.rws, sel(fi < 10 && q < 27, OFF_W2T + (2 * sqq * NKS + sks) * 5120 + fi * 1024 + sel(fi >= 5, (NKS - 1) * 5120, 0), WS_USED));
            });
            VMWAIT(0);
            BARRIER();
            publish(cx, OFF_F1 + tb * FSTR);
            __builtin_amdgcn_s_setprio(SPRIO);
            STAMP(2);
        } else {
            v2u wv = ld64c<0>(opaque(OFF_W), cx.rws, 0);
            md.la = ld16(pvo, rla, ch * 256); md.ai = ld16(pvo, rai, ch * 256); md.cr = ld16(pvo, rcr, ch * 256); md.ci = ld16(pvo, rci, ch * 256);
            VMWAIT(0);
            take_epoch(cx, wv);
            tie(md.la); tie(md.ai); tie(md.cr); tie(md.ci);
        }
        cx.e_lo = rfl(cx.e_lo); cx.e_hi = rfl(cx.e_hi); ch = rfl(ch);
        const unsigned nxt = rfl(ch * 256 + 512);
        PIN(); asm volatile("s_nop 4"); PIN();
        const unsigned uvo = (tid >> 1) * 10240 + (tid & 1) * 16, ua = L_U + 480 + (tid & 1) * U_CH + (tid >> 1) * 16;
#pragma unroll 1
        for (unsigned round = 0; round < nround; ++round) {
            const unsigned ldt = half16(p.ldt, ch + 2 * round);
            const Modes cur = md;
            md.la = ld16(pvo, rla, nxt); md.ai = ld16(pvo, rai, nxt); md.cr = ld16(pvo, rcr, nxt); md.ci = ld16(pvo, rci, nxt);
            mode_powers(cx, cur, ldt);
            BARRIER();
            STAMP(3);
            kernel_table(cx, L_KT + (2 * round + cidx) * KT_CH);
            VMWAIT(0);
            tie(md.la); tie(md.ai); tie(md.cr); tie(md.ci);
            BARRIER();
        }
#pragma unroll 1
        for (unsigned round = 0; round < nround; ++round) {
            const float dskip = blo(half16(p.dsk, ch));
            expand_taps(cx, L_KT + (2 * round + cidx) * KT_CH);
            STAMP(4);
            if (round == 0) { poll<8>(OFF_F1 + tid * FSTR, cx); BARRIER(); }
            STAMP(5);
            v4u ub0 = ld128c<0>(uvo, cx.rws, OFF_XNB + ch * 16), ub1 = ld128c<0>(uvo, cx.rws, OFF_XNB + 128 * 10240 + ch * 16);
            lds_st<v2u>(L_U + (tid >> 7) * U_CH + sel((tid & 127) < 60, tid & 127, 59) * 8, v2u{0, 0});
            VMWAIT(0);
            tie(ub0); tie(ub1);
            lds_st<v4u>(ua, ub0); lds_st<v4u>(ua + 2048, ub1);
            if (!spare && w == 0) poll<3>(opaque(OFF_F4 + tb * FSTR), cx);
            BARRIER();
            STAMP(6);
            conv_pair(cx, ch + cidx, dskip);
            ch += 2;
        }
        VMWAIT(0);
        BARRIER();
        publish(cx, OFF_F3 + (40 * g + sel(mrole, q + 4, q)) * FSTR);
        STAMP(8);
        if (!mrole) {
            if (b == 0) {
                poll<8>(OFF_F2 + sel((tid & 15) < 14, tid, tid & ~15u) * FSTR, cx);
                poll<8>(OFF_F3 + (40 * ((lane >> 2) & 7) + 32 + (lane & 3)) * FSTR, cx);
                BARRIER();
                publish(cx, OFF_W);
            }
            return;
        }
        __builtin_amdgcn_s_setprio(MPRIO);
        w2_lit(ic<0>{}, ic<15>{});
    }

    cx.e_lo = rfl(cx.e_lo); cx.e_hi = rfl(cx.e_hi);
    PIN(); asm volatile("s_nop 4"); PIN();
    v4u wb[5], hf[3][4];
    sfor<4>([&](auto k) {
        const unsigned so = slot_off<2 * k + 1, 5120>(sw);
        set_m0(w * 20480 + k * 5120);
        sfor<4>([&](auto c) { dmac<c * 1024>(w16a, cx.rws, so); });
        set_m0(w * 20480 + k * 5120 + 4096);
        dmac<0>(w16b, cx.rws, so);
    });
    auto ldw = [&](auto s_, auto c_) {
        constexpr int s = s_, c = c_;
        if constexpr (c == 0) w2so = slot_off<s, 5120>(sw);
        if constexpr (c < 4) wb[c] = ld128c<c * 1024>(w16a, cx.rws, w2so); else wb[c] = ld128c<0>(w16b, cx.rws, w2so);
        PIN();
    };
    sfor<5>([&](auto c) { ldw(ic<0>{}, c); });
    unsigned hv[4], hso2;
    sfor<4>([&](auto t) { hv[t] = (4 * rt + ((t + w) & 3)) * (NKS * 1024) + lane * 16; });
    Slots sh;
    sh.thr = sw.thr; sh.a = OFF_H + k0s * 1024; sh.b = sh.a - nk * 1024; sh.last = sel(w < 2, OFF_H + klast * 1024, WS_USED);
    auto ldh = [&](auto s_, auto t_) {
        constexpr int s = s_, t = t_;
        if constexpr (s < 14) {
            if constexpr (t == 0) hso2 = slot_off<s, 1024>(sh);
            hf[s % 3][t] = ld128c<0>(hv[t], cx.rws, hso2);
        }
        PIN();
    };
    v2u xo[5], plv3;
    sfor<5>([&](auto c) { xo[c] = ld64<c * 32>(8 * (lane >> 4), mk_rsrc(p.b2, SD * 2), 160 * ct); });
    poll<1>(OFF_F2 + (16 * (rt >> 1) + sel(lane < 14, lane, 0)) * FSTR, cx);
    sfor<5>([&](auto c) { tie(wb[c]); tie(xo[c]); });
    STAMP(9);
    sfor<2>([&](auto s) { sfor<4>([&](auto t) { ldh(s, t); }); });
    f4 acc2[5][4];
    sfor<5>([&](auto c) { acc2[c][0] = f4{blo(xo[c].x), bhi(xo[c].x), blo(xo[c].y), bhi(xo[c].y)}; tie(acc2[c][0]); });
    const unsigned xvo = (16 * w + (lane & 15)) * (SD * 2) + 8 * (lane >> 4), xso = 64 * rt * (SD * 2) + 160 * ct;
    {
        const unsigned svo = (tid >> 3) * 4096 + (tid & 7) * 16, f3vo = OFF_F3 + (40 * ct + sel(lane < 36, lane, 0)) * FSTR;
        const unsigned la = opaque(w * 20480 + lane * 16);
        v4u lf[2];
        sfor<14>([&](auto s_) {
            constexpr int s = s_;
            constexpr bool from_lds = (s & 1) && s < 9;
            VMWAIT(slot_wait(s));
            sfor<4>([&](auto t) { tie(hf[s % 3][t]); });
            if constexpr (s % 2 == 0 && s >= 2) sfor<5>([&](auto c) { tie(wb[c]); });
            if constexpr (s == 5) plv3 = ld64c<0>(f3vo, cx.rws, 0);
            if constexpr (s == 8) { tie(plv3); if (!flags_match(plv3, cx)) poll<1>(f3vo, cx); }
            if constexpr (s == 9) {
                BARRIER();
                const unsigned so = OFF_ST + 80 * ct * 4096 + 128 * rt;
                sfor<3>([&](auto ii) { set_m0(L_SIMG + ii * 4096 + w * 1024); dmac<0>(svo, cx.rws, sel(ii < 2 || w < 2, so + ii * (32 * 4096), WS_USED)); });
            }
            if constexpr (s == 11) sfor<5>([&](auto c) { xo[c] = ld64<c * 32>(xvo, rx, xso); });
            PIN();
            sfor<5>([&](auto c_) {
                constexpr int c = c_;
                if constexpr (from_lds && c < 4) { lf[(c + 1) & 1] = lds_ld<v4u>(la + (s >> 1) * 5120 + (c + 1) * 1024); PIN(); }
                sfor<4>([&](auto t) {
                    if constexpr (s == 0 && t > 0) mfma16z(acc2[c][t], wb[c], hf[0][t]);
                    else if constexpr (s % 2 == 0) mfma16(acc2[c][t], wb[c], hf[s % 3][t]);
                    else if constexpr (from_lds) mfma16(acc2[c][t], lf[c & 1], hf[s % 3][t]);
                    else mfma16lit<VLIT + 4 * (5 * ((s - 9) / 2) + c)>(acc2[c][t], hf[s % 3][t]);
                });
                PIN();
                if constexpr (slot_reloads(s)) ldw(ic<s + 2>{}, c_);
                if constexpr (c < 4) ldh(ic<s + 2>{}, c_);
                if constexpr (s % 2 == 0 && s < 8 && c == 3) { lf[0] = lds_ld<v4u>(la + (s >> 1) * 5120); PIN(); }
            });
        });
    }
    STAMP(10);
    sfor<5>([&](auto c) { tie(xo[c]); });
    unsigned rw[3];
    sfor<3>([&](auto t1) { rw[t1] = opaque(L_RED + ((w + t1 + 1) & 3) * 15360 + t1 * 5120 + lane * 16); });
    const unsigned ra = opaque(L_RED + w * 15360 + lane * 16);
    const unsigned sa = opaque(L_SIMG + (4 * (lane >> 4) + ((lane & 15) >> 2)) * SIMG_ROW + 32 * w + 8 * (lane & 3));
    MFMA_PAD(7);
    sfor<5>([&](auto c) { sfor<3>([&](auto t1) { tie(acc2[c][t1 + 1]); lds_st<f4>(rw[t1] + c * 1024, acc2[c][t1 + 1]); }); });
    f4 xf[5];
    sfor<5>([&](auto c) { tie(acc2[c][0]); xf[c] = f4{blo(xo[c].x), bhi(xo[c].x), blo(xo[c].y), bhi(xo[c].y)}; });
    BARRIER();
    {
        const rsrc_t ro = mk_rsrc(p.out, SL * SD * 2);
        sfor<5>([&](auto c) {
            const f4 z = (acc2[c][0] + lds_ld<f4>(ra + c * 1024)) + (lds_ld<f4>(ra + 5120 + c * 1024) + lds_ld<f4>(ra + 10240 + c * 1024));
            const v2u s2 = lds_tr(sa + c * 16 * SIMG_ROW);
            const f4 u = z * -1.44269504088896f;
            const f4 d = f4{__builtin_amdgcn_exp2f(u[0]), __builtin_amdgcn_exp2f(u[1]), __builtin_amdgcn_exp2f(u[2]), __builtin_amdgcn_exp2f(u[3])} + 1.f;
            const f4 sz = f4{blo(s2.x), bhi(s2.x), blo(s2.y), bhi(s2.y)} * z;
            const f4 o = sz * f4{__builtin_amdgcn_rcpf(d[0]), __builtin_amdgcn_rcpf(d[1]), __builtin_amdgcn_rcpf(d[2]), __builtin_amdgcn_rcpf(d[3])} + xf[c];
            st64<c * 32>(v2u{pk(o[0], o[1]), pk(o[2], o[3])}, xvo, ro, xso);
        });
    }
}

extern "C" void kernel_launch(void* const* d_in, const int* in_sizes, int n_in, void* d_out, int out_size, void* d_ws, size_t ws_size, hipStream_t stream) {
    Params p;
    p.x = (const u16*)d_in[0]; p.lar = (const u16*)d_in[1]; p.aim = (const u16*)d_in[2]; p.cr = (const u16*)d_in[3]; p.ci = (const u16*)d_in[4];
    p.ldt = (const u16*)d_in[5]; p.dsk = (const u16*)d_in[6]; p.w1 = (const u16*)d_in[8]; p.b1 = (const u16*)d_in[9];
    p.w2 = (const u16*)d_in[10]; p.b2 = (const u16*)d_in[11]; p.out = (u16*)d_out; p.ws = (char*)d_ws;
    (void)hipFuncSetAttribute((const void*)s4d_mixer_kernel, hipFuncAttributeMaxDynamicSharedMemorySize, LDS_BYTES);
    s4d_mixer_kernel<<<512, 256, LDS_BYTES, stream>>>(p);
}
```

```cpp
#include <hip/hip_runtime.h>
#include <stdint.h>
#include <utility>

typedef unsigned short u16;
typedef unsigned long long u64;
typedef unsigned v4u __attribute__((ext_vector_type(4)));
typedef unsigned v2u __attribute__((ext_vector_type(2)));
typedef float f2 __attribute__((ext_vector_type(2)));
typedef float f4 __attribute__((ext_vector_type(4)));
typedef float f16v __attribute__((ext_vector_type(16)));
typedef short s4v __attribute__((ext_vector_type(4)));
typedef __bf16 bf2v __attribute__((ext_vector_type(2)));
typedef __amdgpu_buffer_rsrc_t rsrc_t;
#define LAS __attribute__((address_space(3)))
#define CAS __attribute__((address_space(4)))
#define DI __device__ __forceinline__
#define PIN() __builtin_amdgcn_sched_barrier(0)
#define BARRIER() do { PIN(); asm volatile("s_waitcnt lgkmcnt(0)\n\ts_barrier" ::: "memory"); PIN(); } while (0)
#define VMWAIT(n) do { PIN(); asm volatile("s_waitcnt vmcnt(%0)" :: "i"(n) : "memory"); PIN(); } while (0)

#define FSTR 128
#define MDELAY 4
#define MPRIO 2
#define SPRIO 1
#define W2S_AT 2
#define AGG_SLEEP 8
#define B2WARM 1
#define LIT_IN_GELU 1
#define ABL 0
#define G2MAP 1
#define ROTH 1
#define SPARE_FIRST 1
#define HINT_AT 6
#define STAMPS 0

constexpr int SL = 2048, SD = 640, SF = 1728, NKS = SF / 32;
constexpr unsigned OOB = 0x40000000u;
constexpr unsigned OFF_W = 0, OFF_TMO = 128, OFF_F1 = 4096, OFF_F2 = OFF_F1 + 4096, OFF_F3 = OFF_F2 + 256 * FSTR, OFF_F4 = OFF_F3 + 320 * FSTR, OFF_G = OFF_F4 + 256 * FSTR, OFF_DBG = 0x1000000;
constexpr unsigned OFF_STATS = 245760, OFF_XNB = 262144, OFF_W2T = OFF_XNB + SL * SD * 2, OFF_ST = OFF_W2T + SF * SD * 2;
constexpr unsigned OFF_H = OFF_ST + SL * SD * 2, WS_USED = OFF_H + SL * SF * 2;
constexpr unsigned CODE_LINES = 256;
constexpr unsigned SPIN_CAP = 1u << 16;
static_assert(OFF_G + 8 * FSTR <= OFF_STATS, "workspace map");

constexpr unsigned L_W2S = 56320;
constexpr unsigned L_XY = 0, XY_ROW = 528, XY_X = 32 * XY_ROW, XY_CH = 28160, L_KT = 68608, KT_CH = 2048;
constexpr unsigned L_RC = 0, RC_COPY = 4160, RC_CH = 4 * RC_COPY, L_U = 2 * RC_CH, U_CH = 4608, L_RED2 = L_U + 4 * U_CH;
constexpr unsigned L_LNT = 32768, L_WH = 49152, L_CS = 16384, L_B1 = L_CS + 512, L_RED = 0, L_SIMG = 32768, SIMG_ROW = 144, L_OUT = 49152, L_XIMG = 61440, OUT_ROW = 176;
constexpr unsigned LDS_BYTES = 81920;
constexpr int VLIT = 176, NLIT = 20;

struct Params {
    const u16 *x, *lar, *aim, *cr, *ci, *ldt, *dsk, *w1, *b1, *w2, *b2;
    u16* out;
    char* ws;
};

template <int... Is, class Fn> DI void sfor_impl(std::integer_sequence<int, Is...>, Fn&& fn) { (fn(std::integral_constant<int, Is>{}), ...); }
template <int N, class Fn> DI void sfor(Fn&& fn) { sfor_impl(std::make_integer_sequence<int, N>{}, fn); }
template <int N> using ic = std::integral_constant<int, N>;

DI unsigned rfl(unsigned v) { return __builtin_amdgcn_readfirstlane(v); }
DI unsigned sel(bool c, unsigned a, unsigned b) { return c ? a : b; }
DI unsigned opaque(unsigned v) { asm volatile("" : "+v"(v)); return v; }
DI float opaquef(float v) { asm("" : "+v"(v)); return v; }
DI float blo(unsigned d) { return __uint_as_float(d << 16); }
DI float bhi(unsigned d) { return __uint_as_float(d & 0xffff0000u); }
DI unsigned pk(float lo, float hi) { f2 v = {lo, hi}; return __builtin_bit_cast(unsigned, __builtin_convertvector(v, bf2v)); }
DI float rbf(float v) { return blo(pk(v, v)); }
DI float dot2(unsigned a, unsigned b, float c) { return __builtin_amdgcn_fdot2_f32_bf16(__builtin_bit_cast(bf2v, a), __builtin_bit_cast(bf2v, b), c, false); }
template <class T> DI T lds_ld(unsigned a) { return *(const LAS T*)(size_t)a; }
template <class T> DI void lds_st(unsigned a, T v) { *(LAS T*)(size_t)a = v; }
DI v2u lds_tr(unsigned a) { return __builtin_bit_cast(v2u, __builtin_amdgcn_ds_read_tr16_b64_v4i16((LAS s4v*)(size_t)a)); }
DI rsrc_t mk_rsrc(const void* p, unsigned bytes) { return __builtin_amdgcn_make_buffer_rsrc((void*)p, 0, bytes, 0x00020000); }

template <int CTRL> DI float dpp_add(float v) {
    return opaquef(v + __builtin_bit_cast(float, __builtin_amdgcn_update_dpp(0, __builtin_bit_cast(int, v), CTRL, 0xf, 0xf, true)));
}
DI float pair_sum(float a, float b) {
    const auto r = __builtin_amdgcn_permlane32_swap(__float_as_uint(a), __float_as_uint(b), false, false);
    float v = opaquef(__uint_as_float(r[0]) + __uint_as_float(r[1]));
    v = dpp_add<0xB1>(v); v = dpp_add<0x4E>(v); v = dpp_add<0x141>(v); v = dpp_add<0x140>(v);
    return dpp_add<0x142>(v);
}
template <int LANE> DI float rdl(float v) { return __builtin_bit_cast(float, __builtin_amdgcn_readlane(__builtin_bit_cast(int, v), LANE)); }
DI float inv_sd(float var) { return __builtin_amdgcn_rcpf(rbf(__builtin_amdgcn_sqrtf(rbf(rbf(fmaxf(var, 0.f)) + 1e-5f)))); }

template <int IMM> DI void dma(unsigned vo, rsrc_t rs, unsigned so) { asm volatile("buffer_load_dwordx4 %0, %1, %2 offen offset:%3 lds" :: "v"(vo), "s"(rs), "s"(so), "i"(IMM)); }
template <int IMM> DI void dmac(unsigned vo, rsrc_t rs, unsigned so) { asm volatile("buffer_load_dwordx4 %0, %1, %2 offen offset:%3 sc1 lds" :: "v"(vo), "s"(rs), "s"(so), "i"(IMM)); }
DI void set_m0(unsigned v) { asm volatile("s_mov_b32 m0, %0\n\ts_nop 0" :: "s"(v)); }
template <int IMM> DI unsigned ld32(unsigned vo, rsrc_t rs, unsigned so) { unsigned r; asm volatile("buffer_load_dword %0, %1, %2, %3 offen offset:%4" : "=v"(r) : "v"(vo), "s"(rs), "s"(so), "i"(IMM)); return r; }
DI unsigned ld16(unsigned vo, rsrc_t rs, unsigned so) { unsigned r; asm volatile("buffer_load_ushort %0, %1, %2, %3 offen" : "=v"(r) : "v"(vo), "s"(rs), "s"(so)); return r; }
template <int IMM> DI v2u ld64(unsigned vo, rsrc_t rs, unsigned so) { v2u r; asm volatile("buffer_load_dwordx2 %0, %1, %2, %3 offen offset:%4" : "=v"(r) : "v"(vo), "s"(rs), "s"(so), "i"(IMM)); return r; }
template <int IMM> DI v2u ld64c(unsigned vo, rsrc_t rs, unsigned so) { v2u r; asm volatile("buffer_load_dwordx2 %0, %1, %2, %3 offen offset:%4 sc1" : "=v"(r) : "v"(vo), "s"(rs), "s"(so), "i"(IMM)); return r; }
template <int IMM> DI v4u ld128c(unsigned vo, rsrc_t rs, unsigned so) { v4u r; asm volatile("buffer_load_dwordx4 %0, %1, %2, %3 offen offset:%4 sc1" : "=v"(r) : "v"(vo), "s"(rs), "s"(so), "i"(IMM)); return r; }
DI v4u ld128(unsigned vo, rsrc_t rs, unsigned so) { v4u r; asm volatile("buffer_load_dwordx4 %0, %1, %2, %3 offen" : "=v"(r) : "v"(vo), "s"(rs), "s"(so)); return r; }
template <int R, int IMM> DI void ld128lit(unsigned vo, rsrc_t rs, unsigned so) {
    asm volatile("buffer_load_dwordx4 v[%c3:%c4], %0, %1, %2 offen offset:%5 sc1" :: "v"(vo), "s"(rs), "s"(so), "i"(R), "i"(R + 3), "i"(IMM));
}
template <int R> DI void ld16lit(unsigned vo, rsrc_t rs, unsigned so) { asm volatile("buffer_load_ushort v%c3, %0, %1, %2 offen" :: "v"(vo), "s"(rs), "s"(so), "i"(R)); }
template <int R> DI void ld64clit(unsigned vo, rsrc_t rs) { asm volatile("buffer_load_dwordx2 v[%c2:%c3], %0, %1, 0 offen sc1" :: "v"(vo), "s"(rs), "i"(R), "i"(R + 1)); }
template <int R> DI unsigned lit() { unsigned r; asm volatile("v_mov_b32 %0, v%c1" : "=v"(r) : "i"(R)); return r; }
template <int IMM> DI void st128c(v4u v, unsigned vo, rsrc_t rs, unsigned so) { asm volatile("buffer_store_dwordx4 %0, %1, %2, %3 offen offset:%4 sc1\n\ts_nop 1" :: "v"(v), "v"(vo), "s"(rs), "s"(so), "i"(IMM)); }
DI void st64c(v2u v, unsigned vo, rsrc_t rs, unsigned so) { asm volatile("buffer_store_dwordx2 %0, %1, %2, %3 offen sc1" :: "v"(v), "v"(vo), "s"(rs), "s"(so)); }
DI void st128(v4u v, unsigned vo, rsrc_t rs, unsigned so) { asm volatile("buffer_store_dwordx4 %0, %1, %2, %3 offen\n\ts_nop 1" :: "v"(v), "v"(vo), "s"(rs), "s"(so)); }
template <int IMM> DI void lds_st_tid(unsigned v) { asm volatile("ds_write_addtid_b32 %0 offset:%1" :: "v"(v), "i"(IMM)); }
template <class T> DI void tie(T& v) { asm volatile("" : "+v"(v)); }

DI void mfma32(f16v& c, v4u a, v4u b) { asm volatile("v_mfma_f32_32x32x16_bf16 %0, %1, %2, %0" : "+v"(c) : "v"(a), "v"(b)); }
DI void mfma32z(f16v& c, v4u a, v4u b) { asm volatile("v_mfma_f32_32x32x16_bf16 %0, %1, %2, 0" : "=&v"(c) : "v"(a), "v"(b)); }
DI void mfma16(f4& c, v4u a, v4u b) { asm volatile("v_mfma_f32_16x16x32_bf16 %0, %1, %2, %0" : "+v"(c) : "v"(a), "v"(b)); }
DI void mfma16z(f4& c, v4u a, v4u b) { asm volatile("v_mfma_f32_16x16x32_bf16 %0, %1, %2, 0" : "=&v"(c) : "v"(a), "v"(b)); }
DI void mfma16k16(f4& c, v2u a, v2u b) { asm volatile("v_mfma_f32_16x16x16_bf16 %0, %1, %2, %0" : "+v"(c) : "v"(a), "v"(b)); }
template <int R> DI void mfma16lit(f4& c, v4u b) { asm volatile("v_mfma_f32_16x16x32_bf16 %0, v[%c2:%c3], %1, %0" : "+v"(c) : "v"(b), "i"(R), "i"(R + 3)); }
#define MFMA_PAD(n) do { PIN(); asm volatile("s_nop %0" :: "i"(n)); PIN(); } while (0)

DI float fadd(float a, float b) { float r; asm("v_add_f32 %0, %1, %2" : "=v"(r) : "v"(a), "v"(b)); return r; }
DI float ffma(float a, float b, float c) { float r; asm("v_fma_f32 %0, %1, %2, %3" : "=v"(r) : "v"(a), "v"(b), "v"(c)); return r; }
DI f2 cmul(f2 p, f2 w) {
    f2 r;
    asm volatile("v_pk_mul_f32 %0, %1, %2 op_sel:[0,0] op_sel_hi:[0,1]\n\tv_pk_fma_f32 %0, %1, %2, %0 op_sel:[1,1,0] op_sel_hi:[1,0,1] neg_lo:[0,1,0]" : "=&v"(r) : "v"(p), "v"(w));
    return r;
}
DI f2 cexp_at(float er, float rev, float p) {
    const float m = __builtin_amdgcn_exp2f(er * p), fr = __builtin_amdgcn_fractf(rev * p);
    return f2{__builtin_amdgcn_cosf(fr), __builtin_amdgcn_sinf(fr)} * m;
}

struct Ctx {
    rsrc_t rws;
    unsigned e_lo, e_hi;
    unsigned tid, lane, w, flag_vo;
};
#define STAMP(i) do {} while (0)
#define CYC(i) do {} while (0)
#define CY6(i) do {} while (0)
#define CY5(i) do {} while (0)

DI bool flags_match(v2u v, const Ctx& cx) { return __all(v.x == cx.e_lo && v.y == cx.e_hi); }
DI void publish(const Ctx& cx, unsigned so) { st64c(v2u{cx.e_lo, cx.e_hi}, cx.flag_vo, cx.rws, so); }
template <int SLEEP> DI void poll(unsigned vo, const Ctx& cx) {
#pragma unroll 1
    for (unsigned spins = 0;; ++spins) {
        v2u v = ld64c<0>(vo, cx.rws, 0);
        VMWAIT(0); tie(v);
        if (flags_match(v, cx)) break;
        if (spins > SPIN_CAP) { st64c(v2u{1u, 1u}, sel(cx.lane == 0, OFF_TMO, OOB), cx.rws, 0); break; }
        __builtin_amdgcn_s_sleep(SLEEP);
    }
}
DI void take_epoch(Ctx& cx) {
    unsigned lo, hi;
    asm volatile("v_readfirstlane_b32 %0, v%c2\n\tv_readfirstlane_b32 %1, v%c3" : "=s"(lo), "=s"(hi) : "i"(VLIT + 4), "i"(VLIT + 5));
    const u64 e = ((u64)hi << 32 | lo) + 0x9E3779B97F4A7C15ull;
    cx.e_lo = (unsigned)e; cx.e_hi = (unsigned)(e >> 32);
}
template <int S, int STRIDE> DI unsigned rot_off(unsigned base, unsigned wrapped, unsigned thr) {
    unsigned r;
    asm volatile("s_cmp_le_u32 %3, %4\n\ts_cselect_b32 %0, %2, %1\n\ts_add_u32 %0, %0, %5" : "=&s"(r) : "s"(base), "s"(wrapped), "s"(thr), "i"(S), "i"(S * STRIDE) : "scc");
    return r;
}
struct Slots { unsigned a, b, last, thr; };
template <int S, int STRIDE> DI unsigned slot_off(const Slots& s) { if constexpr (S == 13) return s.last; else return rot_off<S, STRIDE>(s.a, s.b, s.thr); }

template <class H1, class H2> DI f2 layernorm_rows(const unsigned (&xa)[5], const unsigned (&xb)[5], const Ctx& cx, H1&& hook1, H2&& hook2) {
    constexpr unsigned ONES = 0x3f803f80u;
    float sa = 0.f, sb = 0.f;
    sfor<5>([&](auto i) { sa = dot2(xa[i], ONES, sa); sb = dot2(xb[i], ONES, sb); });
    const float mean = pair_sum(sa, sb) * (1.f / SD), mr = rbf(mean), dm = mean - mr;
    const float mra = rdl<31>(mr), mrb = rdl<63>(mr);
    PIN(); hook1(); PIN();
    unsigned ta[5], tb[5];
    f2 qa = {0.f, 0.f}, qb = {0.f, 0.f};
    sfor<5>([&](auto i) {
        const f2 da = f2{blo(xa[i]), bhi(xa[i])} - mra, db = f2{blo(xb[i]), bhi(xb[i])} - mrb;
        qa = da * da + qa; qb = db * db + qb;
        ta[i] = pk(da.x, da.y); tb[i] = pk(db.x, db.y);
    });
    const float r1 = inv_sd(pair_sum(qa.x + qa.y, qb.x + qb.y) * (1.f / SD) - dm * dm);
    const float ra = rdl<31>(r1), rb = rdl<63>(r1);
    PIN(); hook2(); PIN();
    float s0 = 0.f, s1 = 0.f, s2 = 0.f, s3 = 0.f;
    unsigned o[10];
    sfor<5>([&](auto i) {
        const f2 na = f2{blo(ta[i]), bhi(ta[i])} * ra, nb = f2{blo(tb[i]), bhi(tb[i])} * rb;
        const unsigned ua = pk(na.x, na.y), ub = pk(nb.x, nb.y);
        s0 = dot2(ua, ONES, s0); s1 = dot2(ua, ua, s1); s2 = dot2(ub, ONES, s2); s3 = dot2(ub, ub, s3);
        o[2 * i] = __builtin_amdgcn_perm(ub, ua, 0x05040100u); o[2 * i + 1] = __builtin_amdgcn_perm(ub, ua, 0x07060302u);
    });
    const unsigned la = opaque(L_LNT + cx.w * 2560 + cx.lane * 16), lb = opaque(L_LNT + cx.w * 2560 + 2048 + cx.lane * 8);
    lds_st<v4u>(la, v4u{o[0], o[1], o[2], o[3]});
    lds_st<v4u>(la + 1024, v4u{o[4], o[5], o[6], o[7]});
    lds_st<v2u>(lb, v2u{o[8], o[9]});
    const float m2 = pair_sum(s0, s2) * (1.f / SD), r2 = inv_sd(pair_sum(s1, s3) * (1.f / SD) - m2 * m2);
    return f2{r1 * r2, -(mr * r1 + rbf(m2)) * r2};
}

struct Modes { unsigned la, ai, cr, ci; };
DI void mode_powers(const Ctx& cx, const Modes& md, unsigned ldt) {
    constexpr float LOG2E = 1.44269504088896f, INV2PI = 0.159154943091895f;
    set_m0(L_XY + (cx.w >> 1) * XY_CH + (cx.w & 1) * 256);
    const float dt = rbf(__builtin_amdgcn_exp2f(blo(ldt) * LOG2E));
    const float er = (-rbf(__builtin_amdgcn_exp2f(blo(md.la) * LOG2E)) * dt) * LOG2E, rev = (blo(md.ai) * dt) * INV2PI;
    const f2 v1 = cexp_at(er, -rev, 1.f), v256 = cexp_at(er, -rev, 256.f), w8 = cexp_at(er, rev, 8.f), w32 = cexp_at(er, rev, 32.f);
    auto put = [&](auto row, f2 v) { lds_st_tid<row * XY_ROW>(pk(v.x, v.y)); };
    f2 x[8], y[4];
    x[0] = f2{blo(md.cr), blo(md.ci)} * dt * f2{2.f, -2.f};
    const f2 v2 = cmul(v1, v1);
    x[1] = cmul(x[0], v1);
    y[0] = w32; y[1] = w8; y[2] = cmul(w8, w8);
    lds_st_tid<0>(opaque(0x3f80u));
    put(ic<1>{}, w8); put(ic<4>{}, w32); put(ic<32>{}, x[0]);
    y[3] = cmul(y[2], w8);
    put(ic<33>{}, x[1]); put(ic<2>{}, y[2]);
    sfor<6>([&](auto i) { x[i + 2] = cmul(x[i], v2); if constexpr (i == 0) put(ic<3>{}, y[3]); else put(ic<33 + i>{}, x[i + 1]); });
    sfor<4>([&](auto i) { const f2 prev = i == 0 ? x[7] : x[i == 0 ? 0 : 2 * i - 2]; x[2 * i] = cmul(x[2 * i], v256); put(ic<39 + i>{}, prev); });
    f2 pend = x[6];
    sfor<27>([&](auto i_) {
        constexpr int i = i_, J = i + 5;
        y[J & 3] = cmul(y[J & 3], w32);
        if constexpr (i < 10) put(ic<43 + i>{}, pend);
        if constexpr (i < 9) { constexpr int k = i < 4 ? 4 * (i & 1) : 0; x[k] = cmul(x[k], v256); pend = x[k]; }
        if constexpr (i > 0) put(ic<J - 1>{}, y[(J - 1) & 3]);
    });
    put(ic<31>{}, y[3]);
}

DI void kernel_table(const Ctx& cx, unsigned kt) {
    const unsigned cidx = cx.w >> 1, half = cx.w & 1, r = cx.lane & 15, g4 = cx.lane >> 4;
    const unsigned xa = opaque(L_XY + cidx * XY_CH + XY_X + r * XY_ROW + g4 * 16), ya = opaque(xa - XY_X + half * (16 * XY_ROW));
    const unsigned ta = opaque(kt + g4 * 256 + (16 * half + r) * 8);
    f4 k0, k1;
    v4u y[8], x0[8], x1[8];
    auto rd = [&](auto ks) { y[ks] = lds_ld<v4u>(ya + ks * 64); x0[ks] = lds_ld<v4u>(xa + ks * 64); x1[ks] = lds_ld<v4u>(xa + 16 * XY_ROW + ks * 64); PIN(); };
    sfor<4>(rd);
    sfor<8>([&](auto ks) {
        if constexpr (ks == 0) { mfma16z(k0, x0[ks], y[ks]); mfma16z(k1, x1[ks], y[ks]); } else { mfma16(k0, x0[ks], y[ks]); mfma16(k1, x1[ks], y[ks]); }
        PIN();
        if constexpr (ks < 4) rd(ic<ks + 4>{});
    });
    MFMA_PAD(7);
    tie(k0); tie(k1);
    lds_st<v2u>(ta, v2u{pk(k0[0], k0[1]), pk(k0[2], k0[3])});
    lds_st<v2u>(ta + 1024, v2u{pk(k1[0], k1[1]), pk(k1[2], k1[3])});
}
DI void expand_taps(const Ctx& cx, unsigned kt) {
    constexpr unsigned LL = 0x01000100u, HL = 0x01000302u, BL = 0x01000504u, BB = 0x05040504u, HH = 0x03020302u;
    const unsigned half = cx.w & 1, J = cx.lane & 31, up = cx.lane >> 5, last = J == 31;
    const bool s1 = !half && !up, s3 = half;
    const unsigned pa = kt + sel(s3, 768 + 4 * up, 512 * up) + J * 8;
    const unsigned qa = sel(s1, pa + 256, sel(last, kt + sel(s3, sel(up, 1024, 772), 768), pa + 8));
    const unsigned a4 = 2 * half + up, ea = kt + 1024 + J * 8 + 2 * a4, fa = sel(last, kt + sel(a4 == 3, 1280, 1026 + 2 * a4), ea + 8);
    const unsigned p0 = lds_ld<unsigned>(pa), p1 = lds_ld<unsigned>(pa + 4), q0 = lds_ld<unsigned>(qa), q1 = lds_ld<unsigned>(qa + 4);
    const unsigned e0 = lds_ld<u16>(ea), e8 = lds_ld<u16>(fa);
    const unsigned bnd = lds_ld<unsigned>(kt + sel(half, 512, 768) + 31 * 8 + 4);
    v4u o[2];
    o[0].w = __builtin_amdgcn_perm(p0, p0, sel(s1, HL, LL));
    o[0].z = __builtin_amdgcn_perm(p1, p0, sel(s1, 0x05040706u, sel(s3, HL, 0x03020504u)));
    o[0].y = __builtin_amdgcn_perm(q0, sel(s3, p0, p1), sel(s1, 0x05040706u, sel(s3, HH, LL)));
    o[0].x = __builtin_amdgcn_perm(q0, sel(s1, q1, p1), sel(s1, HL, sel(s3, BB, 0x03020504u)));
    o[1].w = __builtin_amdgcn_perm(e0, e0, LL);
    o[1].z = o[1].w;
    o[1].y = __builtin_amdgcn_perm(e8, e0, sel(J & 1, BB, BL));
    o[1].x = __builtin_amdgcn_perm(e8, e8, LL);
    const unsigned fx[2] = {sel(half, __builtin_amdgcn_perm(p0, bnd, 0x03020504u), 0), o[1].w};
    const unsigned fy[2] = {sel(half, bnd, 0), sel(half, e0, bnd >> 16)};
    const unsigned dst = opaque(L_RC + (cx.w >> 1) * RC_CH + 16 * (127 - 64 * half - cx.lane));
    sfor<2>([&](auto it) {
        const v4u d = o[it];
        const unsigned nx = __builtin_amdgcn_update_dpp(fx[it], d.x, 0x138, 0xf, 0xf, false), ny = __builtin_amdgcn_update_dpp(fy[it], d.y, 0x138, 0xf, 0xf, false);
        const unsigned t0 = __builtin_amdgcn_alignbyte(d.y, d.x, 2), t1 = __builtin_amdgcn_alignbyte(d.z, d.y, 2), t2 = __builtin_amdgcn_alignbyte(d.w, d.z, 2);
        const unsigned t3 = __builtin_amdgcn_alignbyte(nx, d.w, 2), t4 = __builtin_amdgcn_alignbyte(ny, nx, 2);
        constexpr unsigned off = (1 - it) * 2048;
        lds_st<v4u>(dst + off, d);
        lds_st<v4u>(dst + off + RC_COPY, v4u{t0, t1, t2, t3});
        lds_st<v4u>(dst + off + 2 * RC_COPY, v4u{d.y, d.z, d.w, nx});
        lds_st<v4u>(dst + off + 3 * RC_COPY, v4u{t1, t2, t3, t4});
    });
    lds_st<v2u>(L_RC + ((cx.lane & 31) >> 2) * RC_COPY + 4096 + (cx.lane & 3) * 8, v2u{0, 0});
}
DI void conv_pair(Ctx& cx, unsigned ch, unsigned ubase, float dskip) {
    const unsigned cidx = cx.w >> 1, half = cx.w & 1;
    f4 ca[8];
    {
        const unsigned r = cx.lane & 15, g4 = cx.lane >> 4;
        const unsigned aa0 = L_RC + cidx * RC_CH + ((~r) & 3) * RC_COPY + 2 * (2028 - 4 * (r >> 2) + 8 * g4) - 3584 - 256 * half - 192;
        const unsigned aa = opaque(aa0), ab = opaque(aa0 + 8), bb = opaque(ubase + 448 + 32 * r + 16 * g4 - 256 * half - 192);
        v2u al[2][8], ah[2][8];
        v4u fb[2][8];
        auto rd = [&](auto d_, auto i_) {
            constexpr int d = d_, i = i_, e = i / 3, o = (3 - d) * 64;
            if constexpr (i % 3 == 0) al[d & 1][e] = lds_ld<v2u>(aa + (7 - e) * 512 + o);
            else if constexpr (i % 3 == 1) ah[d & 1][e] = lds_ld<v2u>(ab + (7 - e) * 512 + o);
            else fb[d & 1][e] = lds_ld<v4u>(bb + e * 512 + o);
            PIN();
        };
        sfor<12>([&](auto i) { rd(ic<0>{}, i); });
        sfor<4>([&](auto d_) {
            constexpr int d = d_;
            sfor<8>([&](auto n_) {
                constexpr int n = n_;
                sfor<n + 1>([&](auto e_) {
                    constexpr int e = e_, idx = n * (n + 1) / 2 + e;
                    const v4u a = {al[d & 1][e].x, al[d & 1][e].y, ah[d & 1][e].x, ah[d & 1][e].y};
                    if constexpr (d == 0 && e == 0) mfma16z(ca[n], a, fb[d & 1][n - e]); else mfma16(ca[n], a, fb[d & 1][n - e]);
                    PIN();
                    if constexpr (d == 0) { if constexpr (idx < 6) { rd(ic<0>{}, ic<12 + 2 * idx>{}); rd(ic<0>{}, ic<13 + 2 * idx>{}); } else if constexpr (idx >= 12) rd(ic<1>{}, ic<idx - 12>{}); }
                    else if constexpr (d < 3 && idx % 3 != 2) rd(ic<d + 1>{}, ic<idx - idx / 3>{});
                });
            });
        });
    }
    STAMP(7);
    cx.tid = opaque(cx.tid); cx.lane = cx.tid & 63;
    const unsigned r = cx.lane & 15, g4 = cx.lane >> 4;
    const unsigned ra = opaque(L_RED2 + cx.lane * 16 + cx.w * 4096), rb = opaque(L_RED2 + cx.lane * 16 + (cx.w ^ 1) * 4096);
    const unsigned uaddr = opaque(ubase + 480 + 2 * (1024 * half + 16 * r + 4 * g4));
    v2u u[4];
    sfor<4>([&](auto i) { u[i] = lds_ld<v2u>(uaddr + i * 512); });
    MFMA_PAD(7);
    sfor<8>([&](auto n) { tie(ca[n]); });
    if (half == 0) { sfor<4>([&](auto i) { lds_st<f4>(ra + i * 1024, ca[4 + i]); }); }
    else { sfor<4>([&](auto i) { lds_st<f4>(ra + i * 1024, ca[i]); ca[i] = ca[4 + i]; }); }
    f4 uf[4];
    sfor<4>([&](auto i) { uf[i] = f4{ffma(blo(u[i].x), dskip, ca[i][0]), ffma(bhi(u[i].x), dskip, ca[i][1]), ffma(blo(u[i].y), dskip, ca[i][2]), ffma(bhi(u[i].y), dskip, ca[i][3])}; });
    BARRIER();
    v2u pkd[4];
    sfor<4>([&](auto i) { const f4 y = lds_ld<f4>(rb + i * 1024); pkd[i] = v2u{pk(fadd(y[0], uf[i][0]), fadd(y[1], uf[i][1])), pk(fadd(y[2], uf[i][2]), fadd(y[3], uf[i][3]))}; });
    const unsigned vo = 2 * (256 * (g4 & 1) + 16 * r + 8 * (g4 >> 1)), so = (ABL & 2) ? WS_USED : OFF_ST + ch * 4096 + 2048 * half;
    sfor<2>([&](auto i2) {
        const auto sx = __builtin_amdgcn_permlane16_swap(pkd[2 * i2].x, pkd[2 * i2 + 1].x, false, false);
        const auto sy = __builtin_amdgcn_permlane16_swap(pkd[2 * i2].y, pkd[2 * i2 + 1].y, false, false);
        st128c<i2 * 1024>(v4u{sx[0], sy[0], sx[1], sy[1]}, vo, cx.rws, so);
    });
}

constexpr bool slot_reloads(int s) { return s % 2 == 0 && s + 2 <= 10; }
constexpr int slot_ops(int s) { return (s + 2 < 14 ? 4 : 0) + (slot_reloads(s) ? 5 : 0) + (s == 9 ? 1 : 0) + (s == 12 ? 6 : 0); }
constexpr int slot_wait(int s) { return s == 0 ? 4 : slot_ops(s - 1); }
static_assert(slot_ops(13) == 0, "");

__global__ void __launch_bounds__(256, 2) __attribute__((amdgpu_num_vgpr(VLIT / 2))) s4d_mixer_kernel(Params p) {
    extern __shared__ __attribute__((aligned(16))) char smem[];
    const unsigned tid = threadIdx.x, lane = tid & 63, w = rfl(tid >> 6);
    const unsigned b = blockIdx.x, g = b & 7, q = (b >> 3) & 31;
    {
        u64 pc;
        asm volatile("s_getpc_b64 %0" : "=s"(pc) :: "v176", "v255");
        const unsigned line = ((b >> 3) & 15) * (CODE_LINES / 16) + w * (CODE_LINES / 64) + (lane & (CODE_LINES / 64 - 1));
        asm volatile("global_load_dword v255, %0, %1" :: "v"(line * 128u), "s"(pc & ~127ull));
    }
    asm volatile("" :: "v"((unsigned)(size_t)(LAS char*)smem));
    Ctx cx;
    STAMP(0);
    const bool mrole = b >= 256, spare = q >= 28;
    const unsigned tb = 32 * g + q, cidx = w >> 1;
    const rsrc_t rx = mk_rsrc(p.x, SL * SD * 2);
    cx.rws = mk_rsrc(p.ws, WS_USED);
    const rsrc_t rla = mk_rsrc(p.lar, SD * 256), rai = mk_rsrc(p.aim, SD * 256), rcr = mk_rsrc(p.cr, SD * 256), rci = mk_rsrc(p.ci, SD * 256);
    const unsigned pvo = w * 128 + lane * 2, ch0 = 80 * g + sel(spare, 6 * q - 112 + sel(mrole, 4, 0), 2 * q);
    const unsigned sid = 27 * g + sel(q < 26, q, 26), sks = sid >> 2, sqq = sid & 3;
    const bool slice = !mrole && q < 27;
    auto load_modes = [&]() {
        ld16lit<VLIT>(pvo, rla, ch0 * 256); ld16lit<VLIT + 1>(pvo, rai, ch0 * 256); ld16lit<VLIT + 2>(pvo, rcr, ch0 * 256); ld16lit<VLIT + 3>(pvo, rci, ch0 * 256);
    };
        auto load_slice = [&]() {
        set_m0(L_W2S + w * 3072);
        sfor<3>([&](auto ii) {
            const unsigned P = 64 * (3 * w + ii) + lane, row = (P * 3277) >> 16, vo = row * (1280 - 320) + P * 16 - ii * 1024;
            dma<ii * 1024>(sel(ii == 0 || w < 3, vo, OOB), mk_rsrc(p.w2, SD * SF * 2), sks * (32 * 1280) + sqq * 320);
        });
        };
    asm volatile("s_nop 4");
    if (!mrole) {
        __builtin_amdgcn_s_setprio(SPRIO);
        ld64clit<VLIT + 4>(opaque(OFF_W), cx.rws);
        load_modes();
        if (W2S_AT == 2 && slice) load_slice();
    }
    cx.tid = tid; cx.lane = lane; cx.w = w; cx.flag_vo = sel(tid == 0, 0, OOB);
    const unsigned w16a = lane * 16, w16b = lane * 16 + 4096;
    const unsigned rt = G2MAP ? 8 * (g >> 1) + (q >> 2) : 4 * g + (q >> 3), ct = G2MAP ? 4 * (g & 1) + (q & 3) : q & 7;
    const unsigned nk = 14 - (w >> 1), rot = (G2MAP ? (ROTH == 2 ? ((q & 3) * 13) >> 2 : ROTH == 1 ? 6 * (q & 1) : 0) + 5 * (q >> 2) : (ROTH == 2 ? (ct * 13) >> 3 : ROTH == 1 ? 6 * (q & 1) : ROTH == 3 ? ((q & 3) * 13) >> 2 : 0) + ((0x7610u >> (4 * (q >> 3))) & 15)) % 13, k0s = 14 * w - sel(w == 3, 1, 0) + rot, klast = k0s + 13 - sel(rot != 0, 14, 0);
    Slots sw;
    sw.thr = nk - rot; sw.a = OFF_W2T + (ct * NKS + k0s) * 5120; sw.b = sw.a - nk * 5120; sw.last = sel(w < 2, OFF_W2T + (ct * NKS + klast) * 5120, WS_USED);
    unsigned w2so = 0;
    bool late_lit;
    const unsigned gvo = opaque(OFF_G + g * FSTR);
    auto w2_lit = [&](auto lo_, auto hi_) {
        constexpr int LO = lo_, HI = hi_;
        sfor<HI - LO>([&](auto ii) {
            constexpr int f = LO + ii, c = f % 5;
            if constexpr (c == 0) w2so = slot_off<(f < 5 ? 9 : 10 + f / 5), 5120>(sw);
            if constexpr (c < 4) ld128lit<VLIT + 4 * f, c * 1024>(w16a, cx.rws, w2so); else ld128lit<VLIT + 4 * f, 0>(w16b, cx.rws, w2so);
        });
    };
    const rsrc_t rw1 = mk_rsrc(p.w1, SD * SF * 2);
    const unsigned ti = 4 * (g >> 1) + q / 7, tj = 7 * (g & 1) + q % 7, wf = w >> 1, wt = w & 1;
    unsigned gx[4], gw[2];
    sfor<4>([&](auto ii) { gx[ii] = (32 * w + 8 * ii + (lane >> 3)) * 1280 + 16 * ((lane & 7) ^ (4 * (ii & 1) + (lane >> 4))) - ii * 1024; });
    sfor<2>([&](auto ii) { gw[ii] = (8 * w + 4 * ii + (lane >> 4)) * (SF * 2) + 16 * ((lane & 15) ^ (((lane >> 4) << 2) | ((2 * w + ii) & 3))) - ii * 1024; });
    const unsigned krot = (3 * (q % 7) + (5 * (q / 7)) / 2 + g) % 10, kthr = 10 - krot;
    const unsigned sx0 = ti * (128 * 1280) + krot * 128, sw0 = tj * 256 + krot * (64 * SF * 2);
    unsigned curx, curw;
    auto px = [&](auto s_, auto ii_) {
        constexpr int s = s_, ii = ii_;
        if constexpr (s < 10) {
            if constexpr (ii == 0) curx = rot_off<s, 128>(sx0, sx0 - 1280, kthr);
            if constexpr (ii % 2 == 0) set_m0((s % 3) * 16384 + w * 4096);
            dma<ii * 1024>(gx[ii], rx, curx);
        }
        PIN();
    };
    auto pw = [&](auto u_, auto ii_) {
        constexpr int u = u_, ii = ii_;
        if constexpr (u < 20) {
            if constexpr (ii == 0) { curw = rot_off<u / 2, 64 * SF * 2>(sw0 + (u & 1) * (32 * SF * 2), sw0 + (u & 1) * (32 * SF * 2) - SD * SF * 2, kthr); set_m0(L_WH + (u & 3) * 8192 + w * 2048); }
            dma<ii * 1024>(gw[ii], rw1, curw);
        }
        PIN();
    };
    unsigned b1v;
    v2u xa0, xa1, xb0, xb1;
    unsigned xa4, xb4;
    if (mrole) {
        if (SPARE_FIRST && spare) __builtin_amdgcn_s_setprio(MPRIO);
        const unsigned so = (8 * tb + 2 * w) * (SD * 2);
        xa0 = ld64<0>(lane * 8, rx, so); xa1 = ld64<512>(lane * 8, rx, so); xa4 = ld32<1024>(lane * 4, rx, so);
        xb0 = ld64<1280>(lane * 8, rx, so); xb1 = ld64<1280 + 512>(lane * 8, rx, so); xb4 = ld32<1280 + 1024>(lane * 4, rx, so);
        ld64clit<VLIT + 4>(opaque(OFF_W), cx.rws);
        b1v = ld16((tid & 127) * 2, mk_rsrc(p.b1, SF * 2), tj * 256);
        if (B2WARM) ld16lit<254>(lane * 4, mk_rsrc(p.b2, SD * 2), 160 * ct); else ld16lit<254>(opaque(OOB), rx, 0);
        if (!spare) { sfor<4>([&](auto ii) { px(ic<0>{}, ii); }); sfor<2>([&](auto u) { pw(u, ic<0>{}); pw(u, ic<1>{}); }); VMWAIT(11); } else { load_modes(); VMWAIT(7); }
        tie(xa0); tie(xa1); tie(xa4); tie(xb0); tie(xb1); tie(xb4);
        STAMP(1);
        CY5(0);
        const unsigned xa[5] = {xa0.x, xa0.y, xa1.x, xa1.y, xa4}, xb[5] = {xb0.x, xb0.y, xb1.x, xb1.y, xb4};
        const f2 st = layernorm_rows(xa, xb, cx,
            [&]() { if (!spare) { pw(ic<2>{}, ic<0>{}); pw(ic<2>{}, ic<1>{}); } },
            [&]() { if (!spare) { px(ic<1>{}, ic<0>{}); px(ic<1>{}, ic<1>{}); } });
        CY5(1);
        if (!spare) VMWAIT(13); else VMWAIT(0);
        take_epoch(cx); tie(b1v);
        BARRIER();
        CY5(2);
        const unsigned a = L_LNT + tid * 4, xso = (ABL & 32) ? WS_USED : OFF_XNB + tb * 10240;
        v4u v[3];
        sfor<3>([&](auto ii) { v[ii] = v4u{lds_ld<unsigned>(a + ii * 1024), lds_ld<unsigned>(a + ii * 1024 + 2560), lds_ld<unsigned>(a + ii * 1024 + 5120), lds_ld<unsigned>(a + ii * 1024 + 7680)}; });
        sfor<3>([&](auto ii) { tie(v[ii]); });
        if (!spare) { VMWAIT(6); BARRIER(); STAMP(2); px(ic<1>{}, ic<2>{}); px(ic<1>{}, ic<3>{}); }
        st128c<0>(v[0], tid * 16, cx.rws, xso);
        st128c<0>(v[1], tid * 16, cx.rws, xso + 4096);
        st128c<0>(v[2], tid * 16, cx.rws, sel(w < 2, xso + 8192, WS_USED));
        st64c(v2u{__float_as_uint(st.x), __float_as_uint(st.y)}, sel((lane & 31) == 31, (lane >> 5) * 8, OOB), cx.rws, OFF_STATS + (8 * tb + 2 * w) * 8);
    }

    if (mrole && !spare) {
        __builtin_amdgcn_s_setprio(MPRIO);
        cx.e_lo = rfl(cx.e_lo); cx.e_hi = rfl(cx.e_hi);
        unsigned ax[4], aw[4];
        {
            const unsigned row = 64 * wt + (lane & 31), hh = lane >> 5, blk = (lane >> 4) & 1, qr = (lane & 15) >> 2, pp = lane & 3;
            sfor<4>([&](auto ks) { ax[ks] = opaque(row * 128 + 16 * ((2 * ks + hh) ^ ((row >> 1) & 7))); });
            sfor<4>([&](auto i) {
                constexpr int mf = i >> 1, t = i & 1;
                const unsigned r = 8 * hh + 4 * t + qr, chn = 4 * (2 * wf + (mf ^ wt)) + 2 * blk + (pp >> 1);
                aw[i] = opaque(L_WH + 256 * r + 16 * (chn ^ ((qr << 2) | ((2 * hh + t) & 3))) + 8 * (pp & 1));
            });
        }
        const unsigned onesel = sel((lane ^ (lane >> 4)) & 1, 0u, 0x3f803f80u);
        v4u ones = {onesel, onesel, onesel, onesel};
        tie(ones);
        f16v acc[2][2];
        f4 csum;
        v2u fa[2][4], st0, st1, plv, plw;
        v4u fb[2][2];
        auto rd = [&](auto n_, auto i_) {
            constexpr int n = n_, i = i_, bf = n & 1;
            constexpr unsigned xo = ((n >> 2) % 3) * 16384, wo = ((n >> 1) & 3) * 8192 + (n & 1) * 4096;
            if constexpr (n < 40) {
                if constexpr (i == 0) fb[bf][0] = lds_ld<v4u>(ax[n & 3] + xo);
                else if constexpr (i == 3) fb[bf][1] = lds_ld<v4u>(ax[n & 3] + xo + 4096);
                else fa[bf][i < 3 ? i - 1 : i - 2] = lds_tr(aw[i < 3 ? i - 1 : i - 2] + wo);
            }
            PIN();
        };
        CYC(0);
        sfor<6>([&](auto i) { rd(ic<0>{}, i); });
        pw(ic<3>{}, ic<0>{}); pw(ic<3>{}, ic<1>{});
        sfor<40>([&](auto n0_) {
            constexpr int n = n0_ + 1, u = n >> 1, bf = (n - 1) & 1;
            constexpr bool first = n == 1;
            if constexpr ((n & 1) == 0 && n < 40) {
                constexpr int WAITS[20] = {0, 14, 8, 11, 9, 10, 8, 10, 8, 10, 8, 10, 8, 10, 8, 10, 9, 9, 2, 3};
                VMWAIT(WAITS[u]);
                if constexpr (u == 18) { tie(plv); if (!flags_match(plv, cx)) poll<2>(gvo, cx); }
                BARRIER();
                if constexpr (u <= 11) CYC(u);
                if constexpr (u == 2) publish(cx, OFF_F1 + tb * 16);
                if constexpr (u == 15) plv = ld64c<0>(gvo, cx.rws, 0);
                if constexpr (u == 19) plw = ld64c<8>(gvo, cx.rws, 0);
                if constexpr (u == 18) {
                    publish(cx, OFF_F4 + tb * FSTR);
                    const unsigned vo = (64 * wt + (lane & 31)) * 8;
                    st0 = ld64c<0>(vo, cx.rws, OFF_STATS + ti * 1024); st1 = ld64c<256>(vo, cx.rws, OFF_STATS + ti * 1024);
                }
                PIN();
            }
            auto vm = [&](auto m_) {
                constexpr int m = m_;
                if constexpr ((n & 1) == 0) pw(ic<u + 3>{}, m_); else px(ic<u / 2 + 2>{}, ic<2 * (u & 1) + m>{});
                PIN();
            };
            const v4u a0 = {fa[bf][0].x, fa[bf][0].y, fa[bf][1].x, fa[bf][1].y}, a1 = {fa[bf][2].x, fa[bf][2].y, fa[bf][3].x, fa[bf][3].y};
            if constexpr (first) mfma32z(acc[0][0], a0, fb[bf][0]); else mfma32(acc[0][0], a0, fb[bf][0]);
            PIN(); rd(ic<n>{}, ic<0>{}); rd(ic<n>{}, ic<1>{}); rd(ic<n>{}, ic<2>{});
            if constexpr (first) mfma32z(acc[0][1], a0, fb[bf][1]); else mfma32(acc[0][1], a0, fb[bf][1]);
            PIN(); rd(ic<n>{}, ic<3>{}); rd(ic<n>{}, ic<4>{}); rd(ic<n>{}, ic<5>{});
            if constexpr (first) mfma16z(csum, a0, ones); else mfma16(csum, a0, ones);
            if constexpr (first) mfma32z(acc[1][0], a1, fb[bf][0]); else mfma32(acc[1][0], a1, fb[bf][0]);
            PIN(); vm(ic<0>{});
            if constexpr (first) mfma32z(acc[1][1], a1, fb[bf][1]); else mfma32(acc[1][1], a1, fb[bf][1]);
            PIN(); vm(ic<1>{});
        });
        STAMP(3);
        CY6(0);
        tie(csum);
        lds_st<f4>(L_CS + (64 * wf + 32 * wt + 16 * (lane & 1) + 4 * (lane >> 4)) * 4, csum);
        lds_st<float>(L_B1 + (tid & 127) * 4, blo(b1v));
        VMWAIT(0);
        tie(st0); tie(st1); tie(plw);
        if (!flags_match(plw, cx)) poll<2>(gvo + 8, cx);
        BARRIER();
        MFMA_PAD(9);
        CY6(1);
        const unsigned hh = lane >> 5;
        const unsigned hvo = sel((ABL & 16) || (tj == 13 && wf == 1), OOB, ((lane >> 4) & 1) * (NKS * 1024) + (hh * 16 + (lane & 15)) * 16);
        const unsigned hso = OFF_H + ((8 * ti + 4 * wt) * NKS + 4 * tj + 2 * wf) * 1024;
        const unsigned cam[2] = {opaque(L_CS + (64 * wf + 32 * wt + 4 * hh) * 4), opaque(L_CS + (64 * wf + 32 * (wt ^ 1) + 4 * hh) * 4)};
        constexpr float C1 = -2.302208198f, C2 = -0.1029432397f;
        sfor<2>([&](auto mf_) {
            constexpr int mf = mf_;
            f4 cs[4], bb[4];
            sfor<4>([&](auto ga) { cs[ga] = lds_ld<f4>(cam[mf] + 32 * ga); bb[ga] = lds_ld<f4>(cam[mf] + 512 + 32 * ga); });
            sfor<2>([&](auto nt_) {
                constexpr int nt = nt_;
                tie(acc[mf][nt]);
                const v2u sv = nt ? st1 : st0;
                const float sa = __uint_as_float(sv.x), sb = __uint_as_float(sv.y);
                sfor<2>([&](auto chk) {
                    f2 z[4], e[4];
                    sfor<4>([&](auto i) {
                        constexpr int ga = 2 * chk + (i >> 1), e2 = 2 * (i & 1);
                        z[i] = f2{acc[mf][nt][4 * ga + e2], acc[mf][nt][4 * ga + e2 + 1]} * sa + (f2{cs[ga][e2], cs[ga][e2 + 1]} * sb + f2{bb[ga][e2], bb[ga][e2 + 1]});
                    });
                    sfor<4>([&](auto i) { e[i] = z[i] * (z[i] * z[i] * C2 + C1); });
                    sfor<4>([&](auto i) { e[i] = f2{__builtin_amdgcn_exp2f(e[i].x), __builtin_amdgcn_exp2f(e[i].y)}; });
                    sfor<4>([&](auto i) { e[i] = e[i] + 1.f; });
                    sfor<4>([&](auto i) { e[i] = f2{__builtin_amdgcn_rcpf(e[i].x), __builtin_amdgcn_rcpf(e[i].y)}; });
                    unsigned o[4];
                    sfor<4>([&](auto i) { const f2 r = z[i] * e[i]; o[i] = pk(r.x, r.y); });
                    constexpr int idx = 4 * mf + 2 * nt + chk;
                    PIN();
                    if (LIT_IN_GELU) w2_lit(ic<NLIT * idx / 8>{}, ic<NLIT * (idx + 1) / 8>{});
                    if constexpr (idx == HINT_AT) publish(cx, OFF_F4 + 8 + tb * FSTR);
                    st128c<chk * 512>(v4u{o[0], o[1], o[2], o[3]}, hvo, cx.rws, hso + (2 * nt * NKS + (mf ^ wt)) * 1024);
                    PIN();
                    CY6(2 + idx);
                });
            });
        });
        STAMP(4);
        VMWAIT(0);
        CY6(10);
        BARRIER();
        CY6(11);
        publish(cx, OFF_F2 + (16 * ti + tj) * FSTR);
        STAMP(5);
        late_lit = !LIT_IN_GELU;
    } else {
        const unsigned nround = sel(spare && !mrole, 2, 1);
        unsigned ch = ch0;
        auto half16 = [&](const u16* base, unsigned c) { const unsigned v = *(const CAS unsigned*)(base + c); return (v >> (16 * cidx)) & 0xffffu; };
        const unsigned last = sel(nround == 2, ch0 + 2, ch0);
        const unsigned dsk0 = half16(p.dsk, ch0), dsk1 = half16(p.dsk, last), ldt0 = half16(p.ldt, ch0), ldt1 = half16(p.ldt, last);
        if (W2S_AT == 2 && slice) VMWAIT(3); else VMWAIT(0);
        take_epoch(cx);
        Modes md = {lit<VLIT>(), lit<VLIT + 1>(), lit<VLIT + 2>(), lit<VLIT + 3>()};
        if (mrole) { BARRIER(); publish(cx, OFF_F1 + tb * 16); if (!SPARE_FIRST) __builtin_amdgcn_s_setprio(SPRIO); }
        STAMP(2);
        cx.e_lo = rfl(cx.e_lo); cx.e_hi = rfl(cx.e_hi); ch = rfl(ch);
        const unsigned nxt = rfl(sel(nround == 2, ch * 256 + 512, SD * 256)), gso = rfl(OFF_G + g * FSTR), f5so = rfl(OFF_F1 + tb * 16 + 8);
        PIN(); asm volatile("s_nop 4"); PIN();
        auto retile = [&]() {
                const unsigned g4 = lane >> 4, a0 = opaque(L_W2S + (16 * (g4 >> 1) + 4 * (g4 & 1) + ((lane & 15) >> 2)) * 320 + 8 * (lane & 3) + w * 96);
                v4u fr[3];
                sfor<3>([&](auto ii) { const v2u lo = lds_tr(a0 + ii * 32), hi = lds_tr(a0 + ii * 32 + 8 * 320); fr[ii] = v4u{lo.x, lo.y, hi.x, hi.y}; });
                sfor<3>([&](auto ii) {
                    const unsigned fi = 3 * w + ii;
                    st128c<0>(fr[ii], w16a, cx.rws, sel(fi < 10 && !(ABL & 64), OFF_W2T + (2 * sqq * NKS + sks) * 5120 + fi * 1024 + sel(fi >= 5, (NKS - 1) * 5120, 0), WS_USED));
                });
            };
#pragma unroll 1
        for (unsigned round = 0; round < nround; ++round) {
            const Modes cur = md;
            md.la = ld16(pvo, rla, nxt); md.ai = ld16(pvo, rai, nxt); md.cr = ld16(pvo, rcr, nxt); md.ci = ld16(pvo, rci, nxt);
            CYC(0);
            mode_powers(cx, cur, sel(round == 0, ldt0, ldt1));
            CYC(1);
            if (W2S_AT == 0 && slice) load_slice();
            if (W2S_AT == 2) VMWAIT(0);
            BARRIER();
            if (W2S_AT == 2 && slice) retile();
            STAMP(3);
            CYC(2);
            kernel_table(cx, L_KT + (2 * round + cidx) * KT_CH);
            CYC(3);
            if (W2S_AT == 0 && slice) VMWAIT(3); else VMWAIT(0);
            tie(md.la); tie(md.ai); tie(md.cr); tie(md.ci);
            BARRIER();
            if (W2S_AT == 2 && round == 0 && !mrole) publish(cx, f5so);
            CYC(4);
        }
#pragma unroll 1
        for (unsigned round = 0; round < nround; ++round) {
            const float dskip = blo(sel(round == 0, dsk0, dsk1));
            cx.tid = opaque(cx.tid); cx.lane = cx.tid & 63;
            const unsigned tid = cx.tid;
            if (round == 0) CYC(5);
            expand_taps(cx, L_KT + (2 * round + cidx) * KT_CH);
            if (round == 0) CYC(6);
            STAMP(4);
            if (round == 0) {
                if (W2S_AT != 2 && slice) {
                    if (W2S_AT == 1) load_slice();
                    VMWAIT(0);
                    BARRIER();
                    retile();
                    VMWAIT(0);
                    BARRIER();
                }
                if (W2S_AT != 2 && !mrole) publish(cx, f5so);
                if (spare) {
                    poll<3>(OFF_F1 + tid * 16, cx);
                    BARRIER();
                } else {
                    if (q == 27) {
                        __builtin_amdgcn_s_sleep(AGG_SLEEP);
                        poll<4>(OFF_F1 + tid * 16, cx); BARRIER(); publish(cx, gso);
                        poll<4>(OFF_F1 + 8 + tid * 16, cx); BARRIER(); publish(cx, gso + 8);
                    }
                    if (w == 0) poll<6>(opaque(OFF_F4 + tb * FSTR), cx);
                    BARRIER();
                }
                STAMP(5);
                const unsigned sh = nround, nb = 256 >> sh, c = tid & (2 * nround - 1), blk = tid >> sh;
                const unsigned uvo = blk * 10240 + c * 16, ua = opaque(L_U + 480 + c * U_CH + blk * 16), uso = OFF_XNB + ch * 16;
                v4u ub[4];
                sfor<4>([&](auto i) { ub[i] = ld128c<0>(uvo, cx.rws, sel(i < 2 * nround, uso + i * nb * 10240, WS_USED)); });
                lds_st<v2u>(L_U + w * U_CH + sel((tid & 63) < 60, tid & 63, 59) * 8, v2u{0, 0});
                VMWAIT(0);
                sfor<4>([&](auto i) { tie(ub[i]); });
                lds_st<v4u>(ua, ub[0]); lds_st<v4u>(ua + nb * 16, ub[1]);
                if (nround == 2) { lds_st<v4u>(ua + 2048, ub[2]); lds_st<v4u>(ua + 3072, ub[3]); }
            }
            if (mrole) __builtin_amdgcn_s_setprio(MPRIO);
            if (w == 0) { if (!spare) poll<3>(opaque(OFF_F4 + 8 + tb * FSTR), cx); else if (mrole) poll<3>(gvo + 8, cx); }
            BARRIER();
            if (mrole) w2_lit(ic<0>{}, ic<NLIT>{});
            STAMP(6);
            conv_pair(cx, ch + cidx, L_U + (2 * round + cidx) * U_CH, dskip);
            ch += 2;
        }
        VMWAIT(0);
        BARRIER();
        publish(cx, OFF_F3 + (40 * g + sel(mrole, q + 4, q)) * FSTR);
        STAMP(8);
        if (!mrole) {
            if (b == 0 && w == 0) { poll<8>(gvo + 8, cx); publish(cx, OFF_W); }
            return;
        }
        late_lit = false;
    }

    cx.e_lo = rfl(cx.e_lo); cx.e_hi = rfl(cx.e_hi);
    PIN(); asm volatile("s_nop 4"); PIN();
    v4u wb[5], hf[3][4];
    sfor<4>([&](auto k) {
        const unsigned so = slot_off<2 * k + 1, 5120>(sw);
        set_m0(w * 20480 + k * 5120);
        sfor<4>([&](auto c) { dmac<c * 1024>(w16a, cx.rws, so); });
        set_m0(w * 20480 + k * 5120 + 4096);
        dmac<0>(w16b, cx.rws, so);
    });
    auto ldw = [&](auto s_, auto c_) {
        constexpr int s = s_, c = c_;
        if constexpr (c == 0) { w2so = slot_off<s, 5120>(sw); if ((ABL & 1) && s > 0) w2so = WS_USED; }
        if constexpr (c < 4) wb[c] = ld128c<c * 1024>(w16a, cx.rws, w2so); else wb[c] = ld128c<0>(w16b, cx.rws, w2so);
        PIN();
    };
    sfor<5>([&](auto c) { ldw(ic<0>{}, c); });
    if (late_lit) w2_lit(ic<0>{}, ic<NLIT>{});
    unsigned hv[4], hso2;
    sfor<4>([&](auto t) { hv[t] = (4 * rt + ((t + w) & 3)) * (NKS * 1024) + lane * 16; });
    Slots sh;
    sh.thr = sw.thr; sh.a = OFF_H + k0s * 1024; sh.b = sh.a - nk * 1024; sh.last = sel(w < 2, OFF_H + klast * 1024, WS_USED);
    auto ldh = [&](auto s_, auto t_) {
        constexpr int s = s_, t = t_;
        if constexpr (s < 14) {
            if constexpr (t == 0) { hso2 = slot_off<s, 1024>(sh); if (ABL & 8) hso2 = WS_USED; }
            hf[s % 3][t] = ld128c<0>(hv[t], cx.rws, hso2);
        }
        PIN();
    };
    v2u xo[5], plv3;
    sfor<5>([&](auto c) { xo[c] = ld64<c * 32>(8 * (lane >> 4), mk_rsrc(p.b2, SD * 2), 160 * ct); });
    if (w == 0) poll<1>(OFF_F2 + (16 * (rt >> 1) + sel(lane < 14, lane, 0)) * FSTR, cx);
    VMWAIT(0);
    BARRIER();
    sfor<5>([&](auto c) { tie(wb[c]); tie(xo[c]); });
    STAMP(9);
    sfor<2>([&](auto s) { sfor<4>([&](auto t) { ldh(s, t); }); });
    f4 acc2[5][4];
    sfor<5>([&](auto c) { acc2[c][0] = f4{blo(xo[c].x), bhi(xo[c].x), blo(xo[c].y), bhi(xo[c].y)}; tie(acc2[c][0]); });
    const unsigned xso = (64 * rt + 16 * w) * (SD * 2) + 160 * ct;
    unsigned xvo[3], rw[3];
    auto give = [&](auto c) {
        if constexpr (c == 0) sfor<3>([&](auto t1) { rw[t1] = opaque(L_RED + ((w + t1 + 1) & 3) * 7680 + t1 * 2560 + opaque(lane) * 8); });
        sfor<3>([&](auto t1) { tie(acc2[c][t1 + 1]); const f4 v = acc2[c][t1 + 1]; lds_st<v2u>(rw[t1] + c * 512, v2u{pk(v[0], v[1]), pk(v[2], v[3])}); });
        PIN();
    };
    {
        const unsigned f3vo = OFF_F3 + (40 * ct + 9 * w + sel(lane < 9, lane, 0)) * FSTR;
        const unsigned la = opaque(w * 20480 + lane * 16);
        v4u lf[2];
        sfor<14>([&](auto s_) {
            constexpr int s = s_;
            constexpr bool from_lds = (s & 1) && s < 9;
            VMWAIT(slot_wait(s));
            sfor<4>([&](auto t) { tie(hf[s % 3][t]); });
            if constexpr (s % 2 == 0 && s >= 2 && s <= 10) sfor<5>([&](auto c) { tie(wb[c]); });
            if constexpr (s == 9) plv3 = ld64c<0>(f3vo, cx.rws, 0);
            if constexpr (s == 12) {
                tie(plv3);
                if (!flags_match(plv3, cx)) poll<1>(f3vo, cx);
                BARRIER();
                const unsigned so = (ABL & 4) ? WS_USED : OFF_ST + 80 * ct * 4096 + 128 * rt;
                sfor<3>([&](auto ii) {
                    const unsigned P = opaque(tid) + 256 * ii, row = (P * 7282) >> 16, pc = P - 9 * row;
                    set_m0(L_SIMG + ii * 4096 + w * 1024);
                    dmac<0>(sel(pc < 8 && row < 80, row * 4096 + pc * 16, OOB), cx.rws, so);
                });
                set_m0(L_XIMG + w * 3072);
                sfor<3>([&](auto i) {
                    const unsigned P = opaque(lane) + 64 * i, row = (P * 5958) >> 16, pc = P - 11 * row;
                    xvo[i] = sel(pc < 10 && row < 16, row * (SD * 2) + pc * 16, OOB);
                    dma<i * 1024>(xvo[i] - i * 1024, rx, xso);
                });
            }
            PIN();
            sfor<5>([&](auto c_) {
                constexpr int c = c_;
                if constexpr (from_lds && c < 4) { lf[(c + 1) & 1] = lds_ld<v4u>(la + (s >> 1) * 5120 + (c + 1) * 1024); PIN(); }
                sfor<4>([&](auto t) {
                    if constexpr (s == 0 && t > 0) mfma16z(acc2[c][t], wb[c], hf[0][t]);
                    else if constexpr (s % 2 == 0 && s <= 10) mfma16(acc2[c][t], wb[c], hf[s % 3][t]);
                    else if constexpr (from_lds) mfma16(acc2[c][t], lf[c & 1], hf[s % 3][t]);
                    else mfma16lit<VLIT + 4 * (5 * (s == 9 ? 0 : s - 10) + c)>(acc2[c][t], hf[s % 3][t]);
                });
                PIN();
                if constexpr (s == 13 && c >= 2) give(ic<c - 2>{});
                if constexpr (slot_reloads(s)) ldw(ic<s + 2>{}, c_);
                if constexpr (c < 4) ldh(ic<s + 2>{}, c_);
                if constexpr (s % 2 == 0 && s < 8 && c == 3) { lf[0] = lds_ld<v4u>(la + (s >> 1) * 5120); PIN(); }
            });
        });
    }
    STAMP(10);
    CY5(5);
    const unsigned ln = opaque(lane), ra = opaque(L_RED + w * 7680 + ln * 8);
    const unsigned sa = opaque(L_SIMG + (4 * (ln >> 4) + ((ln & 15) >> 2)) * SIMG_ROW + 32 * w + 8 * (ln & 3));
    const unsigned oa = opaque(L_OUT + w * 3072 + (ln & 15) * OUT_ROW + 8 * (ln >> 4)), ob = opaque(L_OUT + w * 3072 + ln * 16);
    const unsigned hit = sel(((ln & 15) >> 2) == (ln >> 4), sel(ln & 1, 0x3f800000u, 0x3f80u), 0), p0 = sel(ln & 2, 0, hit), p1 = sel(ln & 2, hit, 0);
    v2u pick = {p0, p1};
    give(ic<3>{});
    tie(pick);
    MFMA_PAD(1);
    give(ic<4>{});
    CY5(6);
    VMWAIT(0);
    BARRIER();
    CY5(7);
    {
        auto wide = [](v2u v) { return f4{blo(v.x), bhi(v.x), blo(v.y), bhi(v.y)}; };
        v2u pa[5][3], sq[5], xq[5];
        sfor<3>([&](auto j) { sfor<5>([&](auto c) { pa[c][j] = lds_ld<v2u>(ra + j * 2560 + c * 512); }); });
        sfor<5>([&](auto c) { sq[c] = lds_tr(sa + c * 16 * SIMG_ROW); xq[c] = lds_ld<v2u>(oa + (L_XIMG - L_OUT) + c * 32); });
        PIN();
        sfor<3>([&](auto j) { sfor<5>([&](auto c) { mfma16k16(acc2[c][0], pick, pa[c][j]); }); });
        f4 sf[5], xf[5], d[5];
        sfor<5>([&](auto c) { sf[c] = wide(sq[c]); xf[c] = wide(xq[c]); tie(sf[c]); tie(xf[c]); });
        PIN();
        sfor<5>([&](auto c) { tie(acc2[c][0]); d[c] = acc2[c][0] * -1.44269504088896f; });
        sfor<5>([&](auto c) { d[c] = f4{__builtin_amdgcn_exp2f(d[c][0]), __builtin_amdgcn_exp2f(d[c][1]), __builtin_amdgcn_exp2f(d[c][2]), __builtin_amdgcn_exp2f(d[c][3])}; });
        sfor<5>([&](auto c) { d[c] = d[c] + 1.f; sf[c] = sf[c] * acc2[c][0]; });
        sfor<5>([&](auto c) { d[c] = f4{__builtin_amdgcn_rcpf(d[c][0]), __builtin_amdgcn_rcpf(d[c][1]), __builtin_amdgcn_rcpf(d[c][2]), __builtin_amdgcn_rcpf(d[c][3])}; });
        sfor<5>([&](auto c) { const f4 o = sf[c] * d[c] + xf[c]; lds_st<v2u>(oa + c * 32, v2u{pk(o[0], o[1]), pk(o[2], o[3])}); });
        CY5(8);
        PIN(); asm volatile("" ::: "memory"); PIN();
        const rsrc_t ro = mk_rsrc(p.out, SL * SD * 2);
        v4u ov[3];
        sfor<3>([&](auto i) { ov[i] = lds_ld<v4u>(ob + i * 1024); });
        PIN();
        sfor<3>([&](auto i) { st128(ov[i], xvo[i], ro, xso); });
    }
}

extern "C" void kernel_launch(void* const* d_in, const int* in_sizes, int n_in, void* d_out, int out_size, void* d_ws, size_t ws_size, hipStream_t stream) {
    Params p;
    p.x = (const u16*)d_in[0]; p.lar = (const u16*)d_in[1]; p.aim = (const u16*)d_in[2]; p.cr = (const u16*)d_in[3]; p.ci = (const u16*)d_in[4];
    p.ldt = (const u16*)d_in[5]; p.dsk = (const u16*)d_in[6]; p.w1 = (const u16*)d_in[8]; p.b1 = (const u16*)d_in[9];
    p.w2 = (const u16*)d_in[10]; p.b2 = (const u16*)d_in[11]; p.out = (u16*)d_out; p.ws = (char*)d_ws;
    (void)hipFuncSetAttribute((const void*)s4d_mixer_kernel, hipFuncAttributeMaxDynamicSharedMemorySize, LDS_BYTES);
    s4d_mixer_kernel<<<512, 256, LDS_BYTES, stream>>>(p);
}
```

```cpp
#include <hip/hip_runtime.h>
#include <stdint.h>
#include <utility>

typedef unsigned short u16;
typedef unsigned long long u64;
typedef unsigned v4u __attribute__((ext_vector_type(4)));
typedef unsigned v2u __attribute__((ext_vector_type(2)));
typedef float f2 __attribute__((ext_vector_type(2)));
typedef float f4 __attribute__((ext_vector_type(4)));
typedef float f16v __attribute__((ext_vector_type(16)));
typedef short s4v __attribute__((ext_vector_type(4)));
typedef __bf16 bf2v __attribute__((ext_vector_type(2)));
typedef __amdgpu_buffer_rsrc_t rsrc_t;
#define LAS __attribute__((address_space(3)))
#define CAS __attribute__((address_space(4)))
#define DI __device__ __forceinline__
#define PIN() __builtin_amdgcn_sched_barrier(0)
#define BARRIER() do { PIN(); asm volatile("s_waitcnt lgkmcnt(0)\n\ts_barrier" ::: "memory"); PIN(); } while (0)
#define VMWAIT(n) do { PIN(); asm volatile("s_waitcnt vmcnt(%0)" :: "i"(n) : "memory"); PIN(); } while (0)

#define FSTR 128
#define MDELAY 4
#define MPRIO 2
#define SPRIO 1
#define W2S_AT 3
#define AGG_SLEEP 8
#define B2WARM 1
#define ABL 0
#define G2MAP 1
#define ROTH 1
#define SPARE_FIRST 0
#define GCHK 17
#define NEARLY 6
#define NTL 0
#define KROT 1
#define F1AT 3
#define SCALAR_POW 0
#define AGG_EARLY 1
#define FASTP 1
#define LEARLY 0
#define HINT_AT 6
#define STAMPS 0

constexpr int SL = 2048, SD = 640, SF = 1728, NKS = SF / 32;
constexpr unsigned OOB = 0x40000000u;
constexpr unsigned OFF_W = 0, OFF_TMO = 128, OFF_F1 = 4096, OFF_F2 = OFF_F1 + 4096, OFF_F3 = OFF_F2 + 256 * FSTR, OFF_F4 = OFF_F3 + 320 * FSTR, OFF_G = OFF_F4 + 256 * FSTR, OFF_DBG = 0x1000000;
constexpr unsigned OFF_STATS = 245760, OFF_XNB = 262144, OFF_W2T = OFF_XNB + SL * SD * 2, OFF_ST = OFF_W2T + SF * SD * 2;
constexpr unsigned OFF_H = OFF_ST + SL * SD * 2, WS_USED = OFF_H + SL * SF * 2;
constexpr unsigned CODE_LINES = 256;
constexpr unsigned SPIN_CAP = 1u << 16;
static_assert(OFF_G + 8 * FSTR <= OFF_STATS, "workspace map");

constexpr unsigned L_W2S = 56320;
constexpr unsigned L_XY = 0, XY_ROW = 528, XY_X = 32 * XY_ROW, XY_CH = 28160, L_KT = 68608, KT_CH = 2048;
constexpr unsigned L_RC = 0, RC_COPY = 4160, RC_CH = 4 * RC_COPY, L_U = 2 * RC_CH, U_CH = 4608, L_RED2 = L_U + 4 * U_CH;
constexpr unsigned L_LNT = 32768, L_WH = 49152, L_CS = 16384, L_B1 = L_CS + 512, L_RED = 0, L_SIMG = 32768, SIMG_ROW = 144, L_OUT = 49152, L_XIMG = 61440, OUT_ROW = 176;
constexpr unsigned LDS_BYTES = 81920;
constexpr int VLIT = 176, NLIT = 20;

struct Params {
    const u16 *x, *lar, *aim, *cr, *ci, *ldt, *dsk, *w1, *b1, *w2, *b2;
    u16* out;
    char* ws;
};

template <int... Is, class Fn> DI void sfor_impl(std::integer_sequence<int, Is...>, Fn&& fn) { (fn(std::integral_constant<int, Is>{}), ...); }
template <int N, class Fn> DI void sfor(Fn&& fn) { sfor_impl(std::make_integer_sequence<int, N>{}, fn); }
template <int N> using ic = std::integral_constant<int, N>;

DI unsigned rfl(unsigned v) { return __builtin_amdgcn_readfirstlane(v); }
DI unsigned sel(bool c, unsigned a, unsigned b) { return c ? a : b; }
DI unsigned opaque(unsigned v) { asm volatile("" : "+v"(v)); return v; }
DI float opaquef(float v) { asm("" : "+v"(v)); return v; }
DI float blo(unsigned d) { return __uint_as_float(d << 16); }
DI float bhi(unsigned d) { return __uint_as_float(d & 0xffff0000u); }
DI unsigned pk(float lo, float hi) { f2 v = {lo, hi}; return __builtin_bit_cast(unsigned, __builtin_convertvector(v, bf2v)); }
DI float rbf(float v) { return blo(pk(v, v)); }
DI float dot2(unsigned a, unsigned b, float c) { return __builtin_amdgcn_fdot2_f32_bf16(__builtin_bit_cast(bf2v, a), __builtin_bit_cast(bf2v, b), c, false); }
template <class T> DI T lds_ld(unsigned a) { return *(const LAS T*)(size_t)a; }
template <class T> DI void lds_st(unsigned a, T v) { *(LAS T*)(size_t)a = v; }
DI v2u lds_tr(unsigned a) { return __builtin_bit_cast(v2u, __builtin_amdgcn_ds_read_tr16_b64_v4i16((LAS s4v*)(size_t)a)); }
DI rsrc_t mk_rsrc(const void* p, unsigned bytes) { return __builtin_amdgcn_make_buffer_rsrc((void*)p, 0, bytes, 0x00020000); }

template <int CTRL> DI float dpp_add(float v) {
    return opaquef(v + __builtin_bit_cast(float, __builtin_amdgcn_update_dpp(0, __builtin_bit_cast(int, v), CTRL, 0xf, 0xf, true)));
}
DI float pair_sum(float a, float b) {
    const auto r = __builtin_amdgcn_permlane32_swap(__float_as_uint(a), __float_as_uint(b), false, false);
    float v = opaquef(__uint_as_float(r[0]) + __uint_as_float(r[1]));
    v = dpp_add<0xB1>(v); v = dpp_add<0x4E>(v); v = dpp_add<0x141>(v); v = dpp_add<0x140>(v);
    return dpp_add<0x142>(v);
}
template <int LANE> DI float rdl(float v) { return __builtin_bit_cast(float, __builtin_amdgcn_readlane(__builtin_bit_cast(int, v), LANE)); }
DI float inv_sd(float var) { return __builtin_amdgcn_rcpf(rbf(__builtin_amdgcn_sqrtf(rbf(rbf(fmaxf(var, 0.f)) + 1e-5f)))); }

template <int IMM> DI void dma(unsigned vo, rsrc_t rs, unsigned so) { asm volatile("buffer_load_dwordx4 %0, %1, %2 offen offset:%3 lds" :: "v"(vo), "s"(rs), "s"(so), "i"(IMM)); }
template <int IMM> DI void dman(unsigned vo, rsrc_t rs, unsigned so) { asm volatile("buffer_load_dwordx4 %0, %1, %2 offen offset:%3 nt lds" :: "v"(vo), "s"(rs), "s"(so), "i"(IMM)); }
template <int IMM> DI void dmac(unsigned vo, rsrc_t rs, unsigned so) { asm volatile("buffer_load_dwordx4 %0, %1, %2 offen offset:%3 sc1 lds" :: "v"(vo), "s"(rs), "s"(so), "i"(IMM)); }
DI void set_m0(unsigned v) { asm volatile("s_mov_b32 m0, %0\n\ts_nop 0" :: "s"(v)); }
template <int IMM> DI unsigned ld32(unsigned vo, rsrc_t rs, unsigned so) { unsigned r; asm volatile("buffer_load_dword %0, %1, %2, %3 offen offset:%4" : "=v"(r) : "v"(vo), "s"(rs), "s"(so), "i"(IMM)); return r; }
DI unsigned ld16(unsigned vo, rsrc_t rs, unsigned so) { unsigned r; asm volatile("buffer_load_ushort %0, %1, %2, %3 offen" : "=v"(r) : "v"(vo), "s"(rs), "s"(so)); return r; }
template <int IMM> DI v2u ld64(unsigned vo, rsrc_t rs, unsigned so) { v2u r; asm volatile("buffer_load_dwordx2 %0, %1, %2, %3 offen offset:%4" : "=v"(r) : "v"(vo), "s"(rs), "s"(so), "i"(IMM)); return r; }
template <int IMM> DI v2u ld64c(unsigned vo, rsrc_t rs, unsigned so) { v2u r; asm volatile("buffer_load_dwordx2 %0, %1, %2, %3 offen offset:%4 sc1" : "=v"(r) : "v"(vo), "s"(rs), "s"(so), "i"(IMM)); return r; }
template <int IMM> DI v4u ld128c(unsigned vo, rsrc_t rs, unsigned so) { v4u r; asm volatile("buffer_load_dwordx4 %0, %1, %2, %3 offen offset:%4 sc1" : "=v"(r) : "v"(vo), "s"(rs), "s"(so), "i"(IMM)); return r; }
DI v4u ld128(unsigned vo, rsrc_t rs, unsigned so) { v4u r; asm volatile("buffer_load_dwordx4 %0, %1, %2, %3 offen" : "=v"(r) : "v"(vo), "s"(rs), "s"(so)); return r; }
template <int R, int IMM> DI void ld128lit(unsigned vo, rsrc_t rs, unsigned so) {
    asm volatile("buffer_load_dwordx4 v[%c3:%c4], %0, %1, %2 offen offset:%5 sc1" :: "v"(vo), "s"(rs), "s"(so), "i"(R), "i"(R + 3), "i"(IMM));
}
template <int R> DI void ld16lit(unsigned vo, rsrc_t rs, unsigned so) {
    if (NTL & 2) asm volatile("buffer_load_ushort v%c3, %0, %1, %2 offen nt" :: "v"(vo), "s"(rs), "s"(so), "i"(R));
    else asm volatile("buffer_load_ushort v%c3, %0, %1, %2 offen" :: "v"(vo), "s"(rs), "s"(so), "i"(R));
}
template <int R> DI void ld64clit(unsigned vo, rsrc_t rs) { asm volatile("buffer_load_dwordx2 v[%c2:%c3], %0, %1, 0 offen sc1" :: "v"(vo), "s"(rs), "i"(R), "i"(R + 1)); }
template <int R> DI unsigned lit() { unsigned r; asm volatile("v_mov_b32 %0, v%c1" : "=v"(r) : "i"(R)); return r; }
template <int IMM> DI void st128c(v4u v, unsigned vo, rsrc_t rs, unsigned so) { asm volatile("buffer_store_dwordx4 %0, %1, %2, %3 offen offset:%4 sc1\n\ts_nop 1" :: "v"(v), "v"(vo), "s"(rs), "s"(so), "i"(IMM)); }
DI void st64c(v2u v, unsigned vo, rsrc_t rs, unsigned so) { asm volatile("buffer_store_dwordx2 %0, %1, %2, %3 offen sc1" :: "v"(v), "v"(vo), "s"(rs), "s"(so)); }
DI void st128(v4u v, unsigned vo, rsrc_t rs, unsigned so) {
    if (NTL & 16) asm volatile("buffer_store_dwordx4 %0, %1, %2, %3 offen nt\n\ts_nop 1" :: "v"(v), "v"(vo), "s"(rs), "s"(so));
    else if (NTL & 32) asm volatile("buffer_store_dwordx4 %0, %1, %2, %3 offen sc1\n\ts_nop 1" :: "v"(v), "v"(vo), "s"(rs), "s"(so));
    else asm volatile("buffer_store_dwordx4 %0, %1, %2, %3 offen\n\ts_nop 1" :: "v"(v), "v"(vo), "s"(rs), "s"(so));
}
template <int IMM> DI void lds_st_tid(unsigned v) { asm volatile("ds_write_addtid_b32 %0 offset:%1" :: "v"(v), "i"(IMM)); }
template <class T> DI void tie(T& v) { asm volatile("" : "+v"(v)); }

DI void mfma32(f16v& c, v4u a, v4u b) { asm volatile("v_mfma_f32_32x32x16_bf16 %0, %1, %2, %0" : "+v"(c) : "v"(a), "v"(b)); }
DI void mfma32z(f16v& c, v4u a, v4u b) { asm volatile("v_mfma_f32_32x32x16_bf16 %0, %1, %2, 0" : "=&v"(c) : "v"(a), "v"(b)); }
DI void mfma16(f4& c, v4u a, v4u b) { asm volatile("v_mfma_f32_16x16x32_bf16 %0, %1, %2, %0" : "+v"(c) : "v"(a), "v"(b)); }
DI void mfma16z(f4& c, v4u a, v4u b) { asm volatile("v_mfma_f32_16x16x32_bf16 %0, %1, %2, 0" : "=&v"(c) : "v"(a), "v"(b)); }
DI void mfma16k16(f4& c, v2u a, v2u b) { asm volatile("v_mfma_f32_16x16x16_bf16 %0, %1, %2, %0" : "+v"(c) : "v"(a), "v"(b)); }
template <int R> DI void mfma16lit(f4& c, v4u b) { asm volatile("v_mfma_f32_16x16x32_bf16 %0, v[%c2:%c3], %1, %0" : "+v"(c) : "v"(b), "i"(R), "i"(R + 3)); }
#define MFMA_PAD(n) do { PIN(); asm volatile("s_nop %0" :: "i"(n)); PIN(); } while (0)

DI float fadd(float a, float b) { float r; asm("v_add_f32 %0, %1, %2" : "=v"(r) : "v"(a), "v"(b)); return r; }
DI float ffma(float a, float b, float c) { float r; asm("v_fma_f32 %0, %1, %2, %3" : "=v"(r) : "v"(a), "v"(b), "v"(c)); return r; }
DI f2 cmul(f2 p, f2 w) {
    if (SCALAR_POW) {
        float a, b;
        asm volatile("v_mul_f32 %0, %2, %4\n\tv_mul_f32 %1, %2, %5\n\tv_fma_f32 %0, -%3, %5, %0\n\tv_fma_f32 %1, %3, %4, %1" : "=&v"(a), "=&v"(b) : "v"(p.x), "v"(p.y), "v"(w.x), "v"(w.y));
        return f2{a, b};
    }
    f2 r;
    asm volatile("v_pk_mul_f32 %0, %1, %2 op_sel:[0,0] op_sel_hi:[0,1]\n\tv_pk_fma_f32 %0, %1, %2, %0 op_sel:[1,1,0] op_sel_hi:[1,0,1] neg_lo:[0,1,0]" : "=&v"(r) : "v"(p), "v"(w));
    return r;
}
DI f2 cexp_at(float er, float rev, float p) {
    const float m = __builtin_amdgcn_exp2f(er * p), fr = __builtin_amdgcn_fractf(rev * p);
    return f2{__builtin_amdgcn_cosf(fr), __builtin_amdgcn_sinf(fr)} * m;
}

struct Ctx {
    rsrc_t rws;
    unsigned e_lo, e_hi;
    unsigned tid, lane, w, flag_vo;
};
#define STAMP(i) do {} while (0)
#define CYC(i) do {} while (0)
#define CY6(i) do {} while (0)
#define CY5(i) do {} while (0)

DI bool flags_match(v2u v, const Ctx& cx) { return __all(v.x == cx.e_lo && v.y == cx.e_hi); }
DI void publish(const Ctx& cx, unsigned so) { st64c(v2u{cx.e_lo, cx.e_hi}, cx.flag_vo, cx.rws, so); }
template <int SLEEP> DI void poll(unsigned vo, const Ctx& cx) {
#pragma unroll 1
    for (unsigned spins = 0;; ++spins) {
        v2u v = ld64c<0>(vo, cx.rws, 0);
        VMWAIT(0); tie(v);
        if (flags_match(v, cx)) break;
        if (spins > SPIN_CAP) { st64c(v2u{1u, 1u}, sel(cx.lane == 0, OFF_TMO, OOB), cx.rws, 0); break; }
        __builtin_amdgcn_s_sleep(SLEEP);
    }
}
DI void take_epoch(Ctx& cx) {
    unsigned lo, hi;
    asm volatile("v_readfirstlane_b32 %0, v%c2\n\tv_readfirstlane_b32 %1, v%c3" : "=s"(lo), "=s"(hi) : "i"(VLIT + 4), "i"(VLIT + 5));
    const u64 e = ((u64)hi << 32 | lo) + 0x9E3779B97F4A7C15ull;
    cx.e_lo = (unsigned)e; cx.e_hi = (unsigned)(e >> 32);
}
template <int S, int STRIDE> DI unsigned rot_off(unsigned base, unsigned wrapped, unsigned thr) {
    unsigned r;
    asm volatile("s_cmp_le_u32 %3, %4\n\ts_cselect_b32 %0, %2, %1\n\ts_add_u32 %0, %0, %5" : "=&s"(r) : "s"(base), "s"(wrapped), "s"(thr), "i"(S), "i"(S * STRIDE) : "scc");
    return r;
}
struct Slots { unsigned a, b, last, thr; };
template <int S, int STRIDE> DI unsigned slot_off(const Slots& s) { if constexpr (S == 13) return s.last; else return rot_off<S, STRIDE>(s.a, s.b, s.thr); }

template <class H1, class H2> DI f2 layernorm_rows(const unsigned (&xa)[5], const unsigned (&xb)[5], const Ctx& cx, H1&& hook1, H2&& hook2) {
    constexpr unsigned ONES = 0x3f803f80u;
    float sa = 0.f, sb = 0.f;
    sfor<5>([&](auto i) { sa = dot2(xa[i], ONES, sa); sb = dot2(xb[i], ONES, sb); });
    const float mean = pair_sum(sa, sb) * (1.f / SD), mr = rbf(mean), dm = mean - mr;
    const float mra = rdl<31>(mr), mrb = rdl<63>(mr);
    PIN(); hook1(); PIN();
    unsigned ta[5], tb[5];
    f2 qa = {0.f, 0.f}, qb = {0.f, 0.f};
    sfor<5>([&](auto i) {
        const f2 da = f2{blo(xa[i]), bhi(xa[i])} - mra, db = f2{blo(xb[i]), bhi(xb[i])} - mrb;
        qa = da * da + qa; qb = db * db + qb;
        ta[i] = pk(da.x, da.y); tb[i] = pk(db.x, db.y);
    });
    const float r1 = inv_sd(pair_sum(qa.x + qa.y, qb.x + qb.y) * (1.f / SD) - dm * dm);
    const float ra = rdl<31>(r1), rb = rdl<63>(r1);
    PIN(); hook2(); PIN();
    float s0 = 0.f, s1 = 0.f, s2 = 0.f, s3 = 0.f;
    unsigned o[10];
    sfor<5>([&](auto i) {
        const f2 na = f2{blo(ta[i]), bhi(ta[i])} * ra, nb = f2{blo(tb[i]), bhi(tb[i])} * rb;
        const unsigned ua = pk(na.x, na.y), ub = pk(nb.x, nb.y);
        s0 = dot2(ua, ONES, s0); s1 = dot2(ua, ua, s1); s2 = dot2(ub, ONES, s2); s3 = dot2(ub, ub, s3);
        o[2 * i] = __builtin_amdgcn_perm(ub, ua, 0x05040100u); o[2 * i + 1] = __builtin_amdgcn_perm(ub, ua, 0x07060302u);
    });
    const unsigned la = opaque(L_LNT + cx.w * 2560 + cx.lane * 16), lb = opaque(L_LNT + cx.w * 2560 + 2048 + cx.lane * 8);
    lds_st<v4u>(la, v4u{o[0], o[1], o[2], o[3]});
    lds_st<v4u>(la + 1024, v4u{o[4], o[5], o[6], o[7]});
    lds_st<v2u>(lb, v2u{o[8], o[9]});
    const float m2 = pair_sum(s0, s2) * (1.f / SD), r2 = inv_sd(pair_sum(s1, s3) * (1.f / SD) - m2 * m2);
    return f2{r1 * r2, -(mr * r1 + rbf(m2)) * r2};
}

struct Modes { unsigned la, ai, cr, ci; };
DI void mode_powers(const Ctx& cx, const Modes& md, unsigned ldt) {
    constexpr float LOG2E = 1.44269504088896f, INV2PI = 0.159154943091895f;
    set_m0(L_XY + (cx.w >> 1) * XY_CH + (cx.w & 1) * 256);
    const float dt = rbf(__builtin_amdgcn_exp2f(blo(ldt) * LOG2E));
    const float er = (-rbf(__builtin_amdgcn_exp2f(blo(md.la) * LOG2E)) * dt) * LOG2E, rev = (blo(md.ai) * dt) * INV2PI;
    const f2 v1 = cexp_at(er, -rev, 1.f), v256 = cexp_at(er, -rev, 256.f), w8 = cexp_at(er, rev, 8.f), w32 = cexp_at(er, rev, 32.f);
    auto put = [&](auto row, f2 v) { lds_st_tid<row * XY_ROW>(pk(v.x, v.y)); };
    f2 x[8], y[4];
    x[0] = f2{blo(md.cr), blo(md.ci)} * dt * f2{2.f, -2.f};
    const f2 v2 = cmul(v1, v1);
    x[1] = cmul(x[0], v1);
    y[0] = w32; y[1] = w8; y[2] = cmul(w8, w8);
    lds_st_tid<0>(opaque(0x3f80u));
    put(ic<1>{}, w8); put(ic<4>{}, w32); put(ic<32>{}, x[0]);
    y[3] = cmul(y[2], w8);
    put(ic<33>{}, x[1]); put(ic<2>{}, y[2]);
    sfor<6>([&](auto i) { x[i + 2] = cmul(x[i], v2); if constexpr (i == 0) put(ic<3>{}, y[3]); else put(ic<33 + i>{}, x[i + 1]); });
    sfor<4>([&](auto i) { const f2 prev = i == 0 ? x[7] : x[i == 0 ? 0 : 2 * i - 2]; x[2 * i] = cmul(x[2 * i], v256); put(ic<39 + i>{}, prev); });
    f2 pend = x[6];
    sfor<27>([&](auto i_) {
        constexpr int i = i_, J = i + 5;
        y[J & 3] = cmul(y[J & 3], w32);
        if constexpr (i < 10) put(ic<43 + i>{}, pend);
        if constexpr (i < 9) { constexpr int k = i < 4 ? 4 * (i & 1) : 0; x[k] = cmul(x[k], v256); pend = x[k]; }
        if constexpr (i > 0) put(ic<J - 1>{}, y[(J - 1) & 3]);
    });
    put(ic<31>{}, y[3]);
}

DI void kernel_table(const Ctx& cx, unsigned kt) {
    const unsigned cidx = cx.w >> 1, half = cx.w & 1, r = cx.lane & 15, g4 = cx.lane >> 4;
    const unsigned xa = opaque(L_XY + cidx * XY_CH + XY_X + r * XY_ROW + g4 * 16), ya = opaque(xa - XY_X + half * (16 * XY_ROW));
    const unsigned ta = opaque(kt + g4 * 256 + (16 * half + r) * 8);
    f4 k0, k1;
    v4u y[8], x0[8], x1[8];
    auto rd = [&](auto ks) { y[ks] = lds_ld<v4u>(ya + ks * 64); x0[ks] = lds_ld<v4u>(xa + ks * 64); x1[ks] = lds_ld<v4u>(xa + 16 * XY_ROW + ks * 64); PIN(); };
    sfor<4>(rd);
    sfor<8>([&](auto ks) {
        if constexpr (ks == 0) { mfma16z(k0, x0[ks], y[ks]); mfma16z(k1, x1[ks], y[ks]); } else { mfma16(k0, x0[ks], y[ks]); mfma16(k1, x1[ks], y[ks]); }
        PIN();
        if constexpr (ks < 4) rd(ic<ks + 4>{});
    });
    MFMA_PAD(7);
    tie(k0); tie(k1);
    lds_st<v2u>(ta, v2u{pk(k0[0], k0[1]), pk(k0[2], k0[3])});
    lds_st<v2u>(ta + 1024, v2u{pk(k1[0], k1[1]), pk(k1[2], k1[3])});
}
DI void expand_taps(const Ctx& cx, unsigned kt) {
    constexpr unsigned LL = 0x01000100u, HL = 0x01000302u, BL = 0x01000504u, BB = 0x05040504u, HH = 0x03020302u;
    const unsigned half = cx.w & 1, J = cx.lane & 31, up = cx.lane >> 5, last = J == 31;
    const bool s1 = !half && !up, s3 = half;
    const unsigned pa = kt + sel(s3, 768 + 4 * up, 512 * up) + J * 8;
    const unsigned qa = sel(s1, pa + 256, sel(last, kt + sel(s3, sel(up, 1024, 772), 768), pa + 8));
    const unsigned a4 = 2 * half + up, ea = kt + 1024 + J * 8 + 2 * a4, fa = sel(last, kt + sel(a4 == 3, 1280, 1026 + 2 * a4), ea + 8);
    const unsigned p0 = lds_ld<unsigned>(pa), p1 = lds_ld<unsigned>(pa + 4), q0 = lds_ld<unsigned>(qa), q1 = lds_ld<unsigned>(qa + 4);
    const unsigned e0 = lds_ld<u16>(ea), e8 = lds_ld<u16>(fa);
    const unsigned bnd = lds_ld<unsigned>(kt + sel(half, 512, 768) + 31 * 8 + 4);
    v4u o[2];
    o[0].w = __builtin_amdgcn_perm(p0, p0, sel(s1, HL, LL));
    o[0].z = __builtin_amdgcn_perm(p1, p0, sel(s1, 0x05040706u, sel(s3, HL, 0x03020504u)));
    o[0].y = __builtin_amdgcn_perm(q0, sel(s3, p0, p1), sel(s1, 0x05040706u, sel(s3, HH, LL)));
    o[0].x = __builtin_amdgcn_perm(q0, sel(s1, q1, p1), sel(s1, HL, sel(s3, BB, 0x03020504u)));
    o[1].w = __builtin_amdgcn_perm(e0, e0, LL);
    o[1].z = o[1].w;
    o[1].y = __builtin_amdgcn_perm(e8, e0, sel(J & 1, BB, BL));
    o[1].x = __builtin_amdgcn_perm(e8, e8, LL);
    const unsigned fx[2] = {sel(half, __builtin_amdgcn_perm(p0, bnd, 0x03020504u), 0), o[1].w};
    const unsigned fy[2] = {sel(half, bnd, 0), sel(half, e0, bnd >> 16)};
    const unsigned dst = opaque(L_RC + (cx.w >> 1) * RC_CH + 16 * (127 - 64 * half - cx.lane));
    sfor<2>([&](auto it) {
        const v4u d = o[it];
        const unsigned nx = __builtin_amdgcn_update_dpp(fx[it], d.x, 0x138, 0xf, 0xf, false), ny = __builtin_amdgcn_update_dpp(fy[it], d.y, 0x138, 0xf, 0xf, false);
        const unsigned t0 = __builtin_amdgcn_alignbyte(d.y, d.x, 2), t1 = __builtin_amdgcn_alignbyte(d.z, d.y, 2), t2 = __builtin_amdgcn_alignbyte(d.w, d.z, 2);
        const unsigned t3 = __builtin_amdgcn_alignbyte(nx, d.w, 2), t4 = __builtin_amdgcn_alignbyte(ny, nx, 2);
        constexpr unsigned off = (1 - it) * 2048;
        lds_st<v4u>(dst + off, d);
        lds_st<v4u>(dst + off + RC_COPY, v4u{t0, t1, t2, t3});
        lds_st<v4u>(dst + off + 2 * RC_COPY, v4u{d.y, d.z, d.w, nx});
        lds_st<v4u>(dst + off + 3 * RC_COPY, v4u{t1, t2, t3, t4});
    });
    lds_st<v2u>(L_RC + ((cx.lane & 31) >> 2) * RC_COPY + 4096 + (cx.lane & 3) * 8, v2u{0, 0});
}
template <class MID> DI void conv_pair(Ctx& cx, unsigned ch, unsigned ubase, float dskip, MID&& mid) {
    const unsigned cidx = cx.w >> 1, half = cx.w & 1;
    f4 ca[8];
    {
        const unsigned r = cx.lane & 15, g4 = cx.lane >> 4;
        const unsigned aa0 = L_RC + cidx * RC_CH + ((~r) & 3) * RC_COPY + 2 * (2028 - 4 * (r >> 2) + 8 * g4) - 3584 - 256 * half - 192;
        const unsigned aa = opaque(aa0), ab = opaque(aa0 + 8), bb = opaque(ubase + 448 + 32 * r + 16 * g4 - 256 * half - 192);
        v2u al[2][8], ah[2][8];
        v4u fb[2][8];
        auto rd = [&](auto d_, auto i_) {
            constexpr int d = d_, i = i_, e = i / 3, o = (3 - d) * 64;
            if constexpr (i % 3 == 0) al[d & 1][e] = lds_ld<v2u>(aa + (7 - e) * 512 + o);
            else if constexpr (i % 3 == 1) ah[d & 1][e] = lds_ld<v2u>(ab + (7 - e) * 512 + o);
            else fb[d & 1][e] = lds_ld<v4u>(bb + e * 512 + o);
            PIN();
        };
        sfor<12>([&](auto i) { rd(ic<0>{}, i); });
        sfor<4>([&](auto d_) {
            constexpr int d = d_;
            sfor<8>([&](auto n_) {
                constexpr int n = n_;
                sfor<n + 1>([&](auto e_) {
                    constexpr int e = e_, idx = n * (n + 1) / 2 + e;
                    const v4u a = {al[d & 1][e].x, al[d & 1][e].y, ah[d & 1][e].x, ah[d & 1][e].y};
                    if constexpr (d == 0 && e == 0) mfma16z(ca[n], a, fb[d & 1][n - e]); else mfma16(ca[n], a, fb[d & 1][n - e]);
                    PIN();
                    if constexpr (d == 0) { if constexpr (idx < 6) { rd(ic<0>{}, ic<12 + 2 * idx>{}); rd(ic<0>{}, ic<13 + 2 * idx>{}); } else if constexpr (idx >= 12) rd(ic<1>{}, ic<idx - 12>{}); }
                    else if constexpr (d < 3 && idx % 3 != 2) rd(ic<d + 1>{}, ic<idx - idx / 3>{});
                    if constexpr (idx == 14 || idx == 35) mid(ic<2 * d + (idx == 35)>{});
                });
            });
        });
    }
    STAMP(7);
    cx.tid = opaque(cx.tid); cx.lane = cx.tid & 63;
    const unsigned r = cx.lane & 15, g4 = cx.lane >> 4;
    const unsigned ra = opaque(L_RED2 + cx.lane * 16 + cx.w * 4096), rb = opaque(L_RED2 + cx.lane * 16 + (cx.w ^ 1) * 4096);
    const unsigned uaddr = opaque(ubase + 480 + 2 * (1024 * half + 16 * r + 4 * g4));
    v2u u[4];
    sfor<4>([&](auto i) { u[i] = lds_ld<v2u>(uaddr + i * 512); });
    MFMA_PAD(7);
    sfor<8>([&](auto n) { tie(ca[n]); });
    if (half == 0) { sfor<4>([&](auto i) { lds_st<f4>(ra + i * 1024, ca[4 + i]); }); }
    else { sfor<4>([&](auto i) { lds_st<f4>(ra + i * 1024, ca[i]); ca[i] = ca[4 + i]; }); }
    f4 uf[4];
    sfor<4>([&](auto i) { uf[i] = f4{ffma(blo(u[i].x), dskip, ca[i][0]), ffma(bhi(u[i].x), dskip, ca[i][1]), ffma(blo(u[i].y), dskip, ca[i][2]), ffma(bhi(u[i].y), dskip, ca[i][3])}; });
    BARRIER();
    v2u pkd[4];
    sfor<4>([&](auto i) { const f4 y = lds_ld<f4>(rb + i * 1024); pkd[i] = v2u{pk(fadd(y[0], uf[i][0]), fadd(y[1], uf[i][1])), pk(fadd(y[2], uf[i][2]), fadd(y[3], uf[i][3]))}; });
    const unsigned vo = 2 * (256 * (g4 & 1) + 16 * r + 8 * (g4 >> 1)), so = (ABL & 2) ? WS_USED : OFF_ST + ch * 4096 + 2048 * half;
    sfor<2>([&](auto i2) {
        const auto sx = __builtin_amdgcn_permlane16_swap(pkd[2 * i2].x, pkd[2 * i2 + 1].x, false, false);
        const auto sy = __builtin_amdgcn_permlane16_swap(pkd[2 * i2].y, pkd[2 * i2 + 1].y, false, false);
        st128c<i2 * 1024>(v4u{sx[0], sy[0], sx[1], sy[1]}, vo, cx.rws, so);
    });
}

constexpr int g1_extra(int u) { return (u == F1AT ? 1 : 0) + (u == GCHK - 3 ? 1 : 0) + (u == 18 ? 3 : 0); }
constexpr int g1_stage(int n) { return (n % 2 == 0) ? (n / 2 + 3 < 20 ? 2 : 0) : (n / 4 + 2 < 10 ? 2 : 0); }
constexpr int g1_early(int n) { return n > 40 - NEARLY / 2 ? 2 : 0; }
constexpr int g1_ops(int lo, int hi) { int k = 0; for (int n = lo; n < hi; ++n) k += (n % 2 == 0 ? g1_extra(n / 2) : 0) + g1_stage(n) + g1_early(n); return k; }
constexpr int g1_wait(int u) {
    return u == 1 ? 10 + g1_ops(0, 2) : u == 2 ? (F1AT == 2 ? 0 : 4) + g1_ops(0, 4) : u % 2 ? g1_early(2 * u - 6) + g1_ops(2 * u - 5, 2 * u) : g1_early(2 * u - 5) + g1_ops(2 * u - 4, 2 * u);
}
static_assert(NEARLY % 2 == 0 && 40 - NEARLY / 2 >= 2 * GCHK - 1 && GCHK <= 18, "early fragments follow the check of the group words");

constexpr bool slot_reloads(int s) { return s % 2 == 0 && s + 2 <= 10; }
constexpr int slot_ops(int s) { return (s + 2 < 14 ? 4 : 0) + (slot_reloads(s) ? 5 : 0) + (s == 9 ? 1 : 0) + (s == 12 ? 6 : 0); }
constexpr int slot_wait(int s) { return s == 0 ? 4 : slot_ops(s - 1); }
static_assert(slot_ops(13) == 0, "");

__global__ void __launch_bounds__(256, 2) __attribute__((amdgpu_num_vgpr(VLIT / 2))) s4d_mixer_kernel(Params p) {
    extern __shared__ __attribute__((aligned(16))) char smem[];
    const unsigned tid = threadIdx.x, lane = tid & 63, w = rfl(tid >> 6);
    const unsigned b = blockIdx.x, g = b & 7, q = (b >> 3) & 31;
    {
        u64 pc;
        asm volatile("s_getpc_b64 %0" : "=s"(pc) :: "v176", "v255");
        const unsigned line = ((b >> 3) & 15) * (CODE_LINES / 16) + w * (CODE_LINES / 64) + (lane & (CODE_LINES / 64 - 1));
        asm volatile("global_load_dword v255, %0, %1" :: "v"(line * 128u), "s"(pc & ~127ull));
    }
    asm volatile("" :: "v"((unsigned)(size_t)(LAS char*)smem));
    Ctx cx;
    STAMP(0);
    const bool mrole = b >= 256, spare = q >= 28;
    const unsigned tb = 32 * g + q, cidx = w >> 1;
    const rsrc_t rx = mk_rsrc(p.x, SL * SD * 2);
    cx.rws = mk_rsrc(p.ws, WS_USED);
    const rsrc_t rla = mk_rsrc(p.lar, SD * 256), rai = mk_rsrc(p.aim, SD * 256), rcr = mk_rsrc(p.cr, SD * 256), rci = mk_rsrc(p.ci, SD * 256);
    const unsigned pvo = w * 128 + lane * 2, ch0 = 80 * g + sel(spare, 6 * q - 112 + sel(mrole, 4, 0), 2 * q);
    const unsigned sid = 27 * g + sel(q < 26, q, 26), sks = sid >> 2, sqq = sid & 3;
    const bool slice = !mrole && q < 27;
    auto load_modes = [&]() {
        ld16lit<VLIT>(pvo, rla, ch0 * 256); ld16lit<VLIT + 1>(pvo, rai, ch0 * 256); ld16lit<VLIT + 2>(pvo, rcr, ch0 * 256); ld16lit<VLIT + 3>(pvo, rci, ch0 * 256);
    };
        auto load_slice = [&]() {
        set_m0(L_W2S + w * 3072);
        sfor<3>([&](auto ii) {
            const unsigned P = 64 * (3 * w + ii) + lane, row = (P * 3277) >> 16, vo = row * (1280 - 320) + P * 16 - ii * 1024;
            if (NTL & 1) dman<ii * 1024>(sel(ii == 0 || w < 3, vo, OOB), mk_rsrc(p.w2, SD * SF * 2), sks * (32 * 1280) + sqq * 320);
            else dma<ii * 1024>(sel(ii == 0 || w < 3, vo, OOB), mk_rsrc(p.w2, SD * SF * 2), sks * (32 * 1280) + sqq * 320);
        });
        };
    asm volatile("s_nop 4");
    if (!mrole) {
        __builtin_amdgcn_s_setprio(SPRIO);
        ld64clit<VLIT + 4>(opaque(OFF_W), cx.rws);
        load_modes();
        if (W2S_AT >= 2 && slice) load_slice();
    }
    cx.tid = tid; cx.lane = lane; cx.w = w; cx.flag_vo = sel(tid == 0, 0, OOB);
    const unsigned w16a = lane * 16, w16b = lane * 16 + 4096;
    const unsigned rt = G2MAP ? 8 * (g >> 1) + (q >> 2) : 4 * g + (q >> 3), ct = G2MAP ? 4 * (g & 1) + (q & 3) : q & 7;
    const unsigned nk = 14 - (w >> 1), rot = (G2MAP ? (ROTH == 2 ? ((q & 3) * 13) >> 2 : ROTH == 1 ? 6 * (q & 1) : 0) + 5 * (q >> 2) : (ROTH == 2 ? (ct * 13) >> 3 : ROTH == 1 ? 6 * (q & 1) : ROTH == 3 ? ((q & 3) * 13) >> 2 : 0) + ((0x7610u >> (4 * (q >> 3))) & 15)) % 13, k0s = 14 * w - sel(w == 3, 1, 0) + rot, klast = k0s + 13 - sel(rot != 0, 14, 0);
    Slots sw;
    sw.thr = nk - rot; sw.a = OFF_W2T + (ct * NKS + k0s) * 5120; sw.b = sw.a - nk * 5120; sw.last = sel(w < 2, OFF_W2T + (ct * NKS + klast) * 5120, WS_USED);
    unsigned w2so = 0;
    const unsigned gvo = opaque(OFF_G + g * FSTR);
    auto w2_lit = [&](auto lo_, auto hi_) {
        constexpr int LO = lo_, HI = hi_;
        sfor<HI - LO>([&](auto ii) {
            constexpr int f = LO + ii, c = f % 5;
            if constexpr (c == 0) w2so = slot_off<(f < 5 ? 9 : 10 + f / 5), 5120>(sw);
            if constexpr (c < 4) ld128lit<VLIT + 4 * f, c * 1024>(w16a, cx.rws, w2so); else ld128lit<VLIT + 4 * f, 0>(w16b, cx.rws, w2so);
        });
    };
    unsigned w2sl = 0;
    auto w2_lds = [&](auto lo_, auto hi_) {
        constexpr int LO = lo_, HI = hi_;
        sfor<HI - LO>([&](auto ii) {
            constexpr int f = LO + ii, c = f % 5;
            if constexpr (c == 0) w2sl = slot_off<2 * (f / 5) + 1, 5120>(sw);
            if constexpr (c == 0 || c == 4 || ii == 0) set_m0(w * 20480 + (f / 5) * 5120 + (c == 4 ? 4096 : 0));
            if constexpr (c < 4) dmac<c * 1024>(w16a, cx.rws, w2sl); else dmac<0>(w16b, cx.rws, w2sl);
        });
    };
    const rsrc_t rw1 = mk_rsrc(p.w1, SD * SF * 2);
    const unsigned ti = 4 * (g >> 1) + q / 7, tj = 7 * (g & 1) + q % 7, wf = w >> 1, wt = w & 1;
    unsigned gx[4], gw[2];
    sfor<4>([&](auto ii) { gx[ii] = (32 * w + 8 * ii + (lane >> 3)) * 1280 + 16 * ((lane & 7) ^ (4 * (ii & 1) + (lane >> 4))) - ii * 1024; });
    sfor<2>([&](auto ii) { gw[ii] = (8 * w + 4 * ii + (lane >> 4)) * (SF * 2) + 16 * ((lane & 15) ^ (((lane >> 4) << 2) | ((2 * w + ii) & 3))) - ii * 1024; });
    const unsigned krot = KROT == 0 ? 0 : KROT == 1 ? 5 * ((q / 7 + q % 7) & 1) : KROT == 2 ? (5 * ((q / 7 + q % 7) & 1) + (g >> 1)) % 10 : KROT == 3 ? (3 * ((q / 7 + q % 7) % 3) + (g >> 1)) % 10 : KROT == 5 ? (g >> 1) * 2 + (g & 1) : (3 * (q % 7) + (5 * (q / 7)) / 2 + g) % 10, kthr = 10 - krot;
    const unsigned sx0 = ti * (128 * 1280) + krot * 128, sw0 = tj * 256 + krot * (64 * SF * 2);
    unsigned curx, curw;
    auto px = [&](auto s_, auto ii_) {
        constexpr int s = s_, ii = ii_;
        if constexpr (s < 10) {
            if constexpr (ii == 0) curx = rot_off<s, 128>(sx0, sx0 - 1280, kthr);
            if constexpr (ii % 2 == 0) set_m0((s % 3) * 16384 + w * 4096);
            if (NTL & 4) dman<ii * 1024>(gx[ii], rx, curx); else dma<ii * 1024>(gx[ii], rx, curx);
        }
        PIN();
    };
    auto pw = [&](auto u_, auto ii_) {
        constexpr int u = u_, ii = ii_;
        if constexpr (u < 20) {
            if constexpr (ii == 0) { curw = rot_off<u / 2, 64 * SF * 2>(sw0 + (u & 1) * (32 * SF * 2), sw0 + (u & 1) * (32 * SF * 2) - SD * SF * 2, kthr); set_m0(L_WH + (u & 3) * 8192 + w * 2048); }
            if (NTL & 8) dman<ii * 1024>(gw[ii], rw1, curw); else dma<ii * 1024>(gw[ii], rw1, curw);
        }
        PIN();
    };
    unsigned b1v;
    v2u xa0, xa1, xb0, xb1;
    unsigned xa4, xb4;
    if (mrole) {
        if (SPARE_FIRST && spare) __builtin_amdgcn_s_setprio(MPRIO);
        const unsigned so = (8 * tb + 2 * w) * (SD * 2);
        xa0 = ld64<0>(lane * 8, rx, so); xa1 = ld64<512>(lane * 8, rx, so); xa4 = ld32<1024>(lane * 4, rx, so);
        xb0 = ld64<1280>(lane * 8, rx, so); xb1 = ld64<1280 + 512>(lane * 8, rx, so); xb4 = ld32<1280 + 1024>(lane * 4, rx, so);
        ld64clit<VLIT + 4>(opaque(OFF_W), cx.rws);
        b1v = ld16((tid & 127) * 2, mk_rsrc(p.b1, SF * 2), tj * 256);
        if (B2WARM) ld16lit<254>(lane * 4, mk_rsrc(p.b2, SD * 2), 160 * ct); else ld16lit<254>(opaque(OOB), rx, 0);
        if (!spare) { sfor<4>([&](auto ii) { px(ic<0>{}, ii); }); sfor<2>([&](auto u) { pw(u, ic<0>{}); pw(u, ic<1>{}); }); VMWAIT(11); } else { load_modes(); VMWAIT(7); }
        tie(xa0); tie(xa1); tie(xa4); tie(xb0); tie(xb1); tie(xb4);
        STAMP(1);
        CY5(0);
        const unsigned xa[5] = {xa0.x, xa0.y, xa1.x, xa1.y, xa4}, xb[5] = {xb0.x, xb0.y, xb1.x, xb1.y, xb4};
        const f2 st = layernorm_rows(xa, xb, cx,
            [&]() { if (!spare) { pw(ic<2>{}, ic<0>{}); pw(ic<2>{}, ic<1>{}); } },
            [&]() { if (!spare) { px(ic<1>{}, ic<0>{}); px(ic<1>{}, ic<1>{}); } });
        CY5(1);
        if (!spare) VMWAIT(13); else VMWAIT(0);
        take_epoch(cx); tie(b1v);
        BARRIER();
        CY5(2);
        const unsigned a = L_LNT + tid * 4, xso = (ABL & 32) ? WS_USED : OFF_XNB + tb * 10240;
        v4u v[3];
        sfor<3>([&](auto ii) { v[ii] = v4u{lds_ld<unsigned>(a + ii * 1024), lds_ld<unsigned>(a + ii * 1024 + 2560), lds_ld<unsigned>(a + ii * 1024 + 5120), lds_ld<unsigned>(a + ii * 1024 + 7680)}; });
        sfor<3>([&](auto ii) { tie(v[ii]); });
        if (!spare) { VMWAIT(6); BARRIER(); STAMP(2); px(ic<1>{}, ic<2>{}); px(ic<1>{}, ic<3>{}); }
        st128c<0>(v[0], tid * 16, cx.rws, xso);
        st128c<0>(v[1], tid * 16, cx.rws, xso + 4096);
        st128c<0>(v[2], tid * 16, cx.rws, sel(w < 2, xso + 8192, WS_USED));
        st64c(v2u{__float_as_uint(st.x), __float_as_uint(st.y)}, sel((lane & 31) == 31, (lane >> 5) * 8, OOB), cx.rws, OFF_STATS + (8 * tb + 2 * w) * 8);
    }

    if (mrole && !spare) {
        __builtin_amdgcn_s_setprio(MPRIO);
        cx.e_lo = rfl(cx.e_lo); cx.e_hi = rfl(cx.e_hi);
        unsigned ax[4], aw[4];
        {
            const unsigned row = 64 * wt + (lane & 31), hh = lane >> 5, blk = (lane >> 4) & 1, qr = (lane & 15) >> 2, pp = lane & 3;
            sfor<4>([&](auto ks) { ax[ks] = opaque(row * 128 + 16 * ((2 * ks + hh) ^ ((row >> 1) & 7))); });
            sfor<4>([&](auto i) {
                constexpr int mf = i >> 1, t = i & 1;
                const unsigned r = 8 * hh + 4 * t + qr, chn = 4 * (2 * wf + (mf ^ wt)) + 2 * blk + (pp >> 1);
                aw[i] = opaque(L_WH + 256 * r + 16 * (chn ^ ((qr << 2) | ((2 * hh + t) & 3))) + 8 * (pp & 1));
            });
        }
        const unsigned onesel = sel((lane ^ (lane >> 4)) & 1, 0u, 0x3f803f80u);
        v4u ones = {onesel, onesel, onesel, onesel};
        tie(ones);
        f16v acc[2][2];
        f4 csum;
        v2u fa[2][4], st0, st1;
        v4u plv;
        v4u fb[2][2];
        auto rd = [&](auto n_, auto i_) {
            constexpr int n = n_, i = i_, bf = n & 1;
            constexpr unsigned xo = ((n >> 2) % 3) * 16384, wo = ((n >> 1) & 3) * 8192 + (n & 1) * 4096;
            if constexpr (n < 40) {
                if constexpr (i == 0) fb[bf][0] = lds_ld<v4u>(ax[n & 3] + xo);
                else if constexpr (i == 3) fb[bf][1] = lds_ld<v4u>(ax[n & 3] + xo + 4096);
                else fa[bf][i < 3 ? i - 1 : i - 2] = lds_tr(aw[i < 3 ? i - 1 : i - 2] + wo);
            }
            PIN();
        };
        CYC(0);
        sfor<6>([&](auto i) { rd(ic<0>{}, i); });
        pw(ic<3>{}, ic<0>{}); pw(ic<3>{}, ic<1>{});
        sfor<40>([&](auto n0_) {
            constexpr int n = n0_ + 1, u = n >> 1, bf = (n - 1) & 1;
            constexpr bool first = n == 1;
            if constexpr ((n & 1) == 0 && n < 40) {
                VMWAIT(g1_wait(u));
                if constexpr (u == GCHK) { tie(plv); if (!flags_match(v2u{plv.x, plv.y}, cx)) poll<2>(gvo, cx); if (!flags_match(v2u{plv.z, plv.w}, cx)) poll<2>(gvo + 8, cx); }
                BARRIER();
                if constexpr (u <= 11) CYC(u);
                if constexpr (u == F1AT) publish(cx, OFF_F1 + tb * 16);
                if constexpr (u == GCHK - 3) plv = ld128c<0>(gvo, cx.rws, 0);
                if constexpr (u == 18) {
                    publish(cx, OFF_F4 + tb * FSTR);
                    const unsigned vo = (64 * wt + (lane & 31)) * 8;
                    st0 = ld64c<0>(vo, cx.rws, OFF_STATS + ti * 1024); st1 = ld64c<256>(vo, cx.rws, OFF_STATS + ti * 1024);
                }
                PIN();
            }
            auto vm = [&](auto m_) {
                constexpr int m = m_;
                if constexpr (g1_early(n) == 0) { if constexpr ((n & 1) == 0) pw(ic<u + 3>{}, m_); else px(ic<u / 2 + 2>{}, ic<2 * (u & 1) + m>{}); }
                else {
                    if constexpr (m == 0) { if constexpr ((n & 1) == 0) { pw(ic<u + 3>{}, ic<0>{}); pw(ic<u + 3>{}, ic<1>{}); } else { px(ic<u / 2 + 2>{}, ic<2 * (u & 1)>{}); px(ic<u / 2 + 2>{}, ic<2 * (u & 1) + 1>{}); } }
                    constexpr int f = 2 * (n - (40 - NEARLY / 2) - 1) + m;
                    w2_lit(ic<f>{}, ic<f + 1>{});
                }
                PIN();
            };
            const v4u a0 = {fa[bf][0].x, fa[bf][0].y, fa[bf][1].x, fa[bf][1].y}, a1 = {fa[bf][2].x, fa[bf][2].y, fa[bf][3].x, fa[bf][3].y};
            if constexpr (first) mfma32z(acc[0][0], a0, fb[bf][0]); else mfma32(acc[0][0], a0, fb[bf][0]);
            PIN(); rd(ic<n>{}, ic<0>{}); rd(ic<n>{}, ic<1>{}); rd(ic<n>{}, ic<2>{});
            if constexpr (first) mfma32z(acc[0][1], a0, fb[bf][1]); else mfma32(acc[0][1], a0, fb[bf][1]);
            PIN(); rd(ic<n>{}, ic<3>{}); rd(ic<n>{}, ic<4>{}); rd(ic<n>{}, ic<5>{});
            if constexpr (first) mfma16z(csum, a0, ones); else mfma16(csum, a0, ones);
            if constexpr (first) mfma32z(acc[1][0], a1, fb[bf][0]); else mfma32(acc[1][0], a1, fb[bf][0]);
            PIN(); vm(ic<0>{});
            if constexpr (first) mfma32z(acc[1][1], a1, fb[bf][1]); else mfma32(acc[1][1], a1, fb[bf][1]);
            PIN(); vm(ic<1>{});
        });
        STAMP(3);
        CY6(0);
        tie(csum);
        lds_st<f4>(L_CS + (64 * wf + 32 * wt + 16 * (lane & 1) + 4 * (lane >> 4)) * 4, csum);
        lds_st<float>(L_B1 + (tid & 127) * 4, blo(b1v));
        VMWAIT(g1_ops(36, 41) - 3);
        tie(st0); tie(st1);
        BARRIER();
        MFMA_PAD(9);
        CY6(1);
        const unsigned hh = lane >> 5;
        const unsigned hvo = sel((ABL & 16) || (tj == 13 && wf == 1), OOB, ((lane >> 4) & 1) * (NKS * 1024) + (hh * 16 + (lane & 15)) * 16);
        const unsigned hso = OFF_H + ((8 * ti + 4 * wt) * NKS + 4 * tj + 2 * wf) * 1024;
        const unsigned cam[2] = {opaque(L_CS + (64 * wf + 32 * wt + 4 * hh) * 4), opaque(L_CS + (64 * wf + 32 * (wt ^ 1) + 4 * hh) * 4)};
        constexpr float C1 = -2.302208198f, C2 = -0.1029432397f;
        sfor<2>([&](auto mf_) {
            constexpr int mf = mf_;
            f4 cs[4], bb[4];
            sfor<4>([&](auto ga) { cs[ga] = lds_ld<f4>(cam[mf] + 32 * ga); bb[ga] = lds_ld<f4>(cam[mf] + 512 + 32 * ga); });
            sfor<2>([&](auto nt_) {
                constexpr int nt = nt_;
                tie(acc[mf][nt]);
                const v2u sv = nt ? st1 : st0;
                const float sa = __uint_as_float(sv.x), sb = __uint_as_float(sv.y);
                sfor<2>([&](auto chk) {
                    f2 z[4], e[4];
                    sfor<4>([&](auto i) {
                        constexpr int ga = 2 * chk + (i >> 1), e2 = 2 * (i & 1);
                        z[i] = f2{acc[mf][nt][4 * ga + e2], acc[mf][nt][4 * ga + e2 + 1]} * sa + (f2{cs[ga][e2], cs[ga][e2 + 1]} * sb + f2{bb[ga][e2], bb[ga][e2 + 1]});
                    });
                    sfor<4>([&](auto i) { e[i] = z[i] * (z[i] * z[i] * C2 + C1); });
                    sfor<4>([&](auto i) { e[i] = f2{__builtin_amdgcn_exp2f(e[i].x), __builtin_amdgcn_exp2f(e[i].y)}; });
                    sfor<4>([&](auto i) { e[i] = e[i] + 1.f; });
                    sfor<4>([&](auto i) { e[i] = f2{__builtin_amdgcn_rcpf(e[i].x), __builtin_amdgcn_rcpf(e[i].y)}; });
                    unsigned o[4];
                    sfor<4>([&](auto i) { const f2 r = z[i] * e[i]; o[i] = pk(r.x, r.y); });
                    constexpr int idx = 4 * mf + 2 * nt + chk;
                    PIN();
                    w2_lit(ic<NEARLY + (NLIT - NEARLY) * idx / 8>{}, ic<NEARLY + (NLIT - NEARLY) * (idx + 1) / 8>{});
                    w2_lds(ic<5 * LEARLY * idx / 8>{}, ic<5 * LEARLY * (idx + 1) / 8>{});
                    if constexpr (idx == HINT_AT) publish(cx, OFF_F4 + 8 + tb * FSTR);
                    st128c<chk * 512>(v4u{o[0], o[1], o[2], o[3]}, hvo, cx.rws, hso + (2 * nt * NKS + (mf ^ wt)) * 1024);
                    PIN();
                    CY6(2 + idx);
                });
            });
        });
        STAMP(4);
        VMWAIT(0);
        CY6(10);
        BARRIER();
        CY6(11);
        publish(cx, OFF_F2 + (16 * ti + tj) * FSTR);
        STAMP(5);
    } else {
        const unsigned nround = sel(spare && !mrole, 2, 1);
        unsigned ch = ch0;
        auto half16 = [&](const u16* base, unsigned c) { const unsigned v = *(const CAS unsigned*)(base + c); return (v >> (16 * cidx)) & 0xffffu; };
        const unsigned last = sel(nround == 2, ch0 + 2, ch0);
        const unsigned dsk0 = half16(p.dsk, ch0), dsk1 = half16(p.dsk, last), ldt0 = half16(p.ldt, ch0), ldt1 = half16(p.ldt, last);
        if (W2S_AT == 2 && slice) VMWAIT(3); else VMWAIT(0);
        take_epoch(cx);
        Modes md = {lit<VLIT>(), lit<VLIT + 1>(), lit<VLIT + 2>(), lit<VLIT + 3>()};
        if (mrole) { BARRIER(); publish(cx, OFF_F1 + tb * 16); if (!SPARE_FIRST) __builtin_amdgcn_s_setprio(SPRIO); }
        cx.e_lo = rfl(cx.e_lo); cx.e_hi = rfl(cx.e_hi); ch = rfl(ch);
        const unsigned nxt = rfl(sel(nround == 2, ch * 256 + 512, SD * 256)), gso = rfl(OFF_G + g * FSTR), f5so = rfl(OFF_F1 + tb * 16 + 8);
        PIN(); asm volatile("s_nop 4"); PIN();
        auto retile = [&]() {
                const unsigned g4 = lane >> 4, a0 = opaque(L_W2S + (16 * (g4 >> 1) + 4 * (g4 & 1) + ((lane & 15) >> 2)) * 320 + 8 * (lane & 3) + w * 96);
                v4u fr[3];
                sfor<3>([&](auto ii) { const v2u lo = lds_tr(a0 + ii * 32), hi = lds_tr(a0 + ii * 32 + 8 * 320); fr[ii] = v4u{lo.x, lo.y, hi.x, hi.y}; });
                sfor<3>([&](auto ii) {
                    const unsigned fi = 3 * w + ii;
                    st128c<0>(fr[ii], w16a, cx.rws, sel(fi < 10 && !(ABL & 64), OFF_W2T + (2 * sqq * NKS + sks) * 5120 + fi * 1024 + sel(fi >= 5, (NKS - 1) * 5120, 0), WS_USED));
                });
            };
        auto gather = [&]() {
            __builtin_amdgcn_s_sleep(AGG_SLEEP);
            poll<4>(OFF_F1 + tid * 16, cx); BARRIER(); publish(cx, gso);
            poll<4>(OFF_F1 + 8 + tid * 16, cx); BARRIER(); publish(cx, gso + 8);
        };
        if (W2S_AT == 3 && slice) { BARRIER(); retile(); }
        STAMP(2);
#pragma unroll 1
        for (unsigned round = 0; round < nround; ++round) {
            const Modes cur = md;
            md.la = ld16(pvo, rla, nxt); md.ai = ld16(pvo, rai, nxt); md.cr = ld16(pvo, rcr, nxt); md.ci = ld16(pvo, rci, nxt);
            CYC(0);
            mode_powers(cx, cur, sel(round == 0, ldt0, ldt1));
            CYC(1);
            if (W2S_AT == 0 && slice) load_slice();
            if (W2S_AT >= 2) VMWAIT(0);
            BARRIER();
            if (W2S_AT == 2 && slice) retile();
            if (W2S_AT == 3 && round == 0 && !mrole) publish(cx, f5so);
            if (AGG_EARLY && !mrole && q == 27) gather();
            STAMP(3);
            CYC(2);
            kernel_table(cx, L_KT + (2 * round + cidx) * KT_CH);
            CYC(3);
            if (W2S_AT == 0 && slice) VMWAIT(3); else VMWAIT(0);
            tie(md.la); tie(md.ai); tie(md.cr); tie(md.ci);
            BARRIER();
            if (W2S_AT == 2 && round == 0 && !mrole) publish(cx, f5so);
            CYC(4);
        }
#pragma unroll 1
        for (unsigned round = 0; round < nround; ++round) {
            const float dskip = blo(sel(round == 0, dsk0, dsk1));
            cx.tid = opaque(cx.tid); cx.lane = cx.tid & 63;
            const unsigned tid = cx.tid;
            if (round == 0) CYC(5);
            expand_taps(cx, L_KT + (2 * round + cidx) * KT_CH);
            if (round == 0) CYC(6);
            STAMP(4);
            if (round == 0) {
                if (W2S_AT < 2 && slice) {
                    if (W2S_AT == 1) load_slice();
                    VMWAIT(0);
                    BARRIER();
                    retile();
                    VMWAIT(0);
                    BARRIER();
                }
                if (W2S_AT < 2 && !mrole) publish(cx, f5so);
                if (spare) {
                    poll<3>(OFF_F1 + tid * 16, cx);
                    BARRIER();
                } else {
                    if (!AGG_EARLY && q == 27) gather();
                    if (w == 0) poll<6>(opaque(OFF_F4 + tb * FSTR), cx);
                    BARRIER();
                }
                STAMP(5);
                const unsigned sh = nround, nb = 256 >> sh, c = tid & (2 * nround - 1), blk = tid >> sh;
                const unsigned uvo = blk * 10240 + c * 16, ua = opaque(L_U + 480 + c * U_CH + blk * 16), uso = OFF_XNB + ch * 16;
                v4u ub[4];
                sfor<4>([&](auto i) { ub[i] = ld128c<0>(uvo, cx.rws, sel(i < 2 * nround, uso + i * nb * 10240, WS_USED)); });
                lds_st<v2u>(L_U + w * U_CH + sel((tid & 63) < 60, tid & 63, 59) * 8, v2u{0, 0});
                VMWAIT(0);
                sfor<4>([&](auto i) { tie(ub[i]); });
                lds_st<v4u>(ua, ub[0]); lds_st<v4u>(ua + nb * 16, ub[1]);
                if (nround == 2) { lds_st<v4u>(ua + 2048, ub[2]); lds_st<v4u>(ua + 3072, ub[3]); }
            }
            if (mrole) __builtin_amdgcn_s_setprio(MPRIO);
            if (w == 0) { if (!spare) poll<3>(opaque(OFF_F4 + 8 + tb * FSTR), cx); else if (mrole) poll<3>(gvo + 8, cx); }
            BARRIER();
            STAMP(6);
            conv_pair(cx, ch + cidx, L_U + (2 * round + cidx) * U_CH, dskip, [&](auto k) { if (mrole) w2_lit(ic<NLIT * k / 8>{}, ic<NLIT * (k + 1) / 8>{}); PIN(); });
            ch += 2;
        }
        VMWAIT(0);
        BARRIER();
        publish(cx, OFF_F3 + (40 * g + sel(mrole, q + 4, q)) * FSTR);
        STAMP(8);
        if (!mrole) {
            if (b == 0 && w == 0) { poll<8>(gvo + 8, cx); publish(cx, OFF_W); }
            return;
        }
    }

    cx.e_lo = rfl(cx.e_lo); cx.e_hi = rfl(cx.e_hi);
    PIN(); asm volatile("s_nop 4"); PIN();
    v4u wb[5], hf[3][4];
    auto lds_frags = [&]() { if (spare) w2_lds(ic<0>{}, ic<5 * LEARLY>{}); w2_lds(ic<5 * LEARLY>{}, ic<20>{}); };
    auto ldw = [&](auto s_, auto c_) {
        constexpr int s = s_, c = c_;
        if constexpr (c == 0) { w2so = slot_off<s, 5120>(sw); if ((ABL & 1) && s > 0) w2so = WS_USED; }
        if constexpr (c < 4) wb[c] = ld128c<c * 1024>(w16a, cx.rws, w2so); else wb[c] = ld128c<0>(w16b, cx.rws, w2so);
        PIN();
    };
    unsigned hv[4], hso2;
    sfor<4>([&](auto t) { hv[t] = (4 * rt + ((t + w) & 3)) * (NKS * 1024) + lane * 16; });
    Slots sh;
    sh.thr = sw.thr; sh.a = OFF_H + k0s * 1024; sh.b = sh.a - nk * 1024; sh.last = sel(w < 2, OFF_H + klast * 1024, WS_USED);
    auto ldh = [&](auto s_, auto t_) {
        constexpr int s = s_, t = t_;
        if constexpr (s < 14) {
            if constexpr (t == 0) { hso2 = slot_off<s, 1024>(sh); if (ABL & 8) hso2 = WS_USED; }
            hf[s % 3][t] = ld128c<0>(hv[t], cx.rws, hso2);
        }
        PIN();
    };
    v2u xo[5], plv3;
    const unsigned j0 = (k0s - rot) >> 2, j1 = (k0s - rot + nk - 1) >> 2, f2vo = OFF_F2 + (16 * (rt >> 1) + sel(j0 + lane <= j1, j0 + lane, j0)) * FSTR;
    v2u plv2 = ld64c<0>(f2vo, cx.rws, 0);
    sfor<5>([&](auto c) { xo[c] = ld64<c * 32>(8 * (lane >> 4), mk_rsrc(p.b2, SD * 2), 160 * ct); });
    sfor<5>([&](auto c) { ldw(ic<0>{}, c); });
    VMWAIT(10);
    tie(plv2);
    const bool ready = FASTP && flags_match(plv2, cx);
    if (!ready) { lds_frags(); poll<1>(f2vo, cx); }
    STAMP(9);
    sfor<2>([&](auto s) { sfor<4>([&](auto t) { ldh(s, t); }); });
    if (ready) { lds_frags(); if (spare) VMWAIT(33); else VMWAIT(33 - 5 * LEARLY); } else VMWAIT(8);
    sfor<5>([&](auto c) { tie(xo[c]); });
    f4 acc2[5][4];
    sfor<5>([&](auto c) { acc2[c][0] = f4{blo(xo[c].x), bhi(xo[c].x), blo(xo[c].y), bhi(xo[c].y)}; tie(acc2[c][0]); });
    const unsigned xso = (64 * rt + 16 * w) * (SD * 2) + 160 * ct;
    unsigned xvo[3], rw[3];
    auto give = [&](auto c) {
        if constexpr (c == 0) sfor<3>([&](auto t1) { rw[t1] = opaque(L_RED + ((w + t1 + 1) & 3) * 7680 + t1 * 2560 + opaque(lane) * 8); });
        sfor<3>([&](auto t1) { tie(acc2[c][t1 + 1]); const f4 v = acc2[c][t1 + 1]; lds_st<v2u>(rw[t1] + c * 512, v2u{pk(v[0], v[1]), pk(v[2], v[3])}); });
        PIN();
    };
    {
        const unsigned f3vo = OFF_F3 + (40 * ct + 9 * w + sel(lane < 9, lane, 0)) * FSTR;
        const unsigned la = opaque(w * 20480 + lane * 16);
        v4u lf[2];
        sfor<14>([&](auto s_) {
            constexpr int s = s_;
            constexpr bool from_lds = (s & 1) && s < 9;
            if constexpr (s < 2) { if (!ready) VMWAIT(slot_wait(s)); else if (spare) VMWAIT(slot_wait(s) + (s == 0 ? 20 : 15)); else VMWAIT(slot_wait(s) + (s == 1 && LEARLY == 0 ? 15 : 20 - 5 * LEARLY)); } else VMWAIT(slot_wait(s));
            sfor<4>([&](auto t) { tie(hf[s % 3][t]); });
            if constexpr (s % 2 == 0 && s <= 10) sfor<5>([&](auto c) { tie(wb[c]); });
            if constexpr (s == 1) lf[0] = lds_ld<v4u>(la);
            if constexpr (s == 9) plv3 = ld64c<0>(f3vo, cx.rws, 0);
            if constexpr (s == 12) {
                tie(plv3);
                if (!flags_match(plv3, cx)) poll<1>(f3vo, cx);
                BARRIER();
                const unsigned so = (ABL & 4) ? WS_USED : OFF_ST + 80 * ct * 4096 + 128 * rt;
                sfor<3>([&](auto ii) {
                    const unsigned P = opaque(tid) + 256 * ii, row = (P * 7282) >> 16, pc = P - 9 * row;
                    set_m0(L_SIMG + ii * 4096 + w * 1024);
                    dmac<0>(sel(pc < 8 && row < 80, row * 4096 + pc * 16, OOB), cx.rws, so);
                });
                set_m0(L_XIMG + w * 3072);
                sfor<3>([&](auto i) {
                    const unsigned P = opaque(lane) + 64 * i, row = (P * 5958) >> 16, pc = P - 11 * row;
                    xvo[i] = sel(pc < 10 && row < 16, row * (SD * 2) + pc * 16, OOB);
                    dma<i * 1024>(xvo[i] - i * 1024, rx, xso);
                });
            }
            PIN();
            sfor<5>([&](auto c_) {
                constexpr int c = c_;
                if constexpr (from_lds && c < 4) { lf[(c + 1) & 1] = lds_ld<v4u>(la + (s >> 1) * 5120 + (c + 1) * 1024); PIN(); }
                sfor<4>([&](auto t) {
                    if constexpr (s == 0 && t > 0) mfma16z(acc2[c][t], wb[c], hf[0][t]);
                    else if constexpr (s % 2 == 0 && s <= 10) mfma16(acc2[c][t], wb[c], hf[s % 3][t]);
                    else if constexpr (from_lds) mfma16(acc2[c][t], lf[c & 1], hf[s % 3][t]);
                    else mfma16lit<VLIT + 4 * (5 * (s == 9 ? 0 : s - 10) + c)>(acc2[c][t], hf[s % 3][t]);
                });
                PIN();
                if constexpr (s == 13 && c >= 2) give(ic<c - 2>{});
                if constexpr (slot_reloads(s)) ldw(ic<s + 2>{}, c_);
                if constexpr (c < 4) ldh(ic<s + 2>{}, c_);
                if constexpr (s % 2 == 0 && s >= 2 && s < 8 && c == 3) { lf[0] = lds_ld<v4u>(la + (s >> 1) * 5120); PIN(); }
            });
        });
    }
    STAMP(10);
    CY5(5);
    const unsigned ln = opaque(lane), ra = opaque(L_RED + w * 7680 + ln * 8);
    const unsigned sa = opaque(L_SIMG + (4 * (ln >> 4) + ((ln & 15) >> 2)) * SIMG_ROW + 32 * w + 8 * (ln & 3));
    const unsigned oa = opaque(L_OUT + w * 3072 + (ln & 15) * OUT_ROW + 8 * (ln >> 4)), ob = opaque(L_OUT + w * 3072 + ln * 16);
    const unsigned hit = sel(((ln & 15) >> 2) == (ln >> 4), sel(ln & 1, 0x3f800000u, 0x3f80u), 0), p0 = sel(ln & 2, 0, hit), p1 = sel(ln & 2, hit, 0);
    v2u pick = {p0, p1};
    give(ic<3>{});
    tie(pick);
    MFMA_PAD(1);
    give(ic<4>{});
    CY5(6);
    VMWAIT(0);
    BARRIER();
    CY5(7);
    {
        auto wide = [](v2u v) { return f4{blo(v.x), bhi(v.x), blo(v.y), bhi(v.y)}; };
        v2u pa[5][3], sq[5], xq[5];
        sfor<3>([&](auto j) { sfor<5>([&](auto c) { pa[c][j] = lds_ld<v2u>(ra + j * 2560 + c * 512); }); });
        sfor<5>([&](auto c) { sq[c] = lds_tr(sa + c * 16 * SIMG_ROW); xq[c] = lds_ld<v2u>(oa + (L_XIMG - L_OUT) + c * 32); });
        PIN();
        sfor<3>([&](auto j) { sfor<5>([&](auto c) { mfma16k16(acc2[c][0], pick, pa[c][j]); }); });
        f4 sf[5], xf[5], d[5];
        sfor<5>([&](auto c) { sf[c] = wide(sq[c]); xf[c] = wide(xq[c]); tie(sf[c]); tie(xf[c]); });
        PIN();
        sfor<5>([&](auto c) { tie(acc2[c][0]); d[c] = acc2[c][0] * -1.44269504088896f; });
        sfor<5>([&](auto c) { d[c] = f4{__builtin_amdgcn_exp2f(d[c][0]), __builtin_amdgcn_exp2f(d[c][1]), __builtin_amdgcn_exp2f(d[c][2]), __builtin_amdgcn_exp2f(d[c][3])}; });
        sfor<5>([&](auto c) { d[c] = d[c] + 1.f; sf[c] = sf[c] * acc2[c][0]; });
        sfor<5>([&](auto c) { d[c] = f4{__builtin_amdgcn_rcpf(d[c][0]), __builtin_amdgcn_rcpf(d[c][1]), __builtin_amdgcn_rcpf(d[c][2]), __builtin_amdgcn_rcpf(d[c][3])}; });
        sfor<5>([&](auto c) { const f4 o = sf[c] * d[c] + xf[c]; lds_st<v2u>(oa + c * 32, v2u{pk(o[0], o[1]), pk(o[2], o[3])}); });
        CY5(8);
        PIN(); asm volatile("" ::: "memory"); PIN();
        const rsrc_t ro = mk_rsrc(p.out, SL * SD * 2);
        v4u ov[3];
        sfor<3>([&](auto i) { ov[i] = lds_ld<v4u>(ob + i * 1024); });
        PIN();
        sfor<3>([&](auto i) { st128(ov[i], xvo[i], ro, xso); });
    }
}

extern "C" void kernel_launch(void* const* d_in, const int* in_sizes, int n_in, void* d_out, int out_size, void* d_ws, size_t ws_size, hipStream_t stream) {
    Params p;
    p.x = (const u16*)d_in[0]; p.lar = (const u16*)d_in[1]; p.aim = (const u16*)d_in[2]; p.cr = (const u16*)d_in[3]; p.ci = (const u16*)d_in[4];
    p.ldt = (const u16*)d_in[5]; p.dsk = (const u16*)d_in[6]; p.w1 = (const u16*)d_in[8]; p.b1 = (const u16*)d_in[9];
    p.w2 = (const u16*)d_in[10]; p.b2 = (const u16*)d_in[11]; p.out = (u16*)d_out; p.ws = (char*)d_ws;
    (void)hipFuncSetAttribute((const void*)s4d_mixer_kernel, hipFuncAttributeMaxDynamicSharedMemorySize, LDS_BYTES);
    s4d_mixer_kernel<<<512, 256, LDS_BYTES, stream>>>(p);
}
```
